```python
import torch

def _softmax(x, axis=-1):
    return torch.softmax(x, dim=axis)
def _gelu(x):
    return torch.nn.functional.gelu(x, approximate="tanh")
def _silu(x):
    return torch.nn.functional.silu(x)
def _sigmoid(x):
    return torch.sigmoid(x)
def _layernorm(x, axis=-1, eps=1e-5):
    m = x.mean(dim=axis, keepdim=True)
    v = x.var(dim=axis, keepdim=True, unbiased=False)
    return (x - m) / torch.sqrt(v + eps)
def _scatter_add(tgt, idx, src, axis=0):
    return tgt.clone().index_add_(axis, idx, src)
def _segment_sum(src, seg, n):
    out = torch.zeros((n,) + src.shape[1:], dtype=src.dtype)
    return out.index_add_(0, seg, src)
def _pad(x, pw):
    flat = tuple(v for pair in reversed(pw) for v in pair)
    return torch.nn.functional.pad(x, flat)

kernel_name = 'standalone__paged_block_table_kv_pagedattention__sampling'

def setup_inputs():

    _w = (lambda a: (lambda t: t.to(torch.bfloat16) if t.is_floating_point() else t)(torch.as_tensor(a)))
    B, Hq, Hkv, Sq, Skv, D = 32, 32, 8, 4, 2048, 128
    bs = 16
    nbps = max(1, Skv // bs)
    num_phys = B * nbps + 4
    bt = torch.randperm(num_phys)[: B * nbps].reshape(B, nbps).to(torch.int64)
    cl = (nbps * bs - (torch.arange(1, B + 1, dtype=torch.int64) * 2654435761) % max(1, nbps * bs // 2)).to(torch.int64)
    return {
        "q": _w((lambda *s: torch.randn(*s, dtype=torch.bfloat16) * 0.8)(B, Hq, Sq, D)),
        "k_cache": _w((lambda *s: torch.randn(*s, dtype=torch.bfloat16) * 0.8)(num_phys, bs, Hkv, D)),
        "v_cache": _w((lambda *s: torch.randn(*s, dtype=torch.bfloat16) * 0.05)(num_phys, bs, Hkv, D)),
        "block_table": _w(bt),
        "context_lens": _w(cl),
    }

def reference(q, k_cache, v_cache, block_table, context_lens):
    B, Hq, Sq, D = q.shape
    bs = k_cache.shape[1]
    Hkv = k_cache.shape[2]
    nbps = block_table.shape[1]
    Skv = nbps * bs
    bt = block_table.reshape(-1)
    k = (lambda x, *d: x.permute(*d))(k_cache[bt].reshape(B, Skv, Hkv, D), 0, 2, 1, 3)
    v = (lambda x, *d: x.permute(*d))(v_cache[bt].reshape(B, Skv, Hkv, D), 0, 2, 1, 3)
    g = Hq // Hkv
    qg = q.reshape(B, Hkv, g, Sq, D)
    scores = torch.einsum("bhgqd,bhkd->bhgqk", qg, k) * (D ** -0.5)
    pos = torch.arange(Skv)
    msk = pos[None, :] < context_lens[:, None]
    scores = torch.where(msk[:, None, None, None, :], scores, -1e9)
    p = _softmax(scores, axis=-1)
    out = torch.einsum("bhgqk,bhkd->bhgqd", p, v)
    return out.reshape(B, Hq, Sq, D)
```

```cpp
#include <hip/hip_runtime.h>
#include <hip/hip_bf16.h>
#include <stdint.h>

#define AHEAD 2
#define NS 6
#define KPOL " nt"
#define VPOL ""
#define DELTA 4
#define TAU16 872
#define POFS 0

typedef __attribute__((ext_vector_type(8))) __bf16 bf16x8;
typedef __attribute__((ext_vector_type(4))) short s16x4;
typedef __attribute__((ext_vector_type(4))) float f32x4;
typedef __attribute__((ext_vector_type(4))) unsigned u32x4;
typedef __attribute__((ext_vector_type(2))) unsigned u32x2;
typedef __attribute__((address_space(1))) unsigned gu32;
#define LDSP(T, a) ((__attribute__((address_space(3))) T*)(uintptr_t)(a))

constexpr int NB = 32, NBPS = 128, SKV = NBPS * 16;
constexpr unsigned SLOT = 16384, EPOCH_OFF = 64 * 8 * SLOT;
constexpr int RS = 9216, PAIR_LDS = NS * RS + 256;

__device__ __forceinline__ unsigned cvtpk(float lo, float hi) {
  typedef __attribute__((ext_vector_type(2))) __bf16 bf16x2;
  bf16x2 r = {(__bf16)lo, (__bf16)hi};
  return __builtin_bit_cast(unsigned, r);
}
__device__ __forceinline__ float vmax(float a, float b) { return __builtin_elementwise_maximum(a, b); }
__device__ __forceinline__ float xg_max(float x) {
  auto s = __builtin_amdgcn_permlane16_swap(__float_as_uint(x), __float_as_uint(x), false, false);
  x = vmax(__uint_as_float(s[0]), __uint_as_float(s[1]));
  auto t = __builtin_amdgcn_permlane32_swap(__float_as_uint(x), __float_as_uint(x), false, false);
  return vmax(__uint_as_float(t[0]), __uint_as_float(t[1]));
}
__device__ __forceinline__ float xg_sum(float x) {
  auto s = __builtin_amdgcn_permlane16_swap(__float_as_uint(x), __float_as_uint(x), false, false);
  x = __uint_as_float(s[0]) + __uint_as_float(s[1]);
  auto t = __builtin_amdgcn_permlane32_swap(__float_as_uint(x), __float_as_uint(x), false, false);
  return __uint_as_float(t[0]) + __uint_as_float(t[1]);
}
__device__ __forceinline__ __amdgpu_buffer_rsrc_t rsrc(const void* p, unsigned n) {
  return __builtin_amdgcn_make_buffer_rsrc((void*)p, 0, n, 0x00020000);
}
__device__ __forceinline__ u32x4 srd(const void* p, unsigned n) {
  const uint64_t a = (uint64_t)p;
  return u32x4{(unsigned)a, (unsigned)(a >> 32) & 0xffffu, n, 0x00020000u};
}
__device__ __forceinline__ int rl(int v, int l) { return __builtin_amdgcn_readlane(v, l); }
__device__ __forceinline__ int lds_get(unsigned a) {
  return __builtin_amdgcn_readfirstlane(__hip_atomic_load(LDSP(int, a), __ATOMIC_RELAXED, __HIP_MEMORY_SCOPE_WORKGROUP));
}
__device__ __forceinline__ void lds_put(unsigned a, int v) {
  __hip_atomic_store(LDSP(int, a), v, __ATOMIC_RELAXED, __HIP_MEMORY_SCOPE_WORKGROUP);
}

#define DMA4(POL, dst, rs, so, v0, v1, v2, v3)                                                        \
  do {                                                                                                \
    unsigned keep_;                                                                                   \
    asm volatile("s_mov_b32 %0, m0\n\ts_mov_b32 m0, %1\n\ts_nop 4\n\t"                                \
                 "buffer_load_dwordx4 %4, %2, %3 offen" POL " lds\n\t"                                \
                 "buffer_load_dwordx4 %5, %2, %3 offen offset:1024" POL " lds\n\t"                    \
                 "buffer_load_dwordx4 %6, %2, %3 offen offset:2048" POL " lds\n\t"                    \
                 "buffer_load_dwordx4 %7, %2, %3 offen offset:3072" POL " lds\n\t"                    \
                 "s_mov_b32 m0, %0"                                                                   \
                 : "=&s"(keep_) : "s"(dst), "s"(rs), "s"(so), "v"(v0), "v"(v1), "v"(v2), "v"(v3) : "memory"); \
  } while (0)
#define DMA1(POL, dst, rs, so, v0)                                                                    \
  do {                                                                                                \
    unsigned keep_;                                                                                   \
    asm volatile("s_mov_b32 %0, m0\n\ts_mov_b32 m0, %1\n\ts_nop 4\n\t"                                \
                 "buffer_load_dwordx4 %4, %2, %3 offen" POL " lds\n\t"                                \
                 "s_mov_b32 m0, %0"                                                                   \
                 : "=&s"(keep_) : "s"(dst), "s"(rs), "s"(so), "v"(v0) : "memory");                    \
  } while (0)
#define WAITVM(n) asm volatile("s_waitcnt vmcnt(%0)" :: "n"(n) : "memory")
#define NOW() ((unsigned)__builtin_amdgcn_s_memrealtime())

__global__ void __launch_bounds__(512)
pa_kernel(const char* __restrict__ q, const char* __restrict__ kc, const char* __restrict__ vc,
          const int* __restrict__ bt, const int* __restrict__ lens, char* __restrict__ out, char* __restrict__ ws) {
  __shared__ __attribute__((aligned(256))) char lds[2 * PAIR_LDS];
  const unsigned t0 = NOW();
  const int lane = threadIdx.x & 63;
  const int wave = __builtin_amdgcn_readfirstlane(threadIdx.x >> 6);
  const int p = blockIdx.x >> 2;
  const int h = 2 * ((blockIdx.x - (p >> 1)) & 3) + (wave & 1);
  const int g = lane >> 4, c = lane & 15;
  const bool loader = wave < 2;

  int len = lens[lane & 31];
  gu32* const ep = (gu32*)(ws + EPOCH_OFF) + h * 64 + ((p + lane) & 63);
  unsigned tagv;
  if (loader) tagv = bt[(wave * 64 + lane) * 32];
  else tagv = __hip_atomic_load(ep, __ATOMIC_RELAXED, __HIP_MEMORY_SCOPE_AGENT);
  const unsigned ring = (unsigned)(uintptr_t)LDSP(char, lds) + (wave & 1) * PAIR_LDS;
  const unsigned fullw = ring + NS * RS, freew = fullw + 128;
  lds_put(loader ? fullw : freew, 0);
  asm volatile("s_waitcnt lgkmcnt(0)\n\ts_barrier" ::: "memory");

  const bool uni = len <= 0;
  if (uni || len > SKV) len = SKV;
  const int np = (len + 15) >> 4;
  const int dl = np >= 16 ? DELTA : 0;
  const int vl = np - dl;
  int incl = vl;
  incl += __builtin_amdgcn_update_dpp(0, incl, 0x111, 0xf, 0xf, false);
  incl += __builtin_amdgcn_update_dpp(0, incl, 0x112, 0xf, 0xf, false);
  incl += __builtin_amdgcn_update_dpp(0, incl, 0x114, 0xf, 0xf, false);
  incl += __builtin_amdgcn_update_dpp(0, incl, 0x118, 0xf, 0xf, false);
  incl += __builtin_amdgcn_update_dpp(0, incl, 0x142, 0xa, 0xf, false);
  const int V = rl(incl, 31), VV = max(V, 64);
  const int lo = (VV * p) >> 6, hi = min(V, (VV * (p + 1)) >> 6);
  if (lo >= hi) return;
  const int b0 = __builtin_popcount((unsigned)__builtin_amdgcn_ballot_w64(incl <= lo));
  const int b1 = __builtin_popcount((unsigned)__builtin_amdgcn_ballot_w64(incl < hi));
  const int ex = incl - vl;
  const int r0v = lo > ex ? lo - ex + dl : 0, cntv = min(hi, incl) - ex + dl - r0v;
  const int nv = rl(r0v, b1) == 0 ? __builtin_popcountll(__builtin_amdgcn_ballot_w64(((VV * lane) >> 6) < rl(incl, b1))) - 1 - p : 0;
  const unsigned loff = lane * 16;
  auto load_partial = [&](unsigned dst, int j) __attribute__((always_inline)) {
    const u32x4 pr = srd(ws + (unsigned)((p + j) * 8 + h) * SLOT, SLOT);
    DMA4(" sc1", dst, pr, 0, loff, loff, loff, loff);
    DMA4(" sc1", dst + 4096, pr, 4096, loff, loff, loff, loff);
    DMA1(" sc1", dst + 8192, pr, 8192, loff);
  };

  if (loader) {
    int n = 0;
    unsigned tab[2] = {0u, 0u};
    int idx[2] = {0, 0};
    for (int s = b0; s <= b1; ++s) {
      const int r0 = rl(r0v, s), cnt = rl(cntv, s), ls = rl(len, s);
#pragma unroll
      for (int u = 0; u < 2; ++u) {
        const int k = lane + 64 * u - n, pg = r0 + k;
        idx[u] = k >= 0 ? s * NBPS + pg : idx[u];
        tab[u] = k >= 0 ? (unsigned)(min(16, ls - 16 * pg) - 1) << 17 : tab[u];
      }
      n += cnt;
    }
    tab[0] |= (unsigned)bt[min(idx[0], NB * NBPS - 1)];
    tab[1] |= (unsigned)bt[min(idx[1], NB * NBPS - 1)];
    unsigned go[4];
#pragma unroll
    for (int r = 0; r < 4; ++r) go[r] = (4 * r + g) * 2048 + ((c ^ (4 * r + g)) << 4) - 1024 * r;
    asm volatile("" : "+v"(tab[0]), "+v"(tab[1]) : "v"(tagv));
    const int ntot = n + nv;
    unsigned dst = ring;
    int freed = 0;
    for (int j = 0; j < ntot; ++j) {
      while (freed + NS <= j) { freed = lds_get(freew); if (freed + NS <= j) __builtin_amdgcn_s_sleep(1); }
      if (TAU16) {
        const int due = ((j * TAU16) >> 4) + POFS;
        while ((int)(NOW() - t0) < due) __builtin_amdgcn_s_sleep(1);
      }
      if (j < n) {
        const unsigned e = rl(j < 64 ? tab[0] : tab[1], j & 63);
        const size_t po = (size_t)(e & 0x1ffffu) * 32768 + h * 256;
        const unsigned nrec = ((e >> 17) & 15) * 2048 + 256;
        const u32x4 kr = srd(kc + po, nrec), vr = srd(vc + po, nrec);
        DMA4(KPOL, dst, kr, 0, go[0], go[1], go[2], go[3]);
        DMA4(VPOL, dst + 4096, vr, 0, go[0], go[1], go[2], go[3]);
      } else {
        load_partial(dst, j - n + 1);
      }
      dst = dst + RS == ring + NS * RS ? ring : dst + RS;
      if (j >= AHEAD) { WAITVM(8 * AHEAD); lds_put(fullw, j - AHEAD + 1); }
    }
    if (AHEAD > 3) { WAITVM(24); lds_put(fullw, ntot - 3); }
    if (AHEAD > 2) { WAITVM(16); lds_put(fullw, ntot - 2); }
    if (AHEAD > 1) { WAITVM(8); lds_put(fullw, ntot - 1); }
    WAITVM(0); lds_put(fullw, ntot);
    return;
  }

  const unsigned unimask = (unsigned)__builtin_amdgcn_ballot_w64(uni);
  const unsigned qoff = c * 256 + g * 16;
  const __amdgpu_buffer_rsrc_t qr = rsrc(q + h * 4096, NB * 32768);
  u32x4 qn[4];
#pragma unroll
  for (int s = 0; s < 4; ++s) qn[s] = __builtin_amdgcn_raw_buffer_load_b128(qr, qoff + 64 * s, b0 * 32768, 0);
  tagv += 0x9E3779B9u;
  const float CS = 0.088388347648318440f * 1.4426950408889634f;
  const unsigned ka = c * 256 + ((g ^ c) << 4);
  const unsigned va = 4096 + (lane >> 2) * 256 + ((((lane & 3) << 2) ^ (lane >> 2)) << 4);
  unsigned cur = ring;
  int i = 0, full = 0;
  for (int b = b0; b <= b1; ++b) {
    const int r0 = rl(r0v, b), cnt = rl(cntv, b);
    const int iend = i + cnt;
    const int lastt = min(15, rl(len, b) - 16 * (r0 + cnt - 1) - 1);
    const float cs = (unimask >> b) & 1 ? 0.f : CS;
    u32x4 qf[4];
#pragma unroll
    for (int s = 0; s < 4; ++s) qf[s] = qn[s];
    asm volatile("" : "+v"(qf[0]), "+v"(qf[1]), "+v"(qf[2]), "+v"(qf[3]));
    if (b < b1) {
#pragma unroll
      for (int s = 0; s < 4; ++s) qn[s] = __builtin_amdgcn_raw_buffer_load_b128(qr, qoff + 64 * s, (b + 1) * 32768, 0);
    }
    f32x4 acc[8];
#pragma unroll
    for (int k = 0; k < 8; ++k) acc[k] = f32x4{0.f, 0.f, 0.f, 0.f};
    float m = -1e30f, l = 0.f;
    do {
      while (full <= i) { full = lds_get(fullw); if (full <= i) __builtin_amdgcn_s_sleep(1); }
      asm volatile("" ::: "memory");
      u32x4 kf[4];
      u32x2 vt[8];
#pragma unroll
      for (int s = 0; s < 4; ++s) kf[s] = *LDSP(const u32x4, (cur + ka) ^ (s << 6));
#pragma unroll
      for (int k = 0; k < 8; ++k)
        vt[k] = __builtin_bit_cast(u32x2, __builtin_amdgcn_ds_read_tr16_b64_v4i16(LDSP(s16x4, ((cur + va) ^ ((k >> 1) << 4)) + 8 * (k & 1))));
      f32x4 sc = {0.f, 0.f, 0.f, 0.f};
#pragma unroll
      for (int s = 0; s < 4; ++s)
        sc = __builtin_amdgcn_mfma_f32_16x16x32_bf16(__builtin_bit_cast(bf16x8, kf[s]), __builtin_bit_cast(bf16x8, qf[s]), sc, 0, 0, 0);
      ++i;
      asm volatile("" : "+v"(vt[0]), "+v"(vt[1]), "+v"(vt[2]), "+v"(vt[3]), "+v"(vt[4]), "+v"(vt[5]), "+v"(vt[6]), "+v"(vt[7]) :: "memory");
      lds_put(freew, i);
      cur = cur + RS == ring + NS * RS ? ring : cur + RS;
      if (i == iend && lastt < 15) {
        u32x2 vm = {(4 * g <= lastt ? 0xffffu : 0u) | (4 * g + 1 <= lastt ? 0xffff0000u : 0u),
                    (4 * g + 2 <= lastt ? 0xffffu : 0u) | (4 * g + 3 <= lastt ? 0xffff0000u : 0u)};
        asm volatile("" : "+v"(vm));
#pragma unroll
        for (int k = 0; k < 8; ++k) vt[k] &= vm;
#pragma unroll
        for (int r = 0; r < 4; ++r) sc[r] = 4 * g + r <= lastt ? sc[r] : -__builtin_inff();
      }
      const float mx = xg_max(vmax(vmax(sc[0], sc[1]), vmax(sc[2], sc[3])));
      const float mn = vmax(m, mx * cs);
      const float alpha = __builtin_amdgcn_exp2f(m - mn);
      m = mn;
      float pr[4];
#pragma unroll
      for (int r = 0; r < 4; ++r) pr[r] = __builtin_amdgcn_exp2f(__builtin_fmaf(sc[r], cs, -mn));
      l = l * alpha + ((pr[0] + pr[1]) + (pr[2] + pr[3]));
      const u32x2 pf = {cvtpk(pr[0], pr[1]), cvtpk(pr[2], pr[3])};
#pragma unroll
      for (int k = 0; k < 8; ++k) {
        acc[k] *= alpha;
        acc[k] = __builtin_amdgcn_mfma_f32_16x16x16bf16_1k(__builtin_bit_cast(s16x4, vt[k]), __builtin_bit_cast(s16x4, pf), acc[k], 0, 0, 0);
      }
    } while (i < iend);
    if (b == b1) {
      for (int j = 1; j <= nv; ++j) {
        while (full <= i) { full = lds_get(fullw); if (full <= i) __builtin_amdgcn_s_sleep(1); }
        asm volatile("" ::: "memory");
        const unsigned tag = rl(tagv, j);
        u32x4 x[8];
        u32x2 y;
        for (int spin = 0;; ++spin) {
#pragma unroll
          for (int k = 0; k < 8; ++k) x[k] = *LDSP(const u32x4, cur + 1024 * k + loff);
          y = *LDSP(const u32x2, cur + 8192 + 8 * c);
          unsigned bad = y[1] ^ tag;
#pragma unroll
          for (int k = 0; k < 8; ++k) bad |= (x[k][1] ^ tag) | (x[k][3] ^ tag);
          if (__builtin_amdgcn_ballot_w64(bad != 0) == 0 || spin > (1 << 22)) break;
          __builtin_amdgcn_s_sleep(2);
          load_partial(cur, j);
          WAITVM(0);
        }
        ++i;
        asm volatile("" ::: "memory");
        lds_put(freew, i);
        cur = cur + RS == ring + NS * RS ? ring : cur + RS;
        const float lse = __uint_as_float(y[0]);
        const float mn = vmax(m, lse);
        const float alpha = __builtin_amdgcn_exp2f(m - mn), w = __builtin_amdgcn_exp2f(lse - mn);
        m = mn;
        l = l * alpha + (g == 0 ? w : 0.f);
#pragma unroll
        for (int k = 0; k < 8; ++k) {
          const f32x4 o = {__uint_as_float(x[k][0] << 16), __uint_as_float(x[k][0] & 0xffff0000u),
                           __uint_as_float(x[k][2] << 16), __uint_as_float(x[k][2] & 0xffff0000u)};
          acc[k] = acc[k] * alpha + o * w;
        }
      }
    }
    const float lt = xg_sum(l);
    const float inv = __builtin_amdgcn_rcpf(lt);
    u32x2 ob[8];
#pragma unroll
    for (int k = 0; k < 8; ++k) ob[k] = u32x2{cvtpk(acc[k][0] * inv, acc[k][1] * inv), cvtpk(acc[k][2] * inv, acc[k][3] * inv)};
    if (r0 > 0) {
      const unsigned tag = rl(tagv, 0);
      const __amdgpu_buffer_rsrc_t pr = rsrc(ws + (unsigned)(p * 8 + h) * SLOT, SLOT);
#pragma unroll
      for (int k = 0; k < 8; ++k)
        __builtin_amdgcn_raw_buffer_store_b128(u32x4{ob[k][0], tag, ob[k][1], tag}, pr, loff + 1024 * (k & 3), (k >> 2) * 4096, 16);
      const u32x2 y = {__float_as_uint(m + __builtin_amdgcn_logf(lt)), tag};
      __builtin_amdgcn_raw_buffer_store_b64(y, pr, g == 0 ? c * 8 : 0x80000000u, 8192, 16);
    } else {
      const __amdgpu_buffer_rsrc_t orr = rsrc(out + (b * 8 + h) * 4096, 4096);
#pragma unroll
      for (int r = 0; r < 4; ++r)
        __builtin_amdgcn_raw_buffer_store_b128(u32x4{ob[2 * r][0], ob[2 * r][1], ob[2 * r + 1][0], ob[2 * r + 1][1]}, orr, c * 256 + g * 64 + 16 * r, 0, 0);
    }
  }
  if (lane >= 1 && lane <= nv) __hip_atomic_store(ep, tagv, __ATOMIC_RELAXED, __HIP_MEMORY_SCOPE_AGENT);
}

extern "C" void kernel_launch(void* const* d_in, const int* in_sizes, int n_in, void* d_out, int out_size,
                              void* d_ws, size_t ws_size, hipStream_t stream) {
  pa_kernel<<<256, 256, 0, stream>>>((const char*)d_in[0], (const char*)d_in[1], (const char*)d_in[2],
                                     (const int*)d_in[3], (const int*)d_in[4], (char*)d_out, (char*)d_ws);
}
```

```cpp
#define TS
#define STAT 0
#include <hip/hip_runtime.h>
#include <hip/hip_bf16.h>
#include <stdint.h>

#define AHEAD 2
#define NS 6
#define KPOL " nt"
#define VPOL ""
#define DELTA 4
#define TAU16 872
#define POFS 0

typedef __attribute__((ext_vector_type(8))) __bf16 bf16x8;
typedef __attribute__((ext_vector_type(4))) short s16x4;
typedef __attribute__((ext_vector_type(4))) float f32x4;
typedef __attribute__((ext_vector_type(4))) unsigned u32x4;
typedef __attribute__((ext_vector_type(2))) unsigned u32x2;
typedef __attribute__((address_space(1))) unsigned gu32;
#define LDSP(T, a) ((__attribute__((address_space(3))) T*)(uintptr_t)(a))

constexpr int NB = 32, NBPS = 128, SKV = NBPS * 16;
constexpr unsigned SLOT = 16384, EPOCH_OFF = 64 * 8 * SLOT;
constexpr int RS = 9216, PAIR_LDS = NS * RS + 256;

__device__ __forceinline__ unsigned cvtpk(float lo, float hi) {
  typedef __attribute__((ext_vector_type(2))) __bf16 bf16x2;
  bf16x2 r = {(__bf16)lo, (__bf16)hi};
  return __builtin_bit_cast(unsigned, r);
}
__device__ __forceinline__ float vmax(float a, float b) { return __builtin_elementwise_maximum(a, b); }
__device__ __forceinline__ float xg_max(float x) {
  auto s = __builtin_amdgcn_permlane16_swap(__float_as_uint(x), __float_as_uint(x), false, false);
  x = vmax(__uint_as_float(s[0]), __uint_as_float(s[1]));
  auto t = __builtin_amdgcn_permlane32_swap(__float_as_uint(x), __float_as_uint(x), false, false);
  return vmax(__uint_as_float(t[0]), __uint_as_float(t[1]));
}
__device__ __forceinline__ float xg_sum(float x) {
  auto s = __builtin_amdgcn_permlane16_swap(__float_as_uint(x), __float_as_uint(x), false, false);
  x = __uint_as_float(s[0]) + __uint_as_float(s[1]);
  auto t = __builtin_amdgcn_permlane32_swap(__float_as_uint(x), __float_as_uint(x), false, false);
  return __uint_as_float(t[0]) + __uint_as_float(t[1]);
}
__device__ __forceinline__ __amdgpu_buffer_rsrc_t rsrc(const void* p, unsigned n) {
  return __builtin_amdgcn_make_buffer_rsrc((void*)p, 0, n, 0x00020000);
}
__device__ __forceinline__ u32x4 srd(const void* p, unsigned n) {
  const uint64_t a = (uint64_t)p;
  return u32x4{(unsigned)a, (unsigned)(a >> 32) & 0xffffu, n, 0x00020000u};
}
__device__ __forceinline__ int rl(int v, int l) { return __builtin_amdgcn_readlane(v, l); }
__device__ __forceinline__ int lds_get(unsigned a) {
  return __builtin_amdgcn_readfirstlane(__hip_atomic_load(LDSP(int, a), __ATOMIC_RELAXED, __HIP_MEMORY_SCOPE_WORKGROUP));
}
__device__ __forceinline__ void lds_put(unsigned a, int v) {
  __hip_atomic_store(LDSP(int, a), v, __ATOMIC_RELAXED, __HIP_MEMORY_SCOPE_WORKGROUP);
}

#define DMA4(POL, dst, rs, so, v0, v1, v2, v3)                                                        \
  do {                                                                                                \
    unsigned keep_;                                                                                   \
    asm volatile("s_mov_b32 %0, m0\n\ts_mov_b32 m0, %1\n\ts_nop 4\n\t"                                \
                 "buffer_load_dwordx4 %4, %2, %3 offen" POL " lds\n\t"                                \
                 "buffer_load_dwordx4 %5, %2, %3 offen offset:1024" POL " lds\n\t"                    \
                 "buffer_load_dwordx4 %6, %2, %3 offen offset:2048" POL " lds\n\t"                    \
                 "buffer_load_dwordx4 %7, %2, %3 offen offset:3072" POL " lds\n\t"                    \
                 "s_mov_b32 m0, %0"                                                                   \
                 : "=&s"(keep_) : "s"(dst), "s"(rs), "s"(so), "v"(v0), "v"(v1), "v"(v2), "v"(v3) : "memory"); \
  } while (0)
#define DMA1(POL, dst, rs, so, v0)                                                                    \
  do {                                                                                                \
    unsigned keep_;                                                                                   \
    asm volatile("s_mov_b32 %0, m0\n\ts_mov_b32 m0, %1\n\ts_nop 4\n\t"                                \
                 "buffer_load_dwordx4 %4, %2, %3 offen" POL " lds\n\t"                                \
                 "s_mov_b32 m0, %0"                                                                   \
                 : "=&s"(keep_) : "s"(dst), "s"(rs), "s"(so), "v"(v0) : "memory");                    \
  } while (0)
#define WAITVM(n) asm volatile("s_waitcnt vmcnt(%0)" :: "n"(n) : "memory")
#define NOW() ((unsigned)__builtin_amdgcn_s_memrealtime())

__global__ void __launch_bounds__(512)
pa_kernel(const char* __restrict__ q, const char* __restrict__ kc, const char* __restrict__ vc,
          const int* __restrict__ bt, const int* __restrict__ lens, char* __restrict__ out, char* __restrict__ ws) {
  __shared__ __attribute__((aligned(256))) char lds[2 * PAIR_LDS];
  const unsigned t0 = NOW();
  const int lane = threadIdx.x & 63;
  const int wave = __builtin_amdgcn_readfirstlane(threadIdx.x >> 6);
  const int p = blockIdx.x >> 2;
  const int h = 2 * ((blockIdx.x - (p >> 1)) & 3) + (wave & 1);
  const int g = lane >> 4, c = lane & 15;
  const bool loader = wave < 2;

  int len = lens[lane & 31];
  gu32* const ep = (gu32*)(ws + EPOCH_OFF) + h * 64 + ((p + lane) & 63);
  unsigned tagv;
  if (loader) tagv = bt[(wave * 64 + lane) * 32];
  else tagv = __hip_atomic_load(ep, __ATOMIC_RELAXED, __HIP_MEMORY_SCOPE_AGENT);
  const unsigned ring = (unsigned)(uintptr_t)LDSP(char, lds) + (wave & 1) * PAIR_LDS;
  const unsigned fullw = ring + NS * RS, freew = fullw + 128;
  lds_put(loader ? fullw : freew, 0);
  asm volatile("s_waitcnt lgkmcnt(0)\n\ts_barrier" ::: "memory");

  const bool uni = len <= 0;
  if (uni || len > SKV) len = SKV;
  const int np = (len + 15) >> 4;
  const int dl = np >= 16 ? DELTA : 0;
  const int vl = np - dl;
  int incl = vl;
  incl += __builtin_amdgcn_update_dpp(0, incl, 0x111, 0xf, 0xf, false);
  incl += __builtin_amdgcn_update_dpp(0, incl, 0x112, 0xf, 0xf, false);
  incl += __builtin_amdgcn_update_dpp(0, incl, 0x114, 0xf, 0xf, false);
  incl += __builtin_amdgcn_update_dpp(0, incl, 0x118, 0xf, 0xf, false);
  incl += __builtin_amdgcn_update_dpp(0, incl, 0x142, 0xa, 0xf, false);
  const int V = rl(incl, 31), VV = max(V, 64);
  const int lo = (VV * p) >> 6, hi = min(V, (VV * (p + 1)) >> 6);
  if (lo >= hi) return;
  const int b0 = __builtin_popcount((unsigned)__builtin_amdgcn_ballot_w64(incl <= lo));
  const int b1 = __builtin_popcount((unsigned)__builtin_amdgcn_ballot_w64(incl < hi));
  const int ex = incl - vl;
  const int r0v = lo > ex ? lo - ex + dl : 0, cntv = min(hi, incl) - ex + dl - r0v;
  const int nv = rl(r0v, b1) == 0 ? __builtin_popcountll(__builtin_amdgcn_ballot_w64(((VV * lane) >> 6) < rl(incl, b1))) - 1 - p : 0;
  const unsigned loff = lane * 16;
  auto load_partial = [&](unsigned dst, int j) __attribute__((always_inline)) {
    const u32x4 pr = srd(ws + (unsigned)((p + j) * 8 + h) * SLOT, SLOT);
    DMA4(" sc1", dst, pr, 0, loff, loff, loff, loff);
    DMA4(" sc1", dst + 4096, pr, 4096, loff, loff, loff, loff);
    DMA1(" sc1", dst + 8192, pr, 8192, loff);
  };
  unsigned* const tsp = (unsigned*)(ws + (64u << 20)) + (blockIdx.x * 2 + (wave & 1)) * 16;

  if (loader) {
    int n = 0;
    unsigned tab[2] = {0u, 0u};
    int idx[2] = {0, 0};
    for (int s = b0; s <= b1; ++s) {
      const int r0 = rl(r0v, s), cnt = rl(cntv, s), ls = rl(len, s);
#pragma unroll
      for (int u = 0; u < 2; ++u) {
        const int k = lane + 64 * u - n, pg = r0 + k;
        idx[u] = k >= 0 ? s * NBPS + pg : idx[u];
        tab[u] = k >= 0 ? (unsigned)(min(16, ls - 16 * pg) - 1) << 17 : tab[u];
      }
      n += cnt;
    }
    tab[0] |= (unsigned)bt[min(idx[0], NB * NBPS - 1)];
    tab[1] |= (unsigned)bt[min(idx[1], NB * NBPS - 1)];
    unsigned go[4];
#pragma unroll
    for (int r = 0; r < 4; ++r) go[r] = (4 * r + g) * 2048 + ((c ^ (4 * r + g)) << 4) - 1024 * r;
    asm volatile("" : "+v"(tab[0]), "+v"(tab[1]) : "v"(tagv));
    const int ntot = n + nv;
    unsigned dst = ring;
    int freed = 0;
    if (lane == 0) { tsp[14] = NOW(); tsp[4] = n; }
    for (int j = 0; j < ntot; ++j) {
      while (freed + NS <= j) { freed = lds_get(freew); if (freed + NS <= j) __builtin_amdgcn_s_sleep(1); }
      if (TAU16) {
        const int due = ((j * TAU16) >> 4) + POFS;
        while ((int)(NOW() - t0) < due) __builtin_amdgcn_s_sleep(1);
      }
      if (j < n) {
        const unsigned e = rl(j < 64 ? tab[0] : tab[1], j & 63);
        const size_t po = (size_t)(e & 0x1ffffu) * 32768 + h * 256;
        const unsigned nrec = ((e >> 17) & 15) * 2048 + 256;
        const u32x4 kr = srd(kc + po, nrec), vr = srd(vc + po, nrec);
        DMA4(KPOL, dst, kr, 0, go[0], go[1], go[2], go[3]);
        DMA4(VPOL, dst + 4096, vr, 0, go[0], go[1], go[2], go[3]);
      } else {
        load_partial(dst, j - n + 1);
      }
      dst = dst + RS == ring + NS * RS ? ring : dst + RS;
      if (j >= AHEAD) { WAITVM(8 * AHEAD); lds_put(fullw, j - AHEAD + 1); }
    }
    if (lane == 0) tsp[15] = NOW();
    if (AHEAD > 3) { WAITVM(24); lds_put(fullw, ntot - 3); }
    if (AHEAD > 2) { WAITVM(16); lds_put(fullw, ntot - 2); }
    if (AHEAD > 1) { WAITVM(8); lds_put(fullw, ntot - 1); }
    WAITVM(0); lds_put(fullw, ntot);
    return;
  }

  const unsigned unimask = (unsigned)__builtin_amdgcn_ballot_w64(uni);
  const unsigned qoff = c * 256 + g * 16;
  const __amdgpu_buffer_rsrc_t qr = rsrc(q + h * 4096, NB * 32768);
  u32x4 qn[4];
#pragma unroll
  for (int s = 0; s < 4; ++s) qn[s] = __builtin_amdgcn_raw_buffer_load_b128(qr, qoff + 64 * s, b0 * 32768, 0);
  tagv += 0x9E3779B9u;
  const float CS = 0.088388347648318440f * 1.4426950408889634f;
  const unsigned ka = c * 256 + ((g ^ c) << 4);
  const unsigned va = 4096 + (lane >> 2) * 256 + ((((lane & 3) << 2) ^ (lane >> 2)) << 4);
  unsigned cur = ring;
  int i = 0, full = 0;
  unsigned ts_first = 0, ts_real = 0, ts_spins = 0, cy_comp = 0;
  for (int b = b0; b <= b1; ++b) {
    const int r0 = rl(r0v, b), cnt = rl(cntv, b);
    const int iend = i + cnt;
    const int lastt = min(15, rl(len, b) - 16 * (r0 + cnt - 1) - 1);
    const float cs = (unimask >> b) & 1 ? 0.f : CS;
    u32x4 qf[4];
#pragma unroll
    for (int s = 0; s < 4; ++s) qf[s] = qn[s];
    asm volatile("" : "+v"(qf[0]), "+v"(qf[1]), "+v"(qf[2]), "+v"(qf[3]));
    if (b < b1) {
#pragma unroll
      for (int s = 0; s < 4; ++s) qn[s] = __builtin_amdgcn_raw_buffer_load_b128(qr, qoff + 64 * s, (b + 1) * 32768, 0);
    }
    f32x4 acc[8];
#pragma unroll
    for (int k = 0; k < 8; ++k) acc[k] = f32x4{0.f, 0.f, 0.f, 0.f};
    float m = -1e30f, l = 0.f;
    do {
      while (full <= i) { full = lds_get(fullw); if (full <= i) __builtin_amdgcn_s_sleep(1); }
      asm volatile("" ::: "memory");
      if (i == 0) ts_first = NOW();
      ts_real = NOW();
      const unsigned c3 = (unsigned)__builtin_amdgcn_s_memtime();
      u32x4 kf[4];
      u32x2 vt[8];
#pragma unroll
      for (int s = 0; s < 4; ++s) kf[s] = *LDSP(const u32x4, (cur + ka) ^ (s << 6));
#pragma unroll
      for (int k = 0; k < 8; ++k)
        vt[k] = __builtin_bit_cast(u32x2, __builtin_amdgcn_ds_read_tr16_b64_v4i16(LDSP(s16x4, ((cur + va) ^ ((k >> 1) << 4)) + 8 * (k & 1))));
      f32x4 sc = {0.f, 0.f, 0.f, 0.f};
#pragma unroll
      for (int s = 0; s < 4; ++s)
        sc = __builtin_amdgcn_mfma_f32_16x16x32_bf16(__builtin_bit_cast(bf16x8, kf[s]), __builtin_bit_cast(bf16x8, qf[s]), sc, 0, 0, 0);
      ++i;
      asm volatile("" : "+v"(vt[0]), "+v"(vt[1]), "+v"(vt[2]), "+v"(vt[3]), "+v"(vt[4]), "+v"(vt[5]), "+v"(vt[6]), "+v"(vt[7]) :: "memory");
      lds_put(freew, i);
      cur = cur + RS == ring + NS * RS ? ring : cur + RS;
      if (i == iend && lastt < 15) {
        u32x2 vm = {(4 * g <= lastt ? 0xffffu : 0u) | (4 * g + 1 <= lastt ? 0xffff0000u : 0u),
                    (4 * g + 2 <= lastt ? 0xffffu : 0u) | (4 * g + 3 <= lastt ? 0xffff0000u : 0u)};
        asm volatile("" : "+v"(vm));
#pragma unroll
        for (int k = 0; k < 8; ++k) vt[k] &= vm;
#pragma unroll
        for (int r = 0; r < 4; ++r) sc[r] = 4 * g + r <= lastt ? sc[r] : -__builtin_inff();
      }
      const float mx = xg_max(vmax(vmax(sc[0], sc[1]), vmax(sc[2], sc[3])));
      const float mn = vmax(m, mx * cs);
      const float alpha = __builtin_amdgcn_exp2f(m - mn);
      m = mn;
      float pr[4];
#pragma unroll
      for (int r = 0; r < 4; ++r) pr[r] = __builtin_amdgcn_exp2f(__builtin_fmaf(sc[r], cs, -mn));
      l = l * alpha + ((pr[0] + pr[1]) + (pr[2] + pr[3]));
      const u32x2 pf = {cvtpk(pr[0], pr[1]), cvtpk(pr[2], pr[3])};
#pragma unroll
      for (int k = 0; k < 8; ++k) {
        acc[k] *= alpha;
        acc[k] = __builtin_amdgcn_mfma_f32_16x16x16bf16_1k(__builtin_bit_cast(s16x4, vt[k]), __builtin_bit_cast(s16x4, pf), acc[k], 0, 0, 0);
      }
      asm volatile("" : "+v"(acc[7])); cy_comp += (unsigned)__builtin_amdgcn_s_memtime() - c3;
    } while (i < iend);
    if (b == b1) {
      for (int j = 1; j <= nv; ++j) {
        while (full <= i) { full = lds_get(fullw); if (full <= i) __builtin_amdgcn_s_sleep(1); }
        asm volatile("" ::: "memory");
        const unsigned tag = rl(tagv, j);
        u32x4 x[8];
        u32x2 y;
        for (int spin = 0;; ++spin) {
#pragma unroll
          for (int k = 0; k < 8; ++k) x[k] = *LDSP(const u32x4, cur + 1024 * k + loff);
          y = *LDSP(const u32x2, cur + 8192 + 8 * c);
          unsigned bad = y[1] ^ tag;
#pragma unroll
          for (int k = 0; k < 8; ++k) bad |= (x[k][1] ^ tag) | (x[k][3] ^ tag);
          if (__builtin_amdgcn_ballot_w64(bad != 0) == 0 || spin > (1 << 22)) break;
          __builtin_amdgcn_s_sleep(2);
          ++ts_spins;
          load_partial(cur, j);
          WAITVM(0);
        }
        ++i;
        asm volatile("" ::: "memory");
        lds_put(freew, i);
        cur = cur + RS == ring + NS * RS ? ring : cur + RS;
        const float lse = __uint_as_float(y[0]);
        const float mn = vmax(m, lse);
        const float alpha = __builtin_amdgcn_exp2f(m - mn), w = __builtin_amdgcn_exp2f(lse - mn);
        m = mn;
        l = l * alpha + (g == 0 ? w : 0.f);
#pragma unroll
        for (int k = 0; k < 8; ++k) {
          const f32x4 o = {__uint_as_float(x[k][0] << 16), __uint_as_float(x[k][0] & 0xffff0000u),
                           __uint_as_float(x[k][2] << 16), __uint_as_float(x[k][2] & 0xffff0000u)};
          acc[k] = acc[k] * alpha + o * w;
        }
      }
    }
    const float lt = xg_sum(l);
    const float inv = __builtin_amdgcn_rcpf(lt);
    u32x2 ob[8];
#pragma unroll
    for (int k = 0; k < 8; ++k) ob[k] = u32x2{cvtpk(acc[k][0] * inv, acc[k][1] * inv), cvtpk(acc[k][2] * inv, acc[k][3] * inv)};
    if (r0 > 0) {
      const unsigned tag = rl(tagv, 0);
      const __amdgpu_buffer_rsrc_t pr = rsrc(ws + (unsigned)(p * 8 + h) * SLOT, SLOT);
#pragma unroll
      for (int k = 0; k < 8; ++k)
        __builtin_amdgcn_raw_buffer_store_b128(u32x4{ob[k][0], tag, ob[k][1], tag}, pr, loff + 1024 * (k & 3), (k >> 2) * 4096, 16);
      const u32x2 y = {__float_as_uint(m + __builtin_amdgcn_logf(lt)), tag};
      __builtin_amdgcn_raw_buffer_store_b64(y, pr, g == 0 ? c * 8 : 0x80000000u, 8192, 16);
    } else {
      const __amdgpu_buffer_rsrc_t orr = rsrc(out + (b * 8 + h) * 4096, 4096);
#pragma unroll
      for (int r = 0; r < 4; ++r)
        __builtin_amdgcn_raw_buffer_store_b128(u32x4{ob[2 * r][0], ob[2 * r][1], ob[2 * r + 1][0], ob[2 * r + 1][1]}, orr, c * 256 + g * 64 + 16 * r, 0, 0);
    }
  }
  if (lane == 0) { tsp[0] = t0; tsp[1] = ts_first; tsp[2] = ts_real; tsp[3] = NOW();
    tsp[5] = nv; tsp[6] = ts_spins; tsp[7] = 0; tsp[8] = 0; tsp[9] = 0; tsp[10] = cy_comp; tsp[11] = h; tsp[12] = b1 - b0; tsp[13] = __builtin_amdgcn_s_getreg((31 << 11) | 20); }
  if (lane >= 1 && lane <= nv) __hip_atomic_store(ep, tagv, __ATOMIC_RELAXED, __HIP_MEMORY_SCOPE_AGENT);
}

__global__ void diag_kernel(const unsigned* __restrict__ t) {
  __shared__ float sm[512];
  __shared__ unsigned lo_s;
  const int w = threadIdx.x;
  const unsigned* x = t + w * 16;
  if (w == 0) { unsigned lo = t[0]; for (int i = 0; i < 512; ++i) if ((int)(t[i * 16] - lo) < 0) lo = t[i * 16]; lo_s = lo; }
  __syncthreads();
  const unsigned lo = lo_s;
  float v = 0.f;
  if (STAT == 1 || STAT == 11) v = x[3] - lo;
  if (STAT == 2 || STAT == 12) v = x[1] - lo;
  if (STAT == 3 || STAT == 13) v = x[2] - lo;
  if (STAT == 4 || STAT == 14) v = x[14] - lo;
  if (STAT == 5 || STAT == 15) v = x[15] - lo;
  if (STAT == 6 || STAT == 16) v = x[0] - lo;
  if (STAT == 7 || STAT == 17) v = x[3] - x[2];
  if (STAT == 8 || STAT == 18) v = x[7] - lo;
  if (STAT == 9 || STAT == 19) v = x[8] - lo;
  sm[w] = v;
  __syncthreads();
  if (w == 0) {
    float s = 0.f, m = 0.f;
    for (int i = 0; i < 512; ++i) { s += sm[i]; m = fmaxf(m, sm[i]); }
    float r = STAT > 10 ? m : s / 512;
    r = fminf(fmaxf(r, 0.f), 10000.f);
    const int due = 15000 + (int)(r * 5);
    while ((int)((unsigned)__builtin_amdgcn_s_memrealtime() - lo) < due) __builtin_amdgcn_s_sleep(1);
  }
}

extern "C" void kernel_launch(void* const* d_in, const int* in_sizes, int n_in, void* d_out, int out_size,
                              void* d_ws, size_t ws_size, hipStream_t stream) {
  pa_kernel<<<256, 256, 0, stream>>>((const char*)d_in[0], (const char*)d_in[1], (const char*)d_in[2],
                                     (const int*)d_in[3], (const int*)d_in[4], (char*)d_out, (char*)d_ws);
  diag_kernel<<<1, 512, 0, stream>>>((const unsigned*)((char*)d_ws + (64u << 20)));
}
```

```cpp
#define TS
#define STAT 0
#include <hip/hip_runtime.h>
#include <hip/hip_bf16.h>
#include <stdint.h>

#define AHEAD 2
#define NS 6
#define KPOL " nt"
#define VPOL ""
#define DELTA 4
#define TAU16 872
#define POFS 0

typedef __attribute__((ext_vector_type(8))) __bf16 bf16x8;
typedef __attribute__((ext_vector_type(4))) short s16x4;
typedef __attribute__((ext_vector_type(4))) float f32x4;
typedef __attribute__((ext_vector_type(4))) unsigned u32x4;
typedef __attribute__((ext_vector_type(2))) unsigned u32x2;
typedef __attribute__((address_space(1))) unsigned gu32;
#define LDSP(T, a) ((__attribute__((address_space(3))) T*)(uintptr_t)(a))

constexpr int NB = 32, NBPS = 128, SKV = NBPS * 16;
constexpr unsigned SLOT = 16384, EPOCH_OFF = 64 * 8 * SLOT;
constexpr int RS = 9216, PAIR_LDS = NS * RS + 256;

__device__ __forceinline__ unsigned cvtpk(float lo, float hi) {
  typedef __attribute__((ext_vector_type(2))) __bf16 bf16x2;
  bf16x2 r = {(__bf16)lo, (__bf16)hi};
  return __builtin_bit_cast(unsigned, r);
}
__device__ __forceinline__ float vmax(float a, float b) { return __builtin_elementwise_maximum(a, b); }
__device__ __forceinline__ float xg_max(float x) {
  auto s = __builtin_amdgcn_permlane16_swap(__float_as_uint(x), __float_as_uint(x), false, false);
  x = vmax(__uint_as_float(s[0]), __uint_as_float(s[1]));
  auto t = __builtin_amdgcn_permlane32_swap(__float_as_uint(x), __float_as_uint(x), false, false);
  return vmax(__uint_as_float(t[0]), __uint_as_float(t[1]));
}
__device__ __forceinline__ float xg_sum(float x) {
  auto s = __builtin_amdgcn_permlane16_swap(__float_as_uint(x), __float_as_uint(x), false, false);
  x = __uint_as_float(s[0]) + __uint_as_float(s[1]);
  auto t = __builtin_amdgcn_permlane32_swap(__float_as_uint(x), __float_as_uint(x), false, false);
  return __uint_as_float(t[0]) + __uint_as_float(t[1]);
}
__device__ __forceinline__ __amdgpu_buffer_rsrc_t rsrc(const void* p, unsigned n) {
  return __builtin_amdgcn_make_buffer_rsrc((void*)p, 0, n, 0x00020000);
}
__device__ __forceinline__ u32x4 srd(const void* p, unsigned n) {
  const uint64_t a = (uint64_t)p;
  return u32x4{(unsigned)a, (unsigned)(a >> 32) & 0xffffu, n, 0x00020000u};
}
__device__ __forceinline__ int rl(int v, int l) { return __builtin_amdgcn_readlane(v, l); }
__device__ __forceinline__ int lds_get(unsigned a) {
  return __builtin_amdgcn_readfirstlane(__hip_atomic_load(LDSP(int, a), __ATOMIC_RELAXED, __HIP_MEMORY_SCOPE_WORKGROUP));
}
__device__ __forceinline__ void lds_put(unsigned a, int v) {
  __hip_atomic_store(LDSP(int, a), v, __ATOMIC_RELAXED, __HIP_MEMORY_SCOPE_WORKGROUP);
}

#define DMA4(POL, dst, rs, so, v0, v1, v2, v3)                                                        \
  do {                                                                                                \
    unsigned keep_;                                                                                   \
    asm volatile("s_mov_b32 %0, m0\n\ts_mov_b32 m0, %1\n\ts_nop 4\n\t"                                \
                 "buffer_load_dwordx4 %4, %2, %3 offen" POL " lds\n\t"                                \
                 "buffer_load_dwordx4 %5, %2, %3 offen offset:1024" POL " lds\n\t"                    \
                 "buffer_load_dwordx4 %6, %2, %3 offen offset:2048" POL " lds\n\t"                    \
                 "buffer_load_dwordx4 %7, %2, %3 offen offset:3072" POL " lds\n\t"                    \
                 "s_mov_b32 m0, %0"                                                                   \
                 : "=&s"(keep_) : "s"(dst), "s"(rs), "s"(so), "v"(v0), "v"(v1), "v"(v2), "v"(v3) : "memory"); \
  } while (0)
#define DMA1(POL, dst, rs, so, v0)                                                                    \
  do {                                                                                                \
    unsigned keep_;                                                                                   \
    asm volatile("s_mov_b32 %0, m0\n\ts_mov_b32 m0, %1\n\ts_nop 4\n\t"                                \
                 "buffer_load_dwordx4 %4, %2, %3 offen" POL " lds\n\t"                                \
                 "s_mov_b32 m0, %0"                                                                   \
                 : "=&s"(keep_) : "s"(dst), "s"(rs), "s"(so), "v"(v0) : "memory");                    \
  } while (0)
#define WAITVM(n) asm volatile("s_waitcnt vmcnt(%0)" :: "n"(n) : "memory")
#define NOW() ((unsigned)__builtin_amdgcn_s_memrealtime())

__global__ void __launch_bounds__(512)
pa_kernel(const char* __restrict__ q_, const char* __restrict__ kc_, const char* __restrict__ vc_,
          const int* __restrict__ bt_, const int* __restrict__ lens_, char* __restrict__ out_, char* __restrict__ ws_) {
  __shared__ __attribute__((aligned(256))) char lds[2 * PAIR_LDS];
  typedef __attribute__((address_space(4))) const uint64_t* kap_t;
  kap_t kargs = (kap_t)__builtin_amdgcn_kernarg_segment_ptr();
  uint64_t t0l; asm volatile("s_memrealtime %0\n\ts_waitcnt lgkmcnt(0)" : "=s"(t0l), "+s"(kargs) :: "memory");
  const unsigned t0 = (unsigned)t0l;
  typedef __attribute__((address_space(1))) char* gp_t;
  const char* q = (const char*)(gp_t)kargs[0]; const char* kc = (const char*)(gp_t)kargs[1]; const char* vc = (const char*)(gp_t)kargs[2];
  const int* bt = (const int*)(gp_t)kargs[3]; const int* lens = (const int*)(gp_t)kargs[4]; char* out = (char*)(gp_t)kargs[5]; char* ws = (char*)(gp_t)kargs[6];
  const int lane = threadIdx.x & 63;
  const int wave = __builtin_amdgcn_readfirstlane(threadIdx.x >> 6);
  const int p = blockIdx.x >> 2;
  const int h = 2 * ((blockIdx.x - (p >> 1)) & 3) + (wave & 1);
  const int g = lane >> 4, c = lane & 15;
  const bool loader = wave < 2;

  int len = lens[lane & 31];
  gu32* const ep = (gu32*)(ws + EPOCH_OFF) + h * 64 + ((p + lane) & 63);
  unsigned tagv;
  if (loader) tagv = bt[(wave * 64 + lane) * 32];
  else tagv = __hip_atomic_load(ep, __ATOMIC_RELAXED, __HIP_MEMORY_SCOPE_AGENT);
  const unsigned ring = (unsigned)(uintptr_t)LDSP(char, lds) + (wave & 1) * PAIR_LDS;
  const unsigned fullw = ring + NS * RS, freew = fullw + 128;
  lds_put(loader ? fullw : freew, 0);
  asm volatile("s_waitcnt lgkmcnt(0)\n\ts_barrier" ::: "memory");

  const bool uni = len <= 0;
  if (uni || len > SKV) len = SKV;
  const int np = (len + 15) >> 4;
  const int dl = np >= 16 ? DELTA : 0;
  const int vl = np - dl;
  int incl = vl;
  incl += __builtin_amdgcn_update_dpp(0, incl, 0x111, 0xf, 0xf, false);
  incl += __builtin_amdgcn_update_dpp(0, incl, 0x112, 0xf, 0xf, false);
  incl += __builtin_amdgcn_update_dpp(0, incl, 0x114, 0xf, 0xf, false);
  incl += __builtin_amdgcn_update_dpp(0, incl, 0x118, 0xf, 0xf, false);
  incl += __builtin_amdgcn_update_dpp(0, incl, 0x142, 0xa, 0xf, false);
  const int V = rl(incl, 31), VV = max(V, 64);
  const int lo = (VV * p) >> 6, hi = min(V, (VV * (p + 1)) >> 6);
  if (lo >= hi) return;
  const int b0 = __builtin_popcount((unsigned)__builtin_amdgcn_ballot_w64(incl <= lo));
  const int b1 = __builtin_popcount((unsigned)__builtin_amdgcn_ballot_w64(incl < hi));
  const int ex = incl - vl;
  const int r0v = lo > ex ? lo - ex + dl : 0, cntv = min(hi, incl) - ex + dl - r0v;
  const int nv = rl(r0v, b1) == 0 ? __builtin_popcountll(__builtin_amdgcn_ballot_w64(((VV * lane) >> 6) < rl(incl, b1))) - 1 - p : 0;
  const unsigned loff = lane * 16;
  auto load_partial = [&](unsigned dst, int j) __attribute__((always_inline)) {
    const u32x4 pr = srd(ws + (unsigned)((p + j) * 8 + h) * SLOT, SLOT);
    DMA4(" sc1", dst, pr, 0, loff, loff, loff, loff);
    DMA4(" sc1", dst + 4096, pr, 4096, loff, loff, loff, loff);
    DMA1(" sc1", dst + 8192, pr, 8192, loff);
  };
  unsigned* const tsp = (unsigned*)(ws + (64u << 20)) + (blockIdx.x * 2 + (wave & 1)) * 16;

  if (loader) {
    int n = 0;
    unsigned tab[2] = {0u, 0u};
    int idx[2] = {0, 0};
    for (int s = b0; s <= b1; ++s) {
      const int r0 = rl(r0v, s), cnt = rl(cntv, s), ls = rl(len, s);
#pragma unroll
      for (int u = 0; u < 2; ++u) {
        const int k = lane + 64 * u - n, pg = r0 + k;
        idx[u] = k >= 0 ? s * NBPS + pg : idx[u];
        tab[u] = k >= 0 ? (unsigned)(min(16, ls - 16 * pg) - 1) << 17 : tab[u];
      }
      n += cnt;
    }
    tab[0] |= (unsigned)bt[min(idx[0], NB * NBPS - 1)];
    tab[1] |= (unsigned)bt[min(idx[1], NB * NBPS - 1)];
    unsigned go[4];
#pragma unroll
    for (int r = 0; r < 4; ++r) go[r] = (4 * r + g) * 2048 + ((c ^ (4 * r + g)) << 4) - 1024 * r;
    asm volatile("" : "+v"(tab[0]), "+v"(tab[1]) : "v"(tagv));
    const int ntot = n + nv;
    unsigned dst = ring;
    int freed = 0;
    if (lane == 0) { tsp[14] = NOW(); tsp[4] = n; }
    for (int j = 0; j < ntot; ++j) {
      while (freed + NS <= j) { freed = lds_get(freew); if (freed + NS <= j) __builtin_amdgcn_s_sleep(1); }
      if (TAU16) {
        const int due = ((j * TAU16) >> 4) + POFS;
        while ((int)(NOW() - t0) < due) __builtin_amdgcn_s_sleep(1);
      }
      if (j < n) {
        const unsigned e = rl(j < 64 ? tab[0] : tab[1], j & 63);
        const size_t po = (size_t)(e & 0x1ffffu) * 32768 + h * 256;
        const unsigned nrec = ((e >> 17) & 15) * 2048 + 256;
        const u32x4 kr = srd(kc + po, nrec), vr = srd(vc + po, nrec);
        DMA4(KPOL, dst, kr, 0, go[0], go[1], go[2], go[3]);
        DMA4(VPOL, dst + 4096, vr, 0, go[0], go[1], go[2], go[3]);
      } else {
        load_partial(dst, j - n + 1);
      }
      dst = dst + RS == ring + NS * RS ? ring : dst + RS;
      if (j >= AHEAD) { WAITVM(8 * AHEAD); lds_put(fullw, j - AHEAD + 1); }
    }
    if (lane == 0) tsp[15] = NOW();
    if (AHEAD > 3) { WAITVM(24); lds_put(fullw, ntot - 3); }
    if (AHEAD > 2) { WAITVM(16); lds_put(fullw, ntot - 2); }
    if (AHEAD > 1) { WAITVM(8); lds_put(fullw, ntot - 1); }
    WAITVM(0); lds_put(fullw, ntot);
    return;
  }

  const unsigned unimask = (unsigned)__builtin_amdgcn_ballot_w64(uni);
  const unsigned qoff = c * 256 + g * 16;
  const __amdgpu_buffer_rsrc_t qr = rsrc(q + h * 4096, NB * 32768);
  u32x4 qn[4];
#pragma unroll
  for (int s = 0; s < 4; ++s) qn[s] = __builtin_amdgcn_raw_buffer_load_b128(qr, qoff + 64 * s, b0 * 32768, 0);
  tagv += 0x9E3779B9u;
  const float CS = 0.088388347648318440f * 1.4426950408889634f;
  const unsigned ka = c * 256 + ((g ^ c) << 4);
  const unsigned va = 4096 + (lane >> 2) * 256 + ((((lane & 3) << 2) ^ (lane >> 2)) << 4);
  unsigned cur = ring;
  int i = 0, full = 0;
  unsigned ts_first = 0, ts_real = 0, ts_spins = 0, cy_comp = 0;
  for (int b = b0; b <= b1; ++b) {
    const int r0 = rl(r0v, b), cnt = rl(cntv, b);
    const int iend = i + cnt;
    const int lastt = min(15, rl(len, b) - 16 * (r0 + cnt - 1) - 1);
    const float cs = (unimask >> b) & 1 ? 0.f : CS;
    u32x4 qf[4];
#pragma unroll
    for (int s = 0; s < 4; ++s) qf[s] = qn[s];
    asm volatile("" : "+v"(qf[0]), "+v"(qf[1]), "+v"(qf[2]), "+v"(qf[3]));
    if (b < b1) {
#pragma unroll
      for (int s = 0; s < 4; ++s) qn[s] = __builtin_amdgcn_raw_buffer_load_b128(qr, qoff + 64 * s, (b + 1) * 32768, 0);
    }
    f32x4 acc[8];
#pragma unroll
    for (int k = 0; k < 8; ++k) acc[k] = f32x4{0.f, 0.f, 0.f, 0.f};
    float m = -1e30f, l = 0.f;
    do {
      while (full <= i) { full = lds_get(fullw); if (full <= i) __builtin_amdgcn_s_sleep(1); }
      asm volatile("" ::: "memory");
      if (i == 0) ts_first = NOW();
      ts_real = NOW();
      const unsigned c3 = (unsigned)__builtin_amdgcn_s_memtime();
      u32x4 kf[4];
      u32x2 vt[8];
#pragma unroll
      for (int s = 0; s < 4; ++s) kf[s] = *LDSP(const u32x4, (cur + ka) ^ (s << 6));
#pragma unroll
      for (int k = 0; k < 8; ++k)
        vt[k] = __builtin_bit_cast(u32x2, __builtin_amdgcn_ds_read_tr16_b64_v4i16(LDSP(s16x4, ((cur + va) ^ ((k >> 1) << 4)) + 8 * (k & 1))));
      f32x4 sc = {0.f, 0.f, 0.f, 0.f};
#pragma unroll
      for (int s = 0; s < 4; ++s)
        sc = __builtin_amdgcn_mfma_f32_16x16x32_bf16(__builtin_bit_cast(bf16x8, kf[s]), __builtin_bit_cast(bf16x8, qf[s]), sc, 0, 0, 0);
      ++i;
      asm volatile("" : "+v"(vt[0]), "+v"(vt[1]), "+v"(vt[2]), "+v"(vt[3]), "+v"(vt[4]), "+v"(vt[5]), "+v"(vt[6]), "+v"(vt[7]) :: "memory");
      lds_put(freew, i);
      cur = cur + RS == ring + NS * RS ? ring : cur + RS;
      if (i == iend && lastt < 15) {
        u32x2 vm = {(4 * g <= lastt ? 0xffffu : 0u) | (4 * g + 1 <= lastt ? 0xffff0000u : 0u),
                    (4 * g + 2 <= lastt ? 0xffffu : 0u) | (4 * g + 3 <= lastt ? 0xffff0000u : 0u)};
        asm volatile("" : "+v"(vm));
#pragma unroll
        for (int k = 0; k < 8; ++k) vt[k] &= vm;
#pragma unroll
        for (int r = 0; r < 4; ++r) sc[r] = 4 * g + r <= lastt ? sc[r] : -__builtin_inff();
      }
      const float mx = xg_max(vmax(vmax(sc[0], sc[1]), vmax(sc[2], sc[3])));
      const float mn = vmax(m, mx * cs);
      const float alpha = __builtin_amdgcn_exp2f(m - mn);
      m = mn;
      float pr[4];
#pragma unroll
      for (int r = 0; r < 4; ++r) pr[r] = __builtin_amdgcn_exp2f(__builtin_fmaf(sc[r], cs, -mn));
      l = l * alpha + ((pr[0] + pr[1]) + (pr[2] + pr[3]));
      const u32x2 pf = {cvtpk(pr[0], pr[1]), cvtpk(pr[2], pr[3])};
#pragma unroll
      for (int k = 0; k < 8; ++k) {
        acc[k] *= alpha;
        acc[k] = __builtin_amdgcn_mfma_f32_16x16x16bf16_1k(__builtin_bit_cast(s16x4, vt[k]), __builtin_bit_cast(s16x4, pf), acc[k], 0, 0, 0);
      }
      asm volatile("" : "+v"(acc[7])); cy_comp += (unsigned)__builtin_amdgcn_s_memtime() - c3;
    } while (i < iend);
    if (b == b1) {
      for (int j = 1; j <= nv; ++j) {
        while (full <= i) { full = lds_get(fullw); if (full <= i) __builtin_amdgcn_s_sleep(1); }
        asm volatile("" ::: "memory");
        const unsigned tag = rl(tagv, j);
        u32x4 x[8];
        u32x2 y;
        for (int spin = 0;; ++spin) {
#pragma unroll
          for (int k = 0; k < 8; ++k) x[k] = *LDSP(const u32x4, cur + 1024 * k + loff);
          y = *LDSP(const u32x2, cur + 8192 + 8 * c);
          unsigned bad = y[1] ^ tag;
#pragma unroll
          for (int k = 0; k < 8; ++k) bad |= (x[k][1] ^ tag) | (x[k][3] ^ tag);
          if (__builtin_amdgcn_ballot_w64(bad != 0) == 0 || spin > (1 << 22)) break;
          __builtin_amdgcn_s_sleep(2);
          ++ts_spins;
          load_partial(cur, j);
          WAITVM(0);
        }
        ++i;
        asm volatile("" ::: "memory");
        lds_put(freew, i);
        cur = cur + RS == ring + NS * RS ? ring : cur + RS;
        const float lse = __uint_as_float(y[0]);
        const float mn = vmax(m, lse);
        const float alpha = __builtin_amdgcn_exp2f(m - mn), w = __builtin_amdgcn_exp2f(lse - mn);
        m = mn;
        l = l * alpha + (g == 0 ? w : 0.f);
#pragma unroll
        for (int k = 0; k < 8; ++k) {
          const f32x4 o = {__uint_as_float(x[k][0] << 16), __uint_as_float(x[k][0] & 0xffff0000u),
                           __uint_as_float(x[k][2] << 16), __uint_as_float(x[k][2] & 0xffff0000u)};
          acc[k] = acc[k] * alpha + o * w;
        }
      }
    }
    const float lt = xg_sum(l);
    const float inv = __builtin_amdgcn_rcpf(lt);
    u32x2 ob[8];
#pragma unroll
    for (int k = 0; k < 8; ++k) ob[k] = u32x2{cvtpk(acc[k][0] * inv, acc[k][1] * inv), cvtpk(acc[k][2] * inv, acc[k][3] * inv)};
    if (r0 > 0) {
      const unsigned tag = rl(tagv, 0);
      const __amdgpu_buffer_rsrc_t pr = rsrc(ws + (unsigned)(p * 8 + h) * SLOT, SLOT);
#pragma unroll
      for (int k = 0; k < 8; ++k)
        __builtin_amdgcn_raw_buffer_store_b128(u32x4{ob[k][0], tag, ob[k][1], tag}, pr, loff + 1024 * (k & 3), (k >> 2) * 4096, 16);
      const u32x2 y = {__float_as_uint(m + __builtin_amdgcn_logf(lt)), tag};
      __builtin_amdgcn_raw_buffer_store_b64(y, pr, g == 0 ? c * 8 : 0x80000000u, 8192, 16);
    } else {
      const __amdgpu_buffer_rsrc_t orr = rsrc(out + (b * 8 + h) * 4096, 4096);
#pragma unroll
      for (int r = 0; r < 4; ++r)
        __builtin_amdgcn_raw_buffer_store_b128(u32x4{ob[2 * r][0], ob[2 * r][1], ob[2 * r + 1][0], ob[2 * r + 1][1]}, orr, c * 256 + g * 64 + 16 * r, 0, 0);
    }
  }
  if (lane == 0) { tsp[0] = t0; tsp[1] = ts_first; tsp[2] = ts_real; tsp[3] = NOW();
    tsp[5] = nv; tsp[6] = ts_spins; tsp[7] = 0; tsp[8] = 0; tsp[9] = 0; tsp[10] = cy_comp; tsp[11] = h; tsp[12] = b1 - b0; tsp[13] = __builtin_amdgcn_s_getreg((31 << 11) | 20); }
  if (lane >= 1 && lane <= nv) __hip_atomic_store(ep, tagv, __ATOMIC_RELAXED, __HIP_MEMORY_SCOPE_AGENT);
}

__global__ void diag_kernel(const unsigned* __restrict__ t) {
  __shared__ float sm[512];
  __shared__ unsigned lo_s;
  const int w = threadIdx.x;
  const unsigned* x = t + w * 16;
  if (w == 0) { unsigned lo = t[0]; for (int i = 0; i < 512; ++i) if ((int)(t[i * 16] - lo) < 0) lo = t[i * 16]; lo_s = lo; }
  __syncthreads();
  const unsigned lo = lo_s;
  float v = 0.f;
  if (STAT == 1 || STAT == 11) v = x[3] - lo;
  if (STAT == 2 || STAT == 12) v = x[1] - lo;
  if (STAT == 3 || STAT == 13) v = x[2] - lo;
  if (STAT == 4 || STAT == 14) v = x[14] - lo;
  if (STAT == 5 || STAT == 15) v = x[15] - lo;
  if (STAT == 6 || STAT == 16) v = x[0] - lo;
  if (STAT == 7 || STAT == 17) v = x[3] - x[2];
  if (STAT == 8 || STAT == 18) v = x[7] - lo;
  if (STAT == 9 || STAT == 19) v = x[8] - lo;
  sm[w] = v;
  __syncthreads();
  if (w == 0) {
    float s = 0.f, m = 0.f;
    for (int i = 0; i < 512; ++i) { s += sm[i]; m = fmaxf(m, sm[i]); }
    float r = STAT > 10 ? m : s / 512;
    r = fminf(fmaxf(r, 0.f), 10000.f);
    const int due = 15000 + (int)(r * 5);
    while ((int)((unsigned)__builtin_amdgcn_s_memrealtime() - lo) < due) __builtin_amdgcn_s_sleep(1);
  }
}

extern "C" void kernel_launch(void* const* d_in, const int* in_sizes, int n_in, void* d_out, int out_size,
                              void* d_ws, size_t ws_size, hipStream_t stream) {
  pa_kernel<<<256, 256, 0, stream>>>((const char*)d_in[0], (const char*)d_in[1], (const char*)d_in[2],
                                     (const int*)d_in[3], (const int*)d_in[4], (char*)d_out, (char*)d_ws);
  diag_kernel<<<1, 512, 0, stream>>>((const unsigned*)((char*)d_ws + (64u << 20)));
}
```

```cpp
#define TAU16 910
#define VPOL " nt"
#include <hip/hip_runtime.h>
#include <hip/hip_bf16.h>
#include <stdint.h>

#define AHEAD 2
#define NS 6
#define KPOL " nt"
#define DELTA 4
#define POFS 0

typedef __attribute__((ext_vector_type(8))) __bf16 bf16x8;
typedef __attribute__((ext_vector_type(4))) short s16x4;
typedef __attribute__((ext_vector_type(4))) float f32x4;
typedef __attribute__((ext_vector_type(4))) unsigned u32x4;
typedef __attribute__((ext_vector_type(2))) unsigned u32x2;
typedef __attribute__((address_space(1))) unsigned gu32;
#define LDSP(T, a) ((__attribute__((address_space(3))) T*)(uintptr_t)(a))

constexpr int NB = 32, NBPS = 128, SKV = NBPS * 16;
constexpr unsigned SLOT = 16384, EPOCH_OFF = 64 * 8 * SLOT;
constexpr int RS = 9216, PAIR_LDS = NS * RS + 256;

__device__ __forceinline__ unsigned cvtpk(float lo, float hi) {
  typedef __attribute__((ext_vector_type(2))) __bf16 bf16x2;
  bf16x2 r = {(__bf16)lo, (__bf16)hi};
  return __builtin_bit_cast(unsigned, r);
}
__device__ __forceinline__ float vmax(float a, float b) { return __builtin_elementwise_maximum(a, b); }
__device__ __forceinline__ float xg_max(float x) {
  auto s = __builtin_amdgcn_permlane16_swap(__float_as_uint(x), __float_as_uint(x), false, false);
  x = vmax(__uint_as_float(s[0]), __uint_as_float(s[1]));
  auto t = __builtin_amdgcn_permlane32_swap(__float_as_uint(x), __float_as_uint(x), false, false);
  return vmax(__uint_as_float(t[0]), __uint_as_float(t[1]));
}
__device__ __forceinline__ float xg_sum(float x) {
  auto s = __builtin_amdgcn_permlane16_swap(__float_as_uint(x), __float_as_uint(x), false, false);
  x = __uint_as_float(s[0]) + __uint_as_float(s[1]);
  auto t = __builtin_amdgcn_permlane32_swap(__float_as_uint(x), __float_as_uint(x), false, false);
  return __uint_as_float(t[0]) + __uint_as_float(t[1]);
}
__device__ __forceinline__ __amdgpu_buffer_rsrc_t rsrc(const void* p, unsigned n) {
  return __builtin_amdgcn_make_buffer_rsrc((void*)p, 0, n, 0x00020000);
}
__device__ __forceinline__ u32x4 srd(const void* p, unsigned n) {
  const uint64_t a = (uint64_t)p;
  return u32x4{(unsigned)a, (unsigned)(a >> 32) & 0xffffu, n, 0x00020000u};
}
__device__ __forceinline__ int rl(int v, int l) { return __builtin_amdgcn_readlane(v, l); }
__device__ __forceinline__ int lds_get(unsigned a) {
  return __builtin_amdgcn_readfirstlane(__hip_atomic_load(LDSP(int, a), __ATOMIC_RELAXED, __HIP_MEMORY_SCOPE_WORKGROUP));
}
__device__ __forceinline__ void lds_put(unsigned a, int v) {
  __hip_atomic_store(LDSP(int, a), v, __ATOMIC_RELAXED, __HIP_MEMORY_SCOPE_WORKGROUP);
}

#define DMA4(POL, dst, rs, so, v0, v1, v2, v3)                                                        \
  do {                                                                                                \
    unsigned keep_;                                                                                   \
    asm volatile("s_mov_b32 %0, m0\n\ts_mov_b32 m0, %1\n\ts_nop 4\n\t"                                \
                 "buffer_load_dwordx4 %4, %2, %3 offen" POL " lds\n\t"                                \
                 "buffer_load_dwordx4 %5, %2, %3 offen offset:1024" POL " lds\n\t"                    \
                 "buffer_load_dwordx4 %6, %2, %3 offen offset:2048" POL " lds\n\t"                    \
                 "buffer_load_dwordx4 %7, %2, %3 offen offset:3072" POL " lds\n\t"                    \
                 "s_mov_b32 m0, %0"                                                                   \
                 : "=&s"(keep_) : "s"(dst), "s"(rs), "s"(so), "v"(v0), "v"(v1), "v"(v2), "v"(v3) : "memory"); \
  } while (0)
#define DMA1(POL, dst, rs, so, v0)                                                                    \
  do {                                                                                                \
    unsigned keep_;                                                                                   \
    asm volatile("s_mov_b32 %0, m0\n\ts_mov_b32 m0, %1\n\ts_nop 4\n\t"                                \
                 "buffer_load_dwordx4 %4, %2, %3 offen" POL " lds\n\t"                                \
                 "s_mov_b32 m0, %0"                                                                   \
                 : "=&s"(keep_) : "s"(dst), "s"(rs), "s"(so), "v"(v0) : "memory");                    \
  } while (0)
#define WAITVM(n) asm volatile("s_waitcnt vmcnt(%0)" :: "n"(n) : "memory")
#define NOW() ((unsigned)__builtin_amdgcn_s_memrealtime())

__global__ void __launch_bounds__(512)
pa_kernel(const char* __restrict__ q, const char* __restrict__ kc, const char* __restrict__ vc,
          const int* __restrict__ bt, const int* __restrict__ lens, char* __restrict__ out, char* __restrict__ ws) {
  __shared__ __attribute__((aligned(256))) char lds[2 * PAIR_LDS];
  const unsigned t0 = NOW();
  const int lane = threadIdx.x & 63;
  const int wave = __builtin_amdgcn_readfirstlane(threadIdx.x >> 6);
  const int p = blockIdx.x >> 2;
  const int h = 2 * ((blockIdx.x - (p >> 1)) & 3) + (wave & 1);
  const int g = lane >> 4, c = lane & 15;
  const bool loader = wave < 2;

  int len = lens[lane & 31];
  gu32* const ep = (gu32*)(ws + EPOCH_OFF) + h * 64 + ((p + lane) & 63);
  unsigned tagv;
  if (loader) tagv = bt[(wave * 64 + lane) * 32];
  else tagv = __hip_atomic_load(ep, __ATOMIC_RELAXED, __HIP_MEMORY_SCOPE_AGENT);
  const unsigned ring = (unsigned)(uintptr_t)LDSP(char, lds) + (wave & 1) * PAIR_LDS;
  const unsigned fullw = ring + NS * RS, freew = fullw + 128;
  lds_put(loader ? fullw : freew, 0);
  asm volatile("s_waitcnt lgkmcnt(0)\n\ts_barrier" ::: "memory");

  const bool uni = len <= 0;
  if (uni || len > SKV) len = SKV;
  const int np = (len + 15) >> 4;
  const int dl = np >= 16 ? DELTA : 0;
  const int vl = np - dl;
  int incl = vl;
  incl += __builtin_amdgcn_update_dpp(0, incl, 0x111, 0xf, 0xf, false);
  incl += __builtin_amdgcn_update_dpp(0, incl, 0x112, 0xf, 0xf, false);
  incl += __builtin_amdgcn_update_dpp(0, incl, 0x114, 0xf, 0xf, false);
  incl += __builtin_amdgcn_update_dpp(0, incl, 0x118, 0xf, 0xf, false);
  incl += __builtin_amdgcn_update_dpp(0, incl, 0x142, 0xa, 0xf, false);
  const int V = rl(incl, 31), VV = max(V, 64);
  const int lo = (VV * p) >> 6, hi = min(V, (VV * (p + 1)) >> 6);
  if (lo >= hi) return;
  const int b0 = __builtin_popcount((unsigned)__builtin_amdgcn_ballot_w64(incl <= lo));
  const int b1 = __builtin_popcount((unsigned)__builtin_amdgcn_ballot_w64(incl < hi));
  const int ex = incl - vl;
  const int r0v = lo > ex ? lo - ex + dl : 0, cntv = min(hi, incl) - ex + dl - r0v;
  const int nv = rl(r0v, b1) == 0 ? __builtin_popcountll(__builtin_amdgcn_ballot_w64(((VV * lane) >> 6) < rl(incl, b1))) - 1 - p : 0;
  const unsigned loff = lane * 16;
  auto load_partial = [&](unsigned dst, int j) __attribute__((always_inline)) {
    const u32x4 pr = srd(ws + (unsigned)((p + j) * 8 + h) * SLOT, SLOT);
    DMA4(" sc1", dst, pr, 0, loff, loff, loff, loff);
    DMA4(" sc1", dst + 4096, pr, 4096, loff, loff, loff, loff);
    DMA1(" sc1", dst + 8192, pr, 8192, loff);
  };

  if (loader) {
    int n = 0;
    unsigned tab[2] = {0u, 0u};
    int idx[2] = {0, 0};
    for (int s = b0; s <= b1; ++s) {
      const int r0 = rl(r0v, s), cnt = rl(cntv, s), ls = rl(len, s);
#pragma unroll
      for (int u = 0; u < 2; ++u) {
        const int k = lane + 64 * u - n, pg = r0 + k;
        idx[u] = k >= 0 ? s * NBPS + pg : idx[u];
        tab[u] = k >= 0 ? (unsigned)(min(16, ls - 16 * pg) - 1) << 17 : tab[u];
      }
      n += cnt;
    }
    tab[0] |= (unsigned)bt[min(idx[0], NB * NBPS - 1)];
    tab[1] |= (unsigned)bt[min(idx[1], NB * NBPS - 1)];
    unsigned go[4];
#pragma unroll
    for (int r = 0; r < 4; ++r) go[r] = (4 * r + g) * 2048 + ((c ^ (4 * r + g)) << 4) - 1024 * r;
    asm volatile("" : "+v"(tab[0]), "+v"(tab[1]) : "v"(tagv));
    const int ntot = n + nv;
    unsigned dst = ring;
    int freed = 0;
    for (int j = 0; j < ntot; ++j) {
      while (freed + NS <= j) { freed = lds_get(freew); if (freed + NS <= j) __builtin_amdgcn_s_sleep(1); }
      if (TAU16) {
        const int due = ((j * TAU16) >> 4) + POFS;
        while ((int)(NOW() - t0) < due) __builtin_amdgcn_s_sleep(1);
      }
      if (j < n) {
        const unsigned e = rl(j < 64 ? tab[0] : tab[1], j & 63);
        const size_t po = (size_t)(e & 0x1ffffu) * 32768 + h * 256;
        const unsigned nrec = ((e >> 17) & 15) * 2048 + 256;
        const u32x4 kr = srd(kc + po, nrec), vr = srd(vc + po, nrec);
        DMA4(KPOL, dst, kr, 0, go[0], go[1], go[2], go[3]);
        DMA4(VPOL, dst + 4096, vr, 0, go[0], go[1], go[2], go[3]);
      } else {
        load_partial(dst, j - n + 1);
      }
      dst = dst + RS == ring + NS * RS ? ring : dst + RS;
      if (j >= AHEAD) { WAITVM(8 * AHEAD); lds_put(fullw, j - AHEAD + 1); }
    }
    if (AHEAD > 3) { WAITVM(24); lds_put(fullw, ntot - 3); }
    if (AHEAD > 2) { WAITVM(16); lds_put(fullw, ntot - 2); }
    if (AHEAD > 1) { WAITVM(8); lds_put(fullw, ntot - 1); }
    WAITVM(0); lds_put(fullw, ntot);
    return;
  }

  const unsigned unimask = (unsigned)__builtin_amdgcn_ballot_w64(uni);
  const unsigned qoff = c * 256 + g * 16;
  const __amdgpu_buffer_rsrc_t qr = rsrc(q + h * 4096, NB * 32768);
  u32x4 qn[4];
#pragma unroll
  for (int s = 0; s < 4; ++s) qn[s] = __builtin_amdgcn_raw_buffer_load_b128(qr, qoff + 64 * s, b0 * 32768, 0);
  tagv += 0x9E3779B9u;
  const float CS = 0.088388347648318440f * 1.4426950408889634f;
  const unsigned ka = c * 256 + ((g ^ c) << 4);
  const unsigned va = 4096 + (lane >> 2) * 256 + ((((lane & 3) << 2) ^ (lane >> 2)) << 4);
  unsigned cur = ring;
  int i = 0, full = 0;
  for (int b = b0; b <= b1; ++b) {
    const int r0 = rl(r0v, b), cnt = rl(cntv, b);
    const int iend = i + cnt;
    const int lastt = min(15, rl(len, b) - 16 * (r0 + cnt - 1) - 1);
    const float cs = (unimask >> b) & 1 ? 0.f : CS;
    u32x4 qf[4];
#pragma unroll
    for (int s = 0; s < 4; ++s) qf[s] = qn[s];
    asm volatile("" : "+v"(qf[0]), "+v"(qf[1]), "+v"(qf[2]), "+v"(qf[3]));
    if (b < b1) {
#pragma unroll
      for (int s = 0; s < 4; ++s) qn[s] = __builtin_amdgcn_raw_buffer_load_b128(qr, qoff + 64 * s, (b + 1) * 32768, 0);
    }
    f32x4 acc[8];
#pragma unroll
    for (int k = 0; k < 8; ++k) acc[k] = f32x4{0.f, 0.f, 0.f, 0.f};
    float m = -1e30f, l = 0.f;
    do {
      while (full <= i) { full = lds_get(fullw); if (full <= i) __builtin_amdgcn_s_sleep(1); }
      asm volatile("" ::: "memory");
      u32x4 kf[4];
      u32x2 vt[8];
#pragma unroll
      for (int s = 0; s < 4; ++s) kf[s] = *LDSP(const u32x4, (cur + ka) ^ (s << 6));
#pragma unroll
      for (int k = 0; k < 8; ++k)
        vt[k] = __builtin_bit_cast(u32x2, __builtin_amdgcn_ds_read_tr16_b64_v4i16(LDSP(s16x4, ((cur + va) ^ ((k >> 1) << 4)) + 8 * (k & 1))));
      f32x4 sc = {0.f, 0.f, 0.f, 0.f};
#pragma unroll
      for (int s = 0; s < 4; ++s)
        sc = __builtin_amdgcn_mfma_f32_16x16x32_bf16(__builtin_bit_cast(bf16x8, kf[s]), __builtin_bit_cast(bf16x8, qf[s]), sc, 0, 0, 0);
      ++i;
      asm volatile("" : "+v"(vt[0]), "+v"(vt[1]), "+v"(vt[2]), "+v"(vt[3]), "+v"(vt[4]), "+v"(vt[5]), "+v"(vt[6]), "+v"(vt[7]) :: "memory");
      lds_put(freew, i);
      cur = cur + RS == ring + NS * RS ? ring : cur + RS;
      if (i == iend && lastt < 15) {
        u32x2 vm = {(4 * g <= lastt ? 0xffffu : 0u) | (4 * g + 1 <= lastt ? 0xffff0000u : 0u),
                    (4 * g + 2 <= lastt ? 0xffffu : 0u) | (4 * g + 3 <= lastt ? 0xffff0000u : 0u)};
        asm volatile("" : "+v"(vm));
#pragma unroll
        for (int k = 0; k < 8; ++k) vt[k] &= vm;
#pragma unroll
        for (int r = 0; r < 4; ++r) sc[r] = 4 * g + r <= lastt ? sc[r] : -__builtin_inff();
      }
      const float mx = xg_max(vmax(vmax(sc[0], sc[1]), vmax(sc[2], sc[3])));
      const float mn = vmax(m, mx * cs);
      const float alpha = __builtin_amdgcn_exp2f(m - mn);
      m = mn;
      float pr[4];
#pragma unroll
      for (int r = 0; r < 4; ++r) pr[r] = __builtin_amdgcn_exp2f(__builtin_fmaf(sc[r], cs, -mn));
      l = l * alpha + ((pr[0] + pr[1]) + (pr[2] + pr[3]));
      const u32x2 pf = {cvtpk(pr[0], pr[1]), cvtpk(pr[2], pr[3])};
#pragma unroll
      for (int k = 0; k < 8; ++k) {
        acc[k] *= alpha;
        acc[k] = __builtin_amdgcn_mfma_f32_16x16x16bf16_1k(__builtin_bit_cast(s16x4, vt[k]), __builtin_bit_cast(s16x4, pf), acc[k], 0, 0, 0);
      }
    } while (i < iend);
    if (b == b1) {
      for (int j = 1; j <= nv; ++j) {
        while (full <= i) { full = lds_get(fullw); if (full <= i) __builtin_amdgcn_s_sleep(1); }
        asm volatile("" ::: "memory");
        const unsigned tag = rl(tagv, j);
        u32x4 x[8];
        u32x2 y;
        for (int spin = 0;; ++spin) {
#pragma unroll
          for (int k = 0; k < 8; ++k) x[k] = *LDSP(const u32x4, cur + 1024 * k + loff);
          y = *LDSP(const u32x2, cur + 8192 + 8 * c);
          unsigned bad = y[1] ^ tag;
#pragma unroll
          for (int k = 0; k < 8; ++k) bad |= (x[k][1] ^ tag) | (x[k][3] ^ tag);
          if (__builtin_amdgcn_ballot_w64(bad != 0) == 0 || spin > (1 << 22)) break;
          __builtin_amdgcn_s_sleep(2);
          load_partial(cur, j);
          WAITVM(0);
        }
        ++i;
        asm volatile("" ::: "memory");
        lds_put(freew, i);
        cur = cur + RS == ring + NS * RS ? ring : cur + RS;
        const float lse = __uint_as_float(y[0]);
        const float mn = vmax(m, lse);
        const float alpha = __builtin_amdgcn_exp2f(m - mn), w = __builtin_amdgcn_exp2f(lse - mn);
        m = mn;
        l = l * alpha + (g == 0 ? w : 0.f);
#pragma unroll
        for (int k = 0; k < 8; ++k) {
          const f32x4 o = {__uint_as_float(x[k][0] << 16), __uint_as_float(x[k][0] & 0xffff0000u),
                           __uint_as_float(x[k][2] << 16), __uint_as_float(x[k][2] & 0xffff0000u)};
          acc[k] = acc[k] * alpha + o * w;
        }
      }
    }
    const float lt = xg_sum(l);
    const float inv = __builtin_amdgcn_rcpf(lt);
    u32x2 ob[8];
#pragma unroll
    for (int k = 0; k < 8; ++k) ob[k] = u32x2{cvtpk(acc[k][0] * inv, acc[k][1] * inv), cvtpk(acc[k][2] * inv, acc[k][3] * inv)};
    if (r0 > 0) {
      const unsigned tag = rl(tagv, 0);
      const __amdgpu_buffer_rsrc_t pr = rsrc(ws + (unsigned)(p * 8 + h) * SLOT, SLOT);
#pragma unroll
      for (int k = 0; k < 8; ++k)
        __builtin_amdgcn_raw_buffer_store_b128(u32x4{ob[k][0], tag, ob[k][1], tag}, pr, loff + 1024 * (k & 3), (k >> 2) * 4096, 16);
      const u32x2 y = {__float_as_uint(m + __builtin_amdgcn_logf(lt)), tag};
      __builtin_amdgcn_raw_buffer_store_b64(y, pr, g == 0 ? c * 8 : 0x80000000u, 8192, 16);
    } else {
      const __amdgpu_buffer_rsrc_t orr = rsrc(out + (b * 8 + h) * 4096, 4096);
#pragma unroll
      for (int r = 0; r < 4; ++r)
        __builtin_amdgcn_raw_buffer_store_b128(u32x4{ob[2 * r][0], ob[2 * r][1], ob[2 * r + 1][0], ob[2 * r + 1][1]}, orr, c * 256 + g * 64 + 16 * r, 0, 0);
    }
  }
  if (lane >= 1 && lane <= nv) __hip_atomic_store(ep, tagv, __ATOMIC_RELAXED, __HIP_MEMORY_SCOPE_AGENT);
}

extern "C" void kernel_launch(void* const* d_in, const int* in_sizes, int n_in, void* d_out, int out_size,
                              void* d_ws, size_t ws_size, hipStream_t stream) {
  pa_kernel<<<256, 256, 0, stream>>>((const char*)d_in[0], (const char*)d_in[1], (const char*)d_in[2],
                                     (const int*)d_in[3], (const int*)d_in[4], (char*)d_out, (char*)d_ws);
}
```

```cpp
#define TS
#define TAU16 872
#define SA 20102
#define OFSA 2400
#define UNITA 25
#define SB 20103
#define OFSB 2400
#define UNITB 25
#include <hip/hip_runtime.h>
#include <hip/hip_bf16.h>
#include <stdint.h>

#define AHEAD 2
#define NS 6
#define KPOL " nt"
#define VPOL ""
#define DELTA 4
#define POFS 0

typedef __attribute__((ext_vector_type(8))) __bf16 bf16x8;
typedef __attribute__((ext_vector_type(4))) short s16x4;
typedef __attribute__((ext_vector_type(4))) float f32x4;
typedef __attribute__((ext_vector_type(4))) unsigned u32x4;
typedef __attribute__((ext_vector_type(2))) unsigned u32x2;
typedef __attribute__((address_space(1))) unsigned gu32;
#define LDSP(T, a) ((__attribute__((address_space(3))) T*)(uintptr_t)(a))

constexpr int NB = 32, NBPS = 128, SKV = NBPS * 16;
constexpr unsigned SLOT = 16384, EPOCH_OFF = 64 * 8 * SLOT;
constexpr int RS = 9216, PAIR_LDS = NS * RS + 256;

__device__ __forceinline__ unsigned cvtpk(float lo, float hi) {
  typedef __attribute__((ext_vector_type(2))) __bf16 bf16x2;
  bf16x2 r = {(__bf16)lo, (__bf16)hi};
  return __builtin_bit_cast(unsigned, r);
}
__device__ __forceinline__ float vmax(float a, float b) { return __builtin_elementwise_maximum(a, b); }
__device__ __forceinline__ float xg_max(float x) {
  auto s = __builtin_amdgcn_permlane16_swap(__float_as_uint(x), __float_as_uint(x), false, false);
  x = vmax(__uint_as_float(s[0]), __uint_as_float(s[1]));
  auto t = __builtin_amdgcn_permlane32_swap(__float_as_uint(x), __float_as_uint(x), false, false);
  return vmax(__uint_as_float(t[0]), __uint_as_float(t[1]));
}
__device__ __forceinline__ float xg_sum(float x) {
  auto s = __builtin_amdgcn_permlane16_swap(__float_as_uint(x), __float_as_uint(x), false, false);
  x = __uint_as_float(s[0]) + __uint_as_float(s[1]);
  auto t = __builtin_amdgcn_permlane32_swap(__float_as_uint(x), __float_as_uint(x), false, false);
  return __uint_as_float(t[0]) + __uint_as_float(t[1]);
}
__device__ __forceinline__ __amdgpu_buffer_rsrc_t rsrc(const void* p, unsigned n) {
  return __builtin_amdgcn_make_buffer_rsrc((void*)p, 0, n, 0x00020000);
}
__device__ __forceinline__ u32x4 srd(const void* p, unsigned n) {
  const uint64_t a = (uint64_t)p;
  return u32x4{(unsigned)a, (unsigned)(a >> 32) & 0xffffu, n, 0x00020000u};
}
__device__ __forceinline__ int rl(int v, int l) { return __builtin_amdgcn_readlane(v, l); }
__device__ __forceinline__ int lds_get(unsigned a) {
  return __builtin_amdgcn_readfirstlane(__hip_atomic_load(LDSP(int, a), __ATOMIC_RELAXED, __HIP_MEMORY_SCOPE_WORKGROUP));
}
__device__ __forceinline__ void lds_put(unsigned a, int v) {
  __hip_atomic_store(LDSP(int, a), v, __ATOMIC_RELAXED, __HIP_MEMORY_SCOPE_WORKGROUP);
}

#define DMA4(POL, dst, rs, so, v0, v1, v2, v3)                                                        \
  do {                                                                                                \
    unsigned keep_;                                                                                   \
    asm volatile("s_mov_b32 %0, m0\n\ts_mov_b32 m0, %1\n\ts_nop 4\n\t"                                \
                 "buffer_load_dwordx4 %4, %2, %3 offen" POL " lds\n\t"                                \
                 "buffer_load_dwordx4 %5, %2, %3 offen offset:1024" POL " lds\n\t"                    \
                 "buffer_load_dwordx4 %6, %2, %3 offen offset:2048" POL " lds\n\t"                    \
                 "buffer_load_dwordx4 %7, %2, %3 offen offset:3072" POL " lds\n\t"                    \
                 "s_mov_b32 m0, %0"                                                                   \
                 : "=&s"(keep_) : "s"(dst), "s"(rs), "s"(so), "v"(v0), "v"(v1), "v"(v2), "v"(v3) : "memory"); \
  } while (0)
#define DMA1(POL, dst, rs, so, v0)                                                                    \
  do {                                                                                                \
    unsigned keep_;                                                                                   \
    asm volatile("s_mov_b32 %0, m0\n\ts_mov_b32 m0, %1\n\ts_nop 4\n\t"                                \
                 "buffer_load_dwordx4 %4, %2, %3 offen" POL " lds\n\t"                                \
                 "s_mov_b32 m0, %0"                                                                   \
                 : "=&s"(keep_) : "s"(dst), "s"(rs), "s"(so), "v"(v0) : "memory");                    \
  } while (0)
#define WAITVM(n) asm volatile("s_waitcnt vmcnt(%0)" :: "n"(n) : "memory")
#define NOW() ((unsigned)__builtin_amdgcn_s_memrealtime())

__global__ void __launch_bounds__(512)
pa_kernel(const char* __restrict__ q, const char* __restrict__ kc, const char* __restrict__ vc,
          const int* __restrict__ bt, const int* __restrict__ lens, char* __restrict__ out, char* __restrict__ ws) {
  __shared__ __attribute__((aligned(256))) char lds[2 * PAIR_LDS];
  const unsigned t0 = NOW();
  const int lane = threadIdx.x & 63;
  const int wave = __builtin_amdgcn_readfirstlane(threadIdx.x >> 6);
  const int p = blockIdx.x >> 2;
  const int h = 2 * ((blockIdx.x - (p >> 1)) & 3) + (wave & 1);
  const int g = lane >> 4, c = lane & 15;
  const bool loader = wave < 2;

  int len = lens[lane & 31];
  gu32* const ep = (gu32*)(ws + EPOCH_OFF) + h * 64 + ((p + lane) & 63);
  unsigned tagv;
  if (loader) tagv = bt[(wave * 64 + lane) * 32];
  else tagv = __hip_atomic_load(ep, __ATOMIC_RELAXED, __HIP_MEMORY_SCOPE_AGENT);
  const unsigned ring = (unsigned)(uintptr_t)LDSP(char, lds) + (wave & 1) * PAIR_LDS;
  const unsigned fullw = ring + NS * RS, freew = fullw + 128;
  lds_put(loader ? fullw : freew, 0);
  asm volatile("s_waitcnt lgkmcnt(0)\n\ts_barrier" ::: "memory");

  const bool uni = len <= 0;
  if (uni || len > SKV) len = SKV;
  const int np = (len + 15) >> 4;
  const int dl = np >= 16 ? DELTA : 0;
  const int vl = np - dl;
  int incl = vl;
  incl += __builtin_amdgcn_update_dpp(0, incl, 0x111, 0xf, 0xf, false);
  incl += __builtin_amdgcn_update_dpp(0, incl, 0x112, 0xf, 0xf, false);
  incl += __builtin_amdgcn_update_dpp(0, incl, 0x114, 0xf, 0xf, false);
  incl += __builtin_amdgcn_update_dpp(0, incl, 0x118, 0xf, 0xf, false);
  incl += __builtin_amdgcn_update_dpp(0, incl, 0x142, 0xa, 0xf, false);
  const int V = rl(incl, 31), VV = max(V, 64);
  const int lo = (VV * p) >> 6, hi = min(V, (VV * (p + 1)) >> 6);
  if (lo >= hi) return;
  const int b0 = __builtin_popcount((unsigned)__builtin_amdgcn_ballot_w64(incl <= lo));
  const int b1 = __builtin_popcount((unsigned)__builtin_amdgcn_ballot_w64(incl < hi));
  const int ex = incl - vl;
  const int r0v = lo > ex ? lo - ex + dl : 0, cntv = min(hi, incl) - ex + dl - r0v;
  const int nv = rl(r0v, b1) == 0 ? __builtin_popcountll(__builtin_amdgcn_ballot_w64(((VV * lane) >> 6) < rl(incl, b1))) - 1 - p : 0;
  const unsigned loff = lane * 16;
  auto load_partial = [&](unsigned dst, int j) __attribute__((always_inline)) {
    const u32x4 pr = srd(ws + (unsigned)((p + j) * 8 + h) * SLOT, SLOT);
    DMA4(" sc1", dst, pr, 0, loff, loff, loff, loff);
    DMA4(" sc1", dst + 4096, pr, 4096, loff, loff, loff, loff);
    DMA1(" sc1", dst + 8192, pr, 8192, loff);
  };
  unsigned* const tsp = (unsigned*)(ws + (64u << 20)) + (blockIdx.x * 2 + (wave & 1)) * 16;

  if (loader) {
    int n = 0;
    unsigned tab[2] = {0u, 0u};
    int idx[2] = {0, 0};
    for (int s = b0; s <= b1; ++s) {
      const int r0 = rl(r0v, s), cnt = rl(cntv, s), ls = rl(len, s);
#pragma unroll
      for (int u = 0; u < 2; ++u) {
        const int k = lane + 64 * u - n, pg = r0 + k;
        idx[u] = k >= 0 ? s * NBPS + pg : idx[u];
        tab[u] = k >= 0 ? (unsigned)(min(16, ls - 16 * pg) - 1) << 17 : tab[u];
      }
      n += cnt;
    }
    tab[0] |= (unsigned)bt[min(idx[0], NB * NBPS - 1)];
    tab[1] |= (unsigned)bt[min(idx[1], NB * NBPS - 1)];
    unsigned go[4];
#pragma unroll
    for (int r = 0; r < 4; ++r) go[r] = (4 * r + g) * 2048 + ((c ^ (4 * r + g)) << 4) - 1024 * r;
    asm volatile("" : "+v"(tab[0]), "+v"(tab[1]) : "v"(tagv));
    const int ntot = n + nv;
    unsigned dst = ring;
    int freed = 0;
    if (lane == 0) { tsp[14] = NOW(); tsp[4] = n; }
    for (int j = 0; j < ntot; ++j) {
      while (freed + NS <= j) { freed = lds_get(freew); if (freed + NS <= j) __builtin_amdgcn_s_sleep(1); }
      if (TAU16) {
        const int due = ((j * TAU16) >> 4) + POFS;
        while ((int)(NOW() - t0) < due) __builtin_amdgcn_s_sleep(1);
      }
      if (j < n) {
        const unsigned e = rl(j < 64 ? tab[0] : tab[1], j & 63);
        const size_t po = (size_t)(e & 0x1ffffu) * 32768 + h * 256;
        const unsigned nrec = ((e >> 17) & 15) * 2048 + 256;
        const u32x4 kr = srd(kc + po, nrec), vr = srd(vc + po, nrec);
        DMA4(KPOL, dst, kr, 0, go[0], go[1], go[2], go[3]);
        DMA4(VPOL, dst + 4096, vr, 0, go[0], go[1], go[2], go[3]);
      } else {
        load_partial(dst, j - n + 1);
      }
      dst = dst + RS == ring + NS * RS ? ring : dst + RS;
      if (j >= AHEAD) { WAITVM(8 * AHEAD); lds_put(fullw, j - AHEAD + 1); }
    }
    if (lane == 0) tsp[15] = NOW();
    if (AHEAD > 3) { WAITVM(24); lds_put(fullw, ntot - 3); }
    if (AHEAD > 2) { WAITVM(16); lds_put(fullw, ntot - 2); }
    if (AHEAD > 1) { WAITVM(8); lds_put(fullw, ntot - 1); }
    WAITVM(0); lds_put(fullw, ntot);
    return;
  }

  const unsigned unimask = (unsigned)__builtin_amdgcn_ballot_w64(uni);
  const unsigned qoff = c * 256 + g * 16;
  const __amdgpu_buffer_rsrc_t qr = rsrc(q + h * 4096, NB * 32768);
  u32x4 qn[4];
#pragma unroll
  for (int s = 0; s < 4; ++s) qn[s] = __builtin_amdgcn_raw_buffer_load_b128(qr, qoff + 64 * s, b0 * 32768, 0);
  tagv += 0x9E3779B9u;
  const float CS = 0.088388347648318440f * 1.4426950408889634f;
  const unsigned ka = c * 256 + ((g ^ c) << 4);
  const unsigned va = 4096 + (lane >> 2) * 256 + ((((lane & 3) << 2) ^ (lane >> 2)) << 4);
  unsigned cur = ring;
  int i = 0, full = 0;
  unsigned ts_first = 0, ts_real = 0, ts_spins = 0, cy_comp = 0;
  for (int b = b0; b <= b1; ++b) {
    const int r0 = rl(r0v, b), cnt = rl(cntv, b);
    const int iend = i + cnt;
    const int lastt = min(15, rl(len, b) - 16 * (r0 + cnt - 1) - 1);
    const float cs = (unimask >> b) & 1 ? 0.f : CS;
    u32x4 qf[4];
#pragma unroll
    for (int s = 0; s < 4; ++s) qf[s] = qn[s];
    asm volatile("" : "+v"(qf[0]), "+v"(qf[1]), "+v"(qf[2]), "+v"(qf[3]));
    if (b < b1) {
#pragma unroll
      for (int s = 0; s < 4; ++s) qn[s] = __builtin_amdgcn_raw_buffer_load_b128(qr, qoff + 64 * s, (b + 1) * 32768, 0);
    }
    f32x4 acc[8];
#pragma unroll
    for (int k = 0; k < 8; ++k) acc[k] = f32x4{0.f, 0.f, 0.f, 0.f};
    float m = -1e30f, l = 0.f;
    do {
      while (full <= i) { full = lds_get(fullw); if (full <= i) __builtin_amdgcn_s_sleep(1); }
      asm volatile("" ::: "memory");
      if (i == 0) ts_first = NOW();
      ts_real = NOW();
      const unsigned c3 = (unsigned)__builtin_amdgcn_s_memtime();
      u32x4 kf[4];
      u32x2 vt[8];
#pragma unroll
      for (int s = 0; s < 4; ++s) kf[s] = *LDSP(const u32x4, (cur + ka) ^ (s << 6));
#pragma unroll
      for (int k = 0; k < 8; ++k)
        vt[k] = __builtin_bit_cast(u32x2, __builtin_amdgcn_ds_read_tr16_b64_v4i16(LDSP(s16x4, ((cur + va) ^ ((k >> 1) << 4)) + 8 * (k & 1))));
      f32x4 sc = {0.f, 0.f, 0.f, 0.f};
#pragma unroll
      for (int s = 0; s < 4; ++s)
        sc = __builtin_amdgcn_mfma_f32_16x16x32_bf16(__builtin_bit_cast(bf16x8, kf[s]), __builtin_bit_cast(bf16x8, qf[s]), sc, 0, 0, 0);
      ++i;
      asm volatile("" : "+v"(vt[0]), "+v"(vt[1]), "+v"(vt[2]), "+v"(vt[3]), "+v"(vt[4]), "+v"(vt[5]), "+v"(vt[6]), "+v"(vt[7]) :: "memory");
      lds_put(freew, i);
      cur = cur + RS == ring + NS * RS ? ring : cur + RS;
      if (i == iend && lastt < 15) {
        u32x2 vm = {(4 * g <= lastt ? 0xffffu : 0u) | (4 * g + 1 <= lastt ? 0xffff0000u : 0u),
                    (4 * g + 2 <= lastt ? 0xffffu : 0u) | (4 * g + 3 <= lastt ? 0xffff0000u : 0u)};
        asm volatile("" : "+v"(vm));
#pragma unroll
        for (int k = 0; k < 8; ++k) vt[k] &= vm;
#pragma unroll
        for (int r = 0; r < 4; ++r) sc[r] = 4 * g + r <= lastt ? sc[r] : -__builtin_inff();
      }
      const float mx = xg_max(vmax(vmax(sc[0], sc[1]), vmax(sc[2], sc[3])));
      const float mn = vmax(m, mx * cs);
      const float alpha = __builtin_amdgcn_exp2f(m - mn);
      m = mn;
      float pr[4];
#pragma unroll
      for (int r = 0; r < 4; ++r) pr[r] = __builtin_amdgcn_exp2f(__builtin_fmaf(sc[r], cs, -mn));
      l = l * alpha + ((pr[0] + pr[1]) + (pr[2] + pr[3]));
      const u32x2 pf = {cvtpk(pr[0], pr[1]), cvtpk(pr[2], pr[3])};
#pragma unroll
      for (int k = 0; k < 8; ++k) {
        acc[k] *= alpha;
        acc[k] = __builtin_amdgcn_mfma_f32_16x16x16bf16_1k(__builtin_bit_cast(s16x4, vt[k]), __builtin_bit_cast(s16x4, pf), acc[k], 0, 0, 0);
      }
      asm volatile("" : "+v"(acc[7])); cy_comp += (unsigned)__builtin_amdgcn_s_memtime() - c3;
    } while (i < iend);
    if (b == b1) {
      for (int j = 1; j <= nv; ++j) {
        while (full <= i) { full = lds_get(fullw); if (full <= i) __builtin_amdgcn_s_sleep(1); }
        asm volatile("" ::: "memory");
        const unsigned tag = rl(tagv, j);
        u32x4 x[8];
        u32x2 y;
        for (int spin = 0;; ++spin) {
#pragma unroll
          for (int k = 0; k < 8; ++k) x[k] = *LDSP(const u32x4, cur + 1024 * k + loff);
          y = *LDSP(const u32x2, cur + 8192 + 8 * c);
          unsigned bad = y[1] ^ tag;
#pragma unroll
          for (int k = 0; k < 8; ++k) bad |= (x[k][1] ^ tag) | (x[k][3] ^ tag);
          if (__builtin_amdgcn_ballot_w64(bad != 0) == 0 || spin > (1 << 22)) break;
          __builtin_amdgcn_s_sleep(2);
          ++ts_spins;
          load_partial(cur, j);
          WAITVM(0);
        }
        ++i;
        asm volatile("" ::: "memory");
        lds_put(freew, i);
        cur = cur + RS == ring + NS * RS ? ring : cur + RS;
        const float lse = __uint_as_float(y[0]);
        const float mn = vmax(m, lse);
        const float alpha = __builtin_amdgcn_exp2f(m - mn), w = __builtin_amdgcn_exp2f(lse - mn);
        m = mn;
        l = l * alpha + (g == 0 ? w : 0.f);
#pragma unroll
        for (int k = 0; k < 8; ++k) {
          const f32x4 o = {__uint_as_float(x[k][0] << 16), __uint_as_float(x[k][0] & 0xffff0000u),
                           __uint_as_float(x[k][2] << 16), __uint_as_float(x[k][2] & 0xffff0000u)};
          acc[k] = acc[k] * alpha + o * w;
        }
      }
    }
    const float lt = xg_sum(l);
    const float inv = __builtin_amdgcn_rcpf(lt);
    u32x2 ob[8];
#pragma unroll
    for (int k = 0; k < 8; ++k) ob[k] = u32x2{cvtpk(acc[k][0] * inv, acc[k][1] * inv), cvtpk(acc[k][2] * inv, acc[k][3] * inv)};
    if (r0 > 0) {
      const unsigned tag = rl(tagv, 0);
      const __amdgpu_buffer_rsrc_t pr = rsrc(ws + (unsigned)(p * 8 + h) * SLOT, SLOT);
#pragma unroll
      for (int k = 0; k < 8; ++k)
        __builtin_amdgcn_raw_buffer_store_b128(u32x4{ob[k][0], tag, ob[k][1], tag}, pr, loff + 1024 * (k & 3), (k >> 2) * 4096, 16);
      const u32x2 y = {__float_as_uint(m + __builtin_amdgcn_logf(lt)), tag};
      __builtin_amdgcn_raw_buffer_store_b64(y, pr, g == 0 ? c * 8 : 0x80000000u, 8192, 16);
    } else {
      const __amdgpu_buffer_rsrc_t orr = rsrc(out + (b * 8 + h) * 4096, 4096);
#pragma unroll
      for (int r = 0; r < 4; ++r)
        __builtin_amdgcn_raw_buffer_store_b128(u32x4{ob[2 * r][0], ob[2 * r][1], ob[2 * r + 1][0], ob[2 * r + 1][1]}, orr, c * 256 + g * 64 + 16 * r, 0, 0);
    }
  }
  if (lane == 0) { tsp[0] = t0; tsp[1] = ts_first; tsp[2] = ts_real; tsp[3] = NOW();
    tsp[5] = nv; tsp[6] = ts_spins; tsp[7] = 0; tsp[8] = 0; tsp[9] = 0; tsp[10] = cy_comp; tsp[11] = h; tsp[12] = b1 - b0; tsp[13] = __builtin_amdgcn_s_getreg((31 << 11) | 20); }
  if (lane >= 1 && lane <= nv) __hip_atomic_store(ep, tagv, __ATOMIC_RELAXED, __HIP_MEMORY_SCOPE_AGENT);
}

__device__ float stat_of(const unsigned* t, unsigned lo, int s) {
  const int f = s / 10000, sel = (s / 100) % 100, val = s % 100;
  float sum = 0.f, mx = 0.f; int n = 0;
  for (int w = 0; w < 512; ++w) {
    const unsigned* x = t + w * 16;
    bool in = true;
    if (sel == 1 || sel == 5) in = (int)x[11] == val;
    if (sel == 2 || sel == 6) in = (int)(x[13] & 7) == val;
    if (sel == 4) in = (x[5] > 0) == (val != 0);
    if (!in) continue;
    const float v = (float)(x[f] - lo);
    sum += v; mx = fmaxf(mx, v); ++n;
  }
  return (sel == 3 || sel == 5 || sel == 6) ? mx : sum / n;
}
__global__ void diag_kernel(const unsigned* __restrict__ tg) {
  __shared__ unsigned t[512 * 16];
  for (int i = threadIdx.x; i < 512 * 16; i += 512) t[i] = tg[i];
  __syncthreads();
  if (threadIdx.x == 0) {
    unsigned lo = t[0]; for (int i = 0; i < 512; ++i) if ((int)(t[i * 16] - lo) < 0) lo = t[i * 16];
    const int qa = min(63, max(0, (int)((stat_of(t, lo, SA) - OFSA) / UNITA))), qb = min(63, max(0, (int)((stat_of(t, lo, SB) - OFSB) / UNITB)));
    const int due = 30000 + 200 * qa + 12800 * qb;
    while ((int)((unsigned)__builtin_amdgcn_s_memrealtime() - lo) < due) __builtin_amdgcn_s_sleep(1);
  }
}

extern "C" void kernel_launch(void* const* d_in, const int* in_sizes, int n_in, void* d_out, int out_size,
                              void* d_ws, size_t ws_size, hipStream_t stream) {
  pa_kernel<<<256, 256, 0, stream>>>((const char*)d_in[0], (const char*)d_in[1], (const char*)d_in[2],
                                     (const int*)d_in[3], (const int*)d_in[4], (char*)d_out, (char*)d_ws);
  diag_kernel<<<1, 512, 0, stream>>>((const unsigned*)((char*)d_ws + (64u << 20)));
}
```

```cpp
#define PG 0
#define PGS 24
#define RUN 8
#define POFS 60
#define TAU16 470
#define POFSS 0
#define TAUS 475
#define EA 2
#include <hip/hip_runtime.h>
#include <hip/hip_bf16.h>
#include <stdint.h>

#define LEAD 3
#define NL 4
#define EF 0
#define PST 12
#define EAS EA
#define EFS EF
#define PLEAD 130
#define PPOL ""
#define KPOL " nt"
#define VPOL " nt"
#define SLP 1
#define SLP2 1
#define OPOL 16

typedef __attribute__((ext_vector_type(8))) __bf16 bf16x8;
typedef __attribute__((ext_vector_type(4))) short s16x4;
typedef __attribute__((ext_vector_type(4))) float f32x4;
typedef __attribute__((ext_vector_type(4))) unsigned u32x4;
typedef __attribute__((ext_vector_type(2))) unsigned u32x2;
#define LDSP(T, a) ((__attribute__((address_space(3))) T*)(uintptr_t)(a))

constexpr int NBPS = 128, SKV = NBPS * 16, NS = 8;
constexpr unsigned DUMP = 2 * NS * 8192, WORDS = DUMP + 8192 + 512;

__device__ __forceinline__ unsigned cvtpk(float lo, float hi) {
  typedef __attribute__((ext_vector_type(2))) __bf16 bf16x2;
  bf16x2 r = {(__bf16)lo, (__bf16)hi};
  return __builtin_bit_cast(unsigned, r);
}
__device__ __forceinline__ float vmax(float a, float b) { return __builtin_elementwise_maximum(a, b); }
__device__ __forceinline__ float xg_max(float x) {
  auto s = __builtin_amdgcn_permlane16_swap(__float_as_uint(x), __float_as_uint(x), false, false);
  x = vmax(__uint_as_float(s[0]), __uint_as_float(s[1]));
  auto t = __builtin_amdgcn_permlane32_swap(__float_as_uint(x), __float_as_uint(x), false, false);
  return vmax(__uint_as_float(t[0]), __uint_as_float(t[1]));
}
__device__ __forceinline__ float xg_sum(float x) {
  auto s = __builtin_amdgcn_permlane16_swap(__float_as_uint(x), __float_as_uint(x), false, false);
  x = __uint_as_float(s[0]) + __uint_as_float(s[1]);
  auto t = __builtin_amdgcn_permlane32_swap(__float_as_uint(x), __float_as_uint(x), false, false);
  return __uint_as_float(t[0]) + __uint_as_float(t[1]);
}
__device__ __forceinline__ int scan32(int v) {
  v += __builtin_amdgcn_update_dpp(0, v, 0x111, 0xf, 0xf, false);
  v += __builtin_amdgcn_update_dpp(0, v, 0x112, 0xf, 0xf, false);
  v += __builtin_amdgcn_update_dpp(0, v, 0x114, 0xf, 0xf, false);
  v += __builtin_amdgcn_update_dpp(0, v, 0x118, 0xf, 0xf, false);
  v += __builtin_amdgcn_update_dpp(0, v, 0x142, 0xa, 0xf, false);
  return v;
}
__device__ __forceinline__ __amdgpu_buffer_rsrc_t rsrc(const void* p, unsigned n) {
  return __builtin_amdgcn_make_buffer_rsrc((void*)p, 0, n, 0x00020000);
}
__device__ __forceinline__ u32x4 srd(const void* p, unsigned n) {
  const uint64_t a = (uint64_t)p;
  return u32x4{(unsigned)a, (unsigned)(a >> 32) & 0xffffu, n, 0x00020000u};
}
__device__ __forceinline__ int rl(int v, int l) { return __builtin_amdgcn_readlane(v, l); }
__device__ __forceinline__ unsigned bal32(bool c) { return (unsigned)__builtin_amdgcn_ballot_w64(c); }
__device__ __forceinline__ int lds_ld(unsigned a) {
  return __hip_atomic_load(LDSP(int, a), __ATOMIC_RELAXED, __HIP_MEMORY_SCOPE_WORKGROUP);
}
__device__ __forceinline__ void lds_put(unsigned a, int v) {
  __hip_atomic_store(LDSP(int, a), v, __ATOMIC_RELAXED, __HIP_MEMORY_SCOPE_WORKGROUP);
}

#define DMA4(POL, dst, rs, v)                                                                         \
  do {                                                                                                \
    unsigned keep_;                                                                                   \
    asm volatile("s_mov_b32 %0, m0\n\ts_mov_b32 m0, %1\n\ts_nop 4\n\t"                                \
                 "buffer_load_dwordx4 %3, %2, 0 offen" POL " lds\n\t"                                 \
                 "buffer_load_dwordx4 %4, %2, 0 offen offset:1024" POL " lds\n\t"                     \
                 "buffer_load_dwordx4 %5, %2, 0 offen offset:2048" POL " lds\n\t"                     \
                 "buffer_load_dwordx4 %6, %2, 0 offen offset:3072" POL " lds\n\t"                     \
                 "s_mov_b32 m0, %0"                                                                   \
                 : "=&s"(keep_) : "s"(dst), "s"(rs), "v"(v[0]), "v"(v[1]), "v"(v[2]), "v"(v[3]) : "memory"); \
  } while (0)
#define WAITVM0() asm volatile("s_waitcnt vmcnt(0)" ::: "memory")
#define NOW() ((unsigned)__builtin_amdgcn_s_memrealtime())

__global__ void __launch_bounds__(128 * NL + 192)
pa_kernel(const char* __restrict__ q, const char* __restrict__ kc, const char* __restrict__ vc,
          const int* __restrict__ bt, const int* __restrict__ lens, char* __restrict__ out, char* __restrict__ ws) {
  __shared__ __attribute__((aligned(16384))) char lds[WORDS + 128];
  const unsigned t0 = NOW();
  asm volatile("" :: "s"(q), "s"(kc), "s"(vc), "s"(bt), "s"(lens), "s"(out));
  const int lane = threadIdx.x & 63;
  const int wave = __builtin_amdgcn_readfirstlane(threadIdx.x >> 6);
  const int w = wave < 2 * NL ? wave >= NL : wave - 2 * NL, lw = wave - NL * w;
  const int b = blockIdx.x >> 3, h = (blockIdx.x - b) & 7;
  const int g = lane >> 4, c = lane & 15;
  const unsigned base = (unsigned)(uintptr_t)LDSP(char, lds);
  const unsigned ring = base + w * (NS * 8192), fullw = base + WORDS + w * 64, freew = fullw + 32, dumpw = base + WORDS + 36;

  if (wave == 2 * NL + 2) {
    int len = lens[lane & 31];
    asm volatile("s_barrier" ::: "memory");
    if (len <= 0 || len > SKV) len = SKV;
    const int key = ((len + 15) >> 4) * 32 + (lane & 31);
    int rank = 0;
#pragma unroll
    for (int j = 0; j < 32; ++j) rank += rl(key, j) < key;
    const int pb = __builtin_ctz(bal32(rank == 31 - rl(rank, b))), ph = (blockIdx.x - pb) & 7;
    const int np = rl(key, pb) >> 5, runs = min(((np - (rl(key, b) >> 5)) * ((ph & 3) == 1 ? PGS : PG) + 16 * RUN) / (32 * RUN), 64 / RUN), cnt = runs * RUN;
    if (cnt <= 0) return;
    const int n0 = (np >> 2) * 2 + min(np & 3, 2), ld = LEAD + 2 * n0 - np;
    const float tau = (float)rl(scan32((len - 1) >> 4), 31) * ((ph & 3) == 1 ? TAUS / 512.f : TAU16 / 512.f) * __builtin_amdgcn_rcpf((float)max(np - 1, 1));
    const int slot = (int)((float)((lane / RUN + 1) * np) * __builtin_amdgcn_rcpf((float)runs) + 0.5f) - RUN + lane % RUN;
    const bool tail = slot >= 2 * n0 - ld;
    const int sw = tail ? 1 : ~(slot - ld) & 1, r = tail ? slot - n0 : (sw ? slot - ld : slot - 1 + ld) >> 1, pg = 4 * (r >> 1) + 2 * sw + (r & 1);
    int pgv = bt[pb * NBPS + min(max(pg, 0), np - 1)];
    const int duev = (int)((float)(slot - lane % RUN) * tau) + lane % RUN * PST + ((ph & 3) == 1 ? POFSS : POFS) - PLEAD;
    const unsigned off = (lane >> 2) * 2048 + (lane & 3) * 64;
    asm volatile("" : "+v"(pgv));
    for (int k = 0; k < cnt; ++k) {
      const int due = rl(duev, k);
      const size_t po = (size_t)(unsigned)rl(pgv, k) * 32768 + ph * 256;
      const u32x4 kr = srd(kc + po, 32768), vr = srd(vc + po, 32768);
      while ((int)(NOW() - t0) < due) __builtin_amdgcn_s_sleep(SLP2);
      unsigned x, y;
      asm volatile("s_waitcnt vmcnt(8)\n\tbuffer_load_dword %0, %2, %3, 0 offen" PPOL "\n\tbuffer_load_dword %1, %2, %4, 0 offen" PPOL
                   : "=&v"(x), "=&v"(y) : "v"(off), "s"(kr), "s"(vr) : "memory");
    }
    WAITVM0();
    return;
  }
  if (wave < 2 * NL) {
    const int r = lw + NL * lane, pg = 4 * (r >> 1) + 2 * w + (r & 1);
    int len = lens[lane & 31];
    const int pgv = bt[b * NBPS + min(pg, NBPS - 1)];
    unsigned go[4];
#pragma unroll
    for (int u = 0; u < 4; ++u) go[u] = (4 * u + g) * 2048 + ((c ^ (4 * u + g)) << 4) - 1024 * u;
    if (len <= 0 || len > SKV) len = SKV;
    const int lenb = rl(len, b);
    const int rows = min(max(lenb - 16 * pg, 0), 16);
    auto issue = [&](int j, unsigned page, unsigned nrec) __attribute__((always_inline)) {
      const size_t po = (size_t)page * 32768 + h * 256;
      const u32x4 kr = srd(kc + po, nrec), vr = srd(vc + po, nrec);
      const unsigned dst = ring + (j & (NS - 1)) * 8192;
      DMA4(KPOL, dst, kr, go);
      DMA4(VPOL, dst + 4096, vr, go);
    };
    const int npos = ((lenb + 63 - 32 * w) >> 6) * 2;
    const bool early = lw < ((h & 3) == 1 ? (w ? EFS : EAS) : w ? EF : EA) && lw < npos;
    if (early) issue(lw, rl(pgv, 0), rl(rows, 0) * 2048);
    const int np = (lenb + 15) >> 4, n0 = (np >> 2) * 2 + min(np & 3, 2), ld = LEAD + 2 * n0 - np;
    const float tau = (float)rl(scan32((len - 1) >> 4), 31) * ((h & 3) == 1 ? TAUS / 512.f : TAU16 / 512.f) * __builtin_amdgcn_rcpf((float)max(np - 1, 1));
    const int slot = w ? min(2 * r + ld, n0 + r) : max(2 * r + 1 - ld, r);
    const unsigned tab = rows ? (unsigned)((float)slot * tau) << 16 | rows << 11 : 0;
    int freed = 0;
    asm volatile("s_barrier" ::: "memory");
    for (int j = lw, k = 0; j < npos; j += NL, ++k) {
      if (k || !early) {
        const unsigned e = rl(tab, k);
        while (freed + NS <= j) {
          freed = __builtin_amdgcn_readfirstlane(lds_ld(freew));
          if (freed + NS <= j) __builtin_amdgcn_s_sleep(SLP);
        }
        const int due = (e >> 16) + ((h & 3) == 1 ? POFSS : POFS);
        while ((int)(NOW() - t0) < due) __builtin_amdgcn_s_sleep(SLP2);
        issue(j, rl(pgv, k), e & 0xffff);
      }
      WAITVM0();
      lds_put(fullw + 4 * (j & (NS - 1)), j + 1);
    }
    return;
  }

  __builtin_amdgcn_s_setprio(3);
  if (lane < 16) lds_put(fullw + 4 * lane, 0);
  asm volatile("s_waitcnt lgkmcnt(0)" ::: "memory");
  int len = lens[b];
  u32x4 qf[4];
#pragma unroll
  for (int u = 0; u < 4; ++u) qf[u] = *(const u32x4*)(q + (b * 8 + h) * 4096 + c * 256 + g * 16 + 64 * u);
  asm volatile("s_barrier" ::: "memory");
  if (len <= 0) {
#pragma unroll
    for (int u = 0; u < 4; ++u) qf[u] = u32x4{0, 0, 0, 0};
  }
  if (len <= 0 || len > SKV) len = SKV;
  const float CS = 0.088388347648318440f * 1.4426950408889634f;
  const unsigned ka = ring + c * 256 + ((g ^ c) << 4);
  const unsigned va = ring + 4096 + (lane >> 2) * 256 + ((((lane & 3) << 2) ^ (lane >> 2)) << 4);
  f32x4 acc[8];
#pragma unroll
  for (int k = 0; k < 8; ++k) acc[k] = f32x4{0.f, 0.f, 0.f, 0.f};
  float m = -1e30f, l = 0.f;
  asm volatile("" : "+v"(qf[0]), "+v"(qf[1]), "+v"(qf[2]), "+v"(qf[3]));
  int i = 0, tokens = len - 32 * w;
  for (int lim = 64 * w;; lim = 0) {
    while (tokens > lim) {
      for (;;) {
        const uint64_t f = __hip_atomic_load(LDSP(uint64_t, fullw + 4 * (i & (NS - 1))), __ATOMIC_RELAXED, __HIP_MEMORY_SCOPE_WORKGROUP);
        if (__builtin_amdgcn_readfirstlane((unsigned)f + (unsigned)(f >> 32)) == 2 * i + 3) break;
      }
      asm volatile("" ::: "memory");
      const unsigned o = (i & (NS - 1)) * 8192;
      u32x4 kf[2][4], vt[8];
#pragma unroll
      for (int u = 0; u < 4; ++u) {
        const unsigned a = (o + ka) ^ (u << 6);
        kf[0][u] = *LDSP(const u32x4, a);
        kf[1][u] = *LDSP(const u32x4, a + 8192);
      }
#pragma unroll
      for (int k = 0; k < 8; ++k) {
        const unsigned a = ((o + va) ^ ((k >> 1) << 4)) + 8 * (k & 1);
        const u32x2 lo = __builtin_bit_cast(u32x2, __builtin_amdgcn_ds_read_tr16_b64_v4i16(LDSP(s16x4, a)));
        const u32x2 hi = __builtin_bit_cast(u32x2, __builtin_amdgcn_ds_read_tr16_b64_v4i16(LDSP(s16x4, a + 8192)));
        vt[k] = u32x4{lo[0], lo[1], hi[0], hi[1]};
      }
      f32x4 sc[2] = {{0.f, 0.f, 0.f, 0.f}, {0.f, 0.f, 0.f, 0.f}};
#pragma unroll
      for (int u = 0; u < 4; ++u)
#pragma unroll
        for (int t = 0; t < 2; ++t)
          sc[t] = __builtin_amdgcn_mfma_f32_16x16x32_bf16(__builtin_bit_cast(bf16x8, kf[t][u]), __builtin_bit_cast(bf16x8, qf[u]), sc[t], 0, 0, 0);
      i += 2;
      if (tokens < 32) {
#pragma unroll
        for (int r = 0; r < 8; ++r) sc[r >> 2][r & 3] = 16 * (r >> 2) + 4 * g + (r & 3) < tokens ? sc[r >> 2][r & 3] : -__builtin_inff();
      }
      tokens -= 64;
      const float mx = xg_max(vmax(vmax(vmax(sc[0][0], sc[0][1]), vmax(sc[0][2], sc[0][3])), vmax(vmax(sc[1][0], sc[1][1]), vmax(sc[1][2], sc[1][3]))));
      const float mn = vmax(m, mx * CS);
      const float alpha = __builtin_amdgcn_exp2f(m - mn);
      m = mn;
      float pr[8];
#pragma unroll
      for (int r = 0; r < 8; ++r) pr[r] = __builtin_amdgcn_exp2f(__builtin_fmaf(sc[r >> 2][r & 3], CS, -mn));
      asm volatile("" : "+v"(vt[0]), "+v"(vt[1]), "+v"(vt[2]), "+v"(vt[3]), "+v"(vt[4]), "+v"(vt[5]), "+v"(vt[6]), "+v"(vt[7]) :: "memory");
      lds_put(freew, i);
      l = l * alpha + (((pr[0] + pr[1]) + (pr[2] + pr[3])) + ((pr[4] + pr[5]) + (pr[6] + pr[7])));
      const u32x4 pf = {cvtpk(pr[0], pr[1]), cvtpk(pr[2], pr[3]), cvtpk(pr[4], pr[5]), cvtpk(pr[6], pr[7])};
#pragma unroll
      for (int k = 0; k < 8; ++k)
        acc[k] = __builtin_amdgcn_mfma_f32_16x16x32_bf16(__builtin_bit_cast(bf16x8, vt[k]), __builtin_bit_cast(bf16x8, pf), acc[k] * alpha, 0, 0, 0);
    }
    if (!lim) break;
    while (!__builtin_amdgcn_readfirstlane(lds_ld(dumpw))) {}
    asm volatile("" ::: "memory");
    const u32x2 y = *LDSP(const u32x2, base + DUMP + 8192 + 8 * lane);
    const float mn = vmax(m, __uint_as_float(y[0]));
    const float alpha = __builtin_amdgcn_exp2f(m - mn), wt = __builtin_amdgcn_exp2f(__uint_as_float(y[0]) - mn);
    m = mn;
    l = l * alpha + __uint_as_float(y[1]) * wt;
#pragma unroll
    for (int k = 0; k < 8; ++k) acc[k] = acc[k] * alpha + *LDSP(const f32x4, base + DUMP + 1024 * k + 16 * lane) * wt;
  }
  if (!w) {
#pragma unroll
    for (int k = 0; k < 8; ++k) *LDSP(f32x4, base + DUMP + 1024 * k + 16 * lane) = acc[k];
    *LDSP(u32x2, base + DUMP + 8192 + 8 * lane) = u32x2{__float_as_uint(m), __float_as_uint(l)};
    asm volatile("s_waitcnt lgkmcnt(0)" ::: "memory");
    lds_put(dumpw, 1);
  } else {
    const float inv = __builtin_amdgcn_rcpf(xg_sum(l));
    const __amdgpu_buffer_rsrc_t orr = rsrc(out + (b * 8 + h) * 4096, 4096);
#pragma unroll
    for (int r = 0; r < 4; ++r)
      __builtin_amdgcn_raw_buffer_store_b128(u32x4{cvtpk(acc[2 * r][0] * inv, acc[2 * r][1] * inv), cvtpk(acc[2 * r][2] * inv, acc[2 * r][3] * inv),
                                                   cvtpk(acc[2 * r + 1][0] * inv, acc[2 * r + 1][1] * inv), cvtpk(acc[2 * r + 1][2] * inv, acc[2 * r + 1][3] * inv)},
                                             orr, c * 256 + g * 64 + r * 16, 0, OPOL);
  }
}

extern "C" void kernel_launch(void* const* d_in, const int* in_sizes, int n_in, void* d_out, int out_size,
                              void* d_ws, size_t ws_size, hipStream_t stream) {
  pa_kernel<<<256, 128 * NL + 192, 0, stream>>>((const char*)d_in[0], (const char*)d_in[1], (const char*)d_in[2],
                                                (const int*)d_in[3], (const int*)d_in[4], (char*)d_out, (char*)d_ws);
}
```

```cpp
#define TS
#define SA 10000
#define OFSA 100
#define UNITA 10
#define SB 140000
#define OFSB 50
#define UNITB 5
#include <hip/hip_runtime.h>
#include <hip/hip_bf16.h>
#include <stdint.h>

#define LEAD 3
#define NL 4
#define EA 3
#define EF 0
#define KPOL " nt"
#define VPOL " nt"
#define TAU16 470
#define POFS 0
#define SLP 1
#define SLP2 1
#define OPOL 16

typedef __attribute__((ext_vector_type(8))) __bf16 bf16x8;
typedef __attribute__((ext_vector_type(4))) short s16x4;
typedef __attribute__((ext_vector_type(4))) float f32x4;
typedef __attribute__((ext_vector_type(4))) unsigned u32x4;
typedef __attribute__((ext_vector_type(2))) unsigned u32x2;
#define LDSP(T, a) ((__attribute__((address_space(3))) T*)(uintptr_t)(a))

constexpr int NBPS = 128, SKV = NBPS * 16, NS = 8;
constexpr unsigned DUMP = 2 * NS * 8192, WORDS = DUMP + 8192 + 512;

__device__ __forceinline__ unsigned cvtpk(float lo, float hi) {
  typedef __attribute__((ext_vector_type(2))) __bf16 bf16x2;
  bf16x2 r = {(__bf16)lo, (__bf16)hi};
  return __builtin_bit_cast(unsigned, r);
}
__device__ __forceinline__ float vmax(float a, float b) { return __builtin_elementwise_maximum(a, b); }
__device__ __forceinline__ float xg_max(float x) {
  auto s = __builtin_amdgcn_permlane16_swap(__float_as_uint(x), __float_as_uint(x), false, false);
  x = vmax(__uint_as_float(s[0]), __uint_as_float(s[1]));
  auto t = __builtin_amdgcn_permlane32_swap(__float_as_uint(x), __float_as_uint(x), false, false);
  return vmax(__uint_as_float(t[0]), __uint_as_float(t[1]));
}
__device__ __forceinline__ float xg_sum(float x) {
  auto s = __builtin_amdgcn_permlane16_swap(__float_as_uint(x), __float_as_uint(x), false, false);
  x = __uint_as_float(s[0]) + __uint_as_float(s[1]);
  auto t = __builtin_amdgcn_permlane32_swap(__float_as_uint(x), __float_as_uint(x), false, false);
  return __uint_as_float(t[0]) + __uint_as_float(t[1]);
}
__device__ __forceinline__ int scan32(int v) {
  v += __builtin_amdgcn_update_dpp(0, v, 0x111, 0xf, 0xf, false);
  v += __builtin_amdgcn_update_dpp(0, v, 0x112, 0xf, 0xf, false);
  v += __builtin_amdgcn_update_dpp(0, v, 0x114, 0xf, 0xf, false);
  v += __builtin_amdgcn_update_dpp(0, v, 0x118, 0xf, 0xf, false);
  v += __builtin_amdgcn_update_dpp(0, v, 0x142, 0xa, 0xf, false);
  return v;
}
__device__ __forceinline__ __amdgpu_buffer_rsrc_t rsrc(const void* p, unsigned n) {
  return __builtin_amdgcn_make_buffer_rsrc((void*)p, 0, n, 0x00020000);
}
__device__ __forceinline__ u32x4 srd(const void* p, unsigned n) {
  const uint64_t a = (uint64_t)p;
  return u32x4{(unsigned)a, (unsigned)(a >> 32) & 0xffffu, n, 0x00020000u};
}
__device__ __forceinline__ int rl(int v, int l) { return __builtin_amdgcn_readlane(v, l); }
__device__ __forceinline__ unsigned bal32(bool c) { return (unsigned)__builtin_amdgcn_ballot_w64(c); }
__device__ __forceinline__ int lds_ld(unsigned a) {
  return __hip_atomic_load(LDSP(int, a), __ATOMIC_RELAXED, __HIP_MEMORY_SCOPE_WORKGROUP);
}
__device__ __forceinline__ void lds_put(unsigned a, int v) {
  __hip_atomic_store(LDSP(int, a), v, __ATOMIC_RELAXED, __HIP_MEMORY_SCOPE_WORKGROUP);
}

#define DMA4(POL, dst, rs, v)                                                                         \
  do {                                                                                                \
    unsigned keep_;                                                                                   \
    asm volatile("s_mov_b32 %0, m0\n\ts_mov_b32 m0, %1\n\ts_nop 4\n\t"                                \
                 "buffer_load_dwordx4 %3, %2, 0 offen" POL " lds\n\t"                                 \
                 "buffer_load_dwordx4 %4, %2, 0 offen offset:1024" POL " lds\n\t"                     \
                 "buffer_load_dwordx4 %5, %2, 0 offen offset:2048" POL " lds\n\t"                     \
                 "buffer_load_dwordx4 %6, %2, 0 offen offset:3072" POL " lds\n\t"                     \
                 "s_mov_b32 m0, %0"                                                                   \
                 : "=&s"(keep_) : "s"(dst), "s"(rs), "v"(v[0]), "v"(v[1]), "v"(v[2]), "v"(v[3]) : "memory"); \
  } while (0)
#define WAITVM0() asm volatile("s_waitcnt vmcnt(0)" ::: "memory")
#define NOW() ((unsigned)__builtin_amdgcn_s_memrealtime())

__global__ void __launch_bounds__(128 * NL + 128)
pa_kernel(const char* __restrict__ q, const char* __restrict__ kc, const char* __restrict__ vc,
          const int* __restrict__ bt, const int* __restrict__ lens, char* __restrict__ out, char* __restrict__ ws) {
  __shared__ __attribute__((aligned(16384))) char lds[WORDS + 128];
  const unsigned t0 = NOW();
  asm volatile("" :: "s"(q), "s"(kc), "s"(vc), "s"(bt), "s"(lens), "s"(out));
  const int lane = threadIdx.x & 63;
  const int wave = __builtin_amdgcn_readfirstlane(threadIdx.x >> 6);
  const int w = wave < 2 * NL ? wave >= NL : wave - 2 * NL, lw = wave - NL * w;
  const int b = blockIdx.x >> 3, h = (blockIdx.x - b) & 7;
  const int g = lane >> 4, c = lane & 15;
  const unsigned base = (unsigned)(uintptr_t)LDSP(char, lds);
  const unsigned ring = base + w * (NS * 8192), fullw = base + WORDS + w * 64, freew = fullw + 32, dumpw = base + WORDS + 36;
  unsigned* const tsp = (unsigned*)(ws + (64u << 20)) + (blockIdx.x * 2 + w) * 16;

  if (wave < 2 * NL) {
    const int r = lw + NL * lane, pg = 4 * (r >> 1) + 2 * w + (r & 1);
    int len = lens[lane & 31];
    const int pgv = bt[b * NBPS + min(pg, NBPS - 1)];
    unsigned go[4];
#pragma unroll
    for (int u = 0; u < 4; ++u) go[u] = (4 * u + g) * 2048 + ((c ^ (4 * u + g)) << 4) - 1024 * u;
    if (len <= 0 || len > SKV) len = SKV;
    const int lenb = rl(len, b);
    const int rows = min(max(lenb - 16 * pg, 0), 16);
    auto issue = [&](int j, unsigned page, unsigned nrec) __attribute__((always_inline)) {
      const size_t po = (size_t)page * 32768 + h * 256;
      const u32x4 kr = srd(kc + po, nrec), vr = srd(vc + po, nrec);
      const unsigned dst = ring + (j & (NS - 1)) * 8192;
      DMA4(KPOL, dst, kr, go);
      DMA4(VPOL, dst + 4096, vr, go);
    };
    const int npos = ((lenb + 63 - 32 * w) >> 6) * 2;
    const bool early = lw < (w ? EF : EA) && lw < npos;
    if (early) issue(lw, rl(pgv, 0), rl(rows, 0) * 2048);
    const int np = (lenb + 15) >> 4, n0 = (np >> 2) * 2 + min(np & 3, 2), ld = LEAD + 2 * n0 - np;
    const float tau = (float)rl(scan32((len - 1) >> 4), 31) * (TAU16 / 512.f) * __builtin_amdgcn_rcpf((float)max(np - 1, 1));
    const int slot = w ? min(2 * r + ld, n0 + r) : max(2 * r + 1 - ld, r);
    const unsigned tab = rows ? (unsigned)((float)slot * tau) << 16 | rows << 11 : 0;
    int freed = 0;
    asm volatile("s_barrier" ::: "memory");
    if (lane == 0 && lw == 0) { tsp[14] = NOW(); tsp[4] = npos; }
    for (int j = lw, k = 0; j < npos; j += NL, ++k) {
      if (k || !early) {
        const unsigned e = rl(tab, k);
        while (freed + NS <= j) {
          freed = __builtin_amdgcn_readfirstlane(lds_ld(freew));
          if (freed + NS <= j) __builtin_amdgcn_s_sleep(SLP);
        }
        const int due = (e >> 16) + POFS;
        while ((int)(NOW() - t0) < due) __builtin_amdgcn_s_sleep(SLP2);
        issue(j, rl(pgv, k), e & 0xffff);
      }
      WAITVM0();
      lds_put(fullw + 4 * (j & (NS - 1)), j + 1);
    }
    if (lane == 0 && lw == 0) tsp[15] = NOW();
    return;
  }

  __builtin_amdgcn_s_setprio(3);
  if (lane < 16) lds_put(fullw + 4 * lane, 0);
  asm volatile("s_waitcnt lgkmcnt(0)" ::: "memory");
  int len = lens[b];
  u32x4 qf[4];
#pragma unroll
  for (int u = 0; u < 4; ++u) qf[u] = *(const u32x4*)(q + (b * 8 + h) * 4096 + c * 256 + g * 16 + 64 * u);
  asm volatile("s_barrier" ::: "memory");
  if (len <= 0) {
#pragma unroll
    for (int u = 0; u < 4; ++u) qf[u] = u32x4{0, 0, 0, 0};
  }
  if (len <= 0 || len > SKV) len = SKV;
  const float CS = 0.088388347648318440f * 1.4426950408889634f;
  const unsigned ka = ring + c * 256 + ((g ^ c) << 4);
  const unsigned va = ring + 4096 + (lane >> 2) * 256 + ((((lane & 3) << 2) ^ (lane >> 2)) << 4);
  f32x4 acc[8];
#pragma unroll
  for (int k = 0; k < 8; ++k) acc[k] = f32x4{0.f, 0.f, 0.f, 0.f};
  float m = -1e30f, l = 0.f;
  unsigned ts_first = 0, ts_real = 0, ts_mid = 0, ts_dump = 0;
  const unsigned ck0 = (unsigned)__builtin_amdgcn_s_memtime(), rt0 = NOW();
  asm volatile("" : "+v"(qf[0]), "+v"(qf[1]), "+v"(qf[2]), "+v"(qf[3]));
  int i = 0, tokens = len - 32 * w;
  for (int lim = 64 * w;; lim = 0) {
    while (tokens > lim) {
      for (;;) {
        const uint64_t f = __hip_atomic_load(LDSP(uint64_t, fullw + 4 * (i & (NS - 1))), __ATOMIC_RELAXED, __HIP_MEMORY_SCOPE_WORKGROUP);
        if (__builtin_amdgcn_readfirstlane((unsigned)f + (unsigned)(f >> 32)) == 2 * i + 3) break;
      }
      asm volatile("" ::: "memory");
      if (i == 0) ts_first = NOW();
      if (i == 24) ts_mid = NOW();
      if (tokens <= 64) ts_real = NOW();
      const unsigned o = (i & (NS - 1)) * 8192;
      u32x4 kf[2][4], vt[8];
#pragma unroll
      for (int u = 0; u < 4; ++u) {
        const unsigned a = (o + ka) ^ (u << 6);
        kf[0][u] = *LDSP(const u32x4, a);
        kf[1][u] = *LDSP(const u32x4, a + 8192);
      }
#pragma unroll
      for (int k = 0; k < 8; ++k) {
        const unsigned a = ((o + va) ^ ((k >> 1) << 4)) + 8 * (k & 1);
        const u32x2 lo = __builtin_bit_cast(u32x2, __builtin_amdgcn_ds_read_tr16_b64_v4i16(LDSP(s16x4, a)));
        const u32x2 hi = __builtin_bit_cast(u32x2, __builtin_amdgcn_ds_read_tr16_b64_v4i16(LDSP(s16x4, a + 8192)));
        vt[k] = u32x4{lo[0], lo[1], hi[0], hi[1]};
      }
      f32x4 sc[2] = {{0.f, 0.f, 0.f, 0.f}, {0.f, 0.f, 0.f, 0.f}};
#pragma unroll
      for (int u = 0; u < 4; ++u)
#pragma unroll
        for (int t = 0; t < 2; ++t)
          sc[t] = __builtin_amdgcn_mfma_f32_16x16x32_bf16(__builtin_bit_cast(bf16x8, kf[t][u]), __builtin_bit_cast(bf16x8, qf[u]), sc[t], 0, 0, 0);
      i += 2;
      if (tokens < 32) {
#pragma unroll
        for (int r = 0; r < 8; ++r) sc[r >> 2][r & 3] = 16 * (r >> 2) + 4 * g + (r & 3) < tokens ? sc[r >> 2][r & 3] : -__builtin_inff();
      }
      tokens -= 64;
      const float mx = xg_max(vmax(vmax(vmax(sc[0][0], sc[0][1]), vmax(sc[0][2], sc[0][3])), vmax(vmax(sc[1][0], sc[1][1]), vmax(sc[1][2], sc[1][3]))));
      const float mn = vmax(m, mx * CS);
      const float alpha = __builtin_amdgcn_exp2f(m - mn);
      m = mn;
      float pr[8];
#pragma unroll
      for (int r = 0; r < 8; ++r) pr[r] = __builtin_amdgcn_exp2f(__builtin_fmaf(sc[r >> 2][r & 3], CS, -mn));
      asm volatile("" : "+v"(vt[0]), "+v"(vt[1]), "+v"(vt[2]), "+v"(vt[3]), "+v"(vt[4]), "+v"(vt[5]), "+v"(vt[6]), "+v"(vt[7]) :: "memory");
      lds_put(freew, i);
      l = l * alpha + (((pr[0] + pr[1]) + (pr[2] + pr[3])) + ((pr[4] + pr[5]) + (pr[6] + pr[7])));
      const u32x4 pf = {cvtpk(pr[0], pr[1]), cvtpk(pr[2], pr[3]), cvtpk(pr[4], pr[5]), cvtpk(pr[6], pr[7])};
#pragma unroll
      for (int k = 0; k < 8; ++k)
        acc[k] = __builtin_amdgcn_mfma_f32_16x16x32_bf16(__builtin_bit_cast(bf16x8, vt[k]), __builtin_bit_cast(bf16x8, pf), acc[k] * alpha, 0, 0, 0);
    }
    if (!lim) break;
    while (!__builtin_amdgcn_readfirstlane(lds_ld(dumpw))) {}
    asm volatile("" ::: "memory");
    ts_dump = NOW();
    const u32x2 y = *LDSP(const u32x2, base + DUMP + 8192 + 8 * lane);
    const float mn = vmax(m, __uint_as_float(y[0]));
    const float alpha = __builtin_amdgcn_exp2f(m - mn), wt = __builtin_amdgcn_exp2f(__uint_as_float(y[0]) - mn);
    m = mn;
    l = l * alpha + __uint_as_float(y[1]) * wt;
#pragma unroll
    for (int k = 0; k < 8; ++k) acc[k] = acc[k] * alpha + *LDSP(const f32x4, base + DUMP + 1024 * k + 16 * lane) * wt;
  }
  if (!w) {
#pragma unroll
    for (int k = 0; k < 8; ++k) *LDSP(f32x4, base + DUMP + 1024 * k + 16 * lane) = acc[k];
    *LDSP(u32x2, base + DUMP + 8192 + 8 * lane) = u32x2{__float_as_uint(m), __float_as_uint(l)};
    asm volatile("s_waitcnt lgkmcnt(0)" ::: "memory");
    lds_put(dumpw, 1);
  } else {
    const float inv = __builtin_amdgcn_rcpf(xg_sum(l));
    const __amdgpu_buffer_rsrc_t orr = rsrc(out + (b * 8 + h) * 4096, 4096);
#pragma unroll
    for (int r = 0; r < 4; ++r)
      __builtin_amdgcn_raw_buffer_store_b128(u32x4{cvtpk(acc[2 * r][0] * inv, acc[2 * r][1] * inv), cvtpk(acc[2 * r][2] * inv, acc[2 * r][3] * inv),
                                                   cvtpk(acc[2 * r + 1][0] * inv, acc[2 * r + 1][1] * inv), cvtpk(acc[2 * r + 1][2] * inv, acc[2 * r + 1][3] * inv)},
                                             orr, c * 256 + g * 64 + r * 16, 0, OPOL);
  }
  if (lane == 0) { unsigned* t = tsp; t[0] = t0; t[1] = ts_first; t[2] = ts_real; t[3] = NOW();
    t[5] = w; t[6] = ts_dump - t0; t[7] = ts_mid; t[8] = (unsigned)__builtin_amdgcn_s_memtime() - ck0; t[9] = NOW() - rt0; t[10] = 0; t[11] = h; t[12] = 0; t[13] = __builtin_amdgcn_s_getreg((31 << 11) | 20); }
}

__device__ float stat_of(const unsigned* t, unsigned lo, int s) {
  const int f = s / 10000, sel = (s / 100) % 100, val = s % 100;
  float sum = 0.f, mx = 0.f; int n = 0;
  for (int w = 0; w < 512; ++w) {
    const unsigned* x = t + w * 16;
    bool in = true;
    if (sel == 1 || sel == 5) in = (int)x[11] == val;
    if (sel == 2 || sel == 6) in = (int)(x[13] & 7) == val;
    if (sel == 4) in = (x[5] > 0) == (val != 0);
    if (!in) continue;
    const float v = (float)(x[f] - lo);
    sum += v; mx = fmaxf(mx, v); ++n;
  }
  return (sel == 3 || sel == 5 || sel == 6) ? mx : sum / n;
}
__global__ void diag_kernel(const unsigned* __restrict__ tg) {
  __shared__ unsigned t[512 * 16];
  for (int i = threadIdx.x; i < 512 * 16; i += 512) t[i] = tg[i];
  __syncthreads();
  if (threadIdx.x == 0) {
    unsigned lo = t[0]; for (int i = 0; i < 512; ++i) if ((int)(t[i * 16] - lo) < 0) lo = t[i * 16];
    const int qa = min(63, max(0, (int)((stat_of(t, lo, SA) - OFSA) / UNITA))), qb = min(63, max(0, (int)((stat_of(t, lo, SB) - OFSB) / UNITB)));
    const int due = 30000 + 200 * qa + 12800 * qb;
    while ((int)((unsigned)__builtin_amdgcn_s_memrealtime() - lo) < due) __builtin_amdgcn_s_sleep(1);
  }
}

extern "C" void kernel_launch(void* const* d_in, const int* in_sizes, int n_in, void* d_out, int out_size,
                              void* d_ws, size_t ws_size, hipStream_t stream) {
  pa_kernel<<<256, 128 * NL + 128, 0, stream>>>((const char*)d_in[0], (const char*)d_in[1], (const char*)d_in[2],
                                                (const int*)d_in[3], (const int*)d_in[4], (char*)d_out, (char*)d_ws);
  diag_kernel<<<1, 512, 0, stream>>>((const unsigned*)((char*)d_ws + (64u << 20)));
}
```

```cpp
#define TAU16 480
#include <hip/hip_runtime.h>
#include <hip/hip_bf16.h>
#include <stdint.h>

#define LEAD 3
#define CAPS 58
#define CAPT 75
#define LMAX 84
#define EA 3
#define XMIN 4
#define MARG 600
#define STAG 30
#define KPOL " nt"
#define VPOL " nt"
#define POFS 0
#define SLP 1
#define SLP2 1
#define OPOL 16

typedef __attribute__((ext_vector_type(8))) __bf16 bf16x8;
typedef __attribute__((ext_vector_type(4))) short s16x4;
typedef __attribute__((ext_vector_type(4))) float f32x4;
typedef __attribute__((ext_vector_type(4))) unsigned u32x4;
typedef __attribute__((ext_vector_type(2))) unsigned u32x2;
typedef __attribute__((address_space(1))) unsigned gu32;
#define LDSP(T, a) ((__attribute__((address_space(3))) T*)(uintptr_t)(a))

constexpr int NBPS = 128, SKV = NBPS * 16, NS = 8;
constexpr unsigned DUMP = 2 * NS * 8192, DSZ = 8192 + 512, WORDS = DUMP + 2 * DSZ;
constexpr unsigned SLOT = 8192, FLAG_OFF = 4096 + 128, EPOCH_OFF = 4096 + 256;
constexpr unsigned TAGK = 0x9E3779B9u;

__device__ __forceinline__ unsigned cvtpk(float lo, float hi) {
  typedef __attribute__((ext_vector_type(2))) __bf16 bf16x2;
  bf16x2 r = {(__bf16)lo, (__bf16)hi};
  return __builtin_bit_cast(unsigned, r);
}
__device__ __forceinline__ float vmax(float a, float b) { return __builtin_elementwise_maximum(a, b); }
__device__ __forceinline__ float xg_max(float x) {
  auto s = __builtin_amdgcn_permlane16_swap(__float_as_uint(x), __float_as_uint(x), false, false);
  x = vmax(__uint_as_float(s[0]), __uint_as_float(s[1]));
  auto t = __builtin_amdgcn_permlane32_swap(__float_as_uint(x), __float_as_uint(x), false, false);
  return vmax(__uint_as_float(t[0]), __uint_as_float(t[1]));
}
__device__ __forceinline__ float xg_sum(float x) {
  auto s = __builtin_amdgcn_permlane16_swap(__float_as_uint(x), __float_as_uint(x), false, false);
  x = __uint_as_float(s[0]) + __uint_as_float(s[1]);
  auto t = __builtin_amdgcn_permlane32_swap(__float_as_uint(x), __float_as_uint(x), false, false);
  return __uint_as_float(t[0]) + __uint_as_float(t[1]);
}
__device__ __forceinline__ int scan32(int v) {
  v += __builtin_amdgcn_update_dpp(0, v, 0x111, 0xf, 0xf, false);
  v += __builtin_amdgcn_update_dpp(0, v, 0x112, 0xf, 0xf, false);
  v += __builtin_amdgcn_update_dpp(0, v, 0x114, 0xf, 0xf, false);
  v += __builtin_amdgcn_update_dpp(0, v, 0x118, 0xf, 0xf, false);
  v += __builtin_amdgcn_update_dpp(0, v, 0x142, 0xa, 0xf, false);
  return v;
}
__device__ __forceinline__ __amdgpu_buffer_rsrc_t rsrc(const void* p, unsigned n) {
  return __builtin_amdgcn_make_buffer_rsrc((void*)p, 0, n, 0x00020000);
}
__device__ __forceinline__ u32x4 srd(const void* p, unsigned n) {
  const uint64_t a = (uint64_t)p;
  return u32x4{(unsigned)a, (unsigned)(a >> 32) & 0xffffu, n, 0x00020000u};
}
__device__ __forceinline__ int rl(int v, int l) { return __builtin_amdgcn_readlane(v, l); }
__device__ __forceinline__ unsigned bal32(bool c) { return (unsigned)__builtin_amdgcn_ballot_w64(c); }
__device__ __forceinline__ int lds_ld(unsigned a) {
  return __hip_atomic_load(LDSP(int, a), __ATOMIC_RELAXED, __HIP_MEMORY_SCOPE_WORKGROUP);
}
__device__ __forceinline__ void lds_put(unsigned a, int v) {
  __hip_atomic_store(LDSP(int, a), v, __ATOMIC_RELAXED, __HIP_MEMORY_SCOPE_WORKGROUP);
}

#define DMA4(POL, dst, rs, v)                                                                         \
  do {                                                                                                \
    unsigned keep_;                                                                                   \
    asm volatile("s_mov_b32 %0, m0\n\ts_mov_b32 m0, %1\n\ts_nop 4\n\t"                                \
                 "buffer_load_dwordx4 %3, %2, 0 offen" POL " lds\n\t"                                 \
                 "buffer_load_dwordx4 %4, %2, 0 offen offset:1024" POL " lds\n\t"                     \
                 "buffer_load_dwordx4 %5, %2, 0 offen offset:2048" POL " lds\n\t"                     \
                 "buffer_load_dwordx4 %6, %2, 0 offen offset:3072" POL " lds\n\t"                     \
                 "s_mov_b32 m0, %0"                                                                   \
                 : "=&s"(keep_) : "s"(dst), "s"(rs), "v"(v[0]), "v"(v[1]), "v"(v[2]), "v"(v[3]) : "memory"); \
  } while (0)
#define WAITVM0() asm volatile("s_waitcnt vmcnt(0)" ::: "memory")
#define NOW() ((unsigned)__builtin_amdgcn_s_memrealtime())

__global__ void __launch_bounds__(896)
pa_kernel(const char* __restrict__ q, const char* __restrict__ kc, const char* __restrict__ vc,
          const int* __restrict__ bt, const int* __restrict__ lens, char* __restrict__ out, char* __restrict__ ws) {
  __shared__ __attribute__((aligned(16384))) char lds[WORDS + 192];
  const unsigned t0 = NOW();
  asm volatile("" :: "s"(q), "s"(kc), "s"(vc), "s"(bt), "s"(lens), "s"(out), "s"(ws));
  const int lane = threadIdx.x & 63;
  const int wave = __builtin_amdgcn_readfirstlane(threadIdx.x >> 6);
  const int b = blockIdx.x >> 3, h = (blockIdx.x - b) & 7;
  const int g = lane >> 4, c = lane & 15;
  const unsigned base = (unsigned)(uintptr_t)LDSP(char, lds);
  const int w = wave < 8 ? wave >> 2 : min(wave - 8, 2);

  const int pg = 4 * (lane >> 1) + (wave >> 1 & 2) + (lane & 1);
  const int ly = lane < 32 ? lane : (lane ^ 16) & 31;
  int len = lens[lane & 31], leny = lens[ly];
  int pgv = bt[b * NBPS + pg];
  u32x4 qf[4];
  if ((wave & 14) == 8) {
#pragma unroll
    for (int u = 0; u < 4; ++u) qf[u] = *(const u32x4*)(q + (b * 8 + h) * 4096 + c * 256 + g * 16 + 64 * u);
  }
  if (wave == 10 && lane < 48) lds_put(base + WORDS + 4 * lane, 0);
  asm volatile("s_waitcnt lgkmcnt(0)\n\ts_barrier" ::: "memory");
  unsigned go[4];
#pragma unroll
  for (int u = 0; u < 4; ++u) go[u] = (4 * u + g) * 2048 + ((c ^ (4 * u + g)) << 4) - 1024 * u;
  asm volatile("" : "+v"(len), "+v"(leny), "+v"(pgv));
  const unsigned unimask = bal32(len <= 0);
  if (len <= 0 || len > SKV) len = SKV;
  int lenb = rl(len, b);
  auto issue = [&](unsigned dst, unsigned page, int hd, unsigned nrec) __attribute__((always_inline)) {
    const size_t po = (size_t)page * 32768 + hd * 256;
    const u32x4 kr = srd(kc + po, nrec), vr = srd(vc + po, nrec);
    DMA4(KPOL, dst, kr, go);
    DMA4(VPOL, dst + 4096, vr, go);
  };
  const bool early = wave < EA && (wave < 2 || lenb > 64);
  if (early) issue(base + wave * 8192, rl(pgv, wave), h, __builtin_amdgcn_readfirstlane(min(max(lenb - 16 * (wave + (wave & 2)), 0), 16) * 2048));

  if (leny <= 0 || leny > SKV) leny = SKV;
  const int key = ((len + 15) >> 4) * 32 + (lane & 31), keyy = ((leny + 15) >> 4) * 32 + ly;
  unsigned less = (unsigned)(keyy - key) >> 31;
#define ROR(n) less = __builtin_amdgcn_alignbit(less, __builtin_amdgcn_update_dpp(0, keyy, 0x120 + n, 0xf, 0xf, true) - key, 31);
  ROR(1) ROR(2) ROR(3) ROR(4) ROR(5) ROR(6) ROR(7) ROR(8) ROR(9) ROR(10) ROR(11) ROR(12) ROR(13) ROR(14) ROR(15)
  int rank = __builtin_popcount(less);
  {
    auto t = __builtin_amdgcn_permlane32_swap(rank, rank, false, false);
    rank = t[0] + t[1];
  }
  const int tot = rl(scan32(key >> 5), 31), rb = rl(rank, b);
  const bool gives = (h & 1) && h != 3, pair73 = (h & 3) == 3;
  const int rg = gives ? 31 - rb : pair73 ? rb : 2 * rb + (h >> 1 & 1);
  const int ph = pair73 ? h ^ 4 : gives ? h - 1 + 2 * (rg & 1) : (h & 4) | 1;
  const int pb = __builtin_ctz(bal32(rank == (gives ? (pair73 ? rg : rg >> 1) : 31 - rg)) | 0x80000000u);
  const int npb = (lenb + 15) >> 4, lenp = rl(len, pb), npp = (lenp + 15) >> 4;
  const int room = ((tot * LMAX) >> 11) - (gives ? npp : npb);
  int x = min((gives ? npb : npp) - max((tot * (pair73 ? CAPT : CAPS)) >> 11, 8), min(pair73 ? room : (room * 43) >> 6, 64));
  if (x < XMIN || rg > 31) x = 0;
  const bool helps = x && !gives;
  if (x && gives) lenb = 16 * (npb - x);
  const int tend = ((tot - 32) * TAU16) >> 9, tend2 = max(tend - MARG - STAG * rg, 0);
  const int msk = helps && w != 1 ? 3 : 7;
  const unsigned ring = base + (w == 1 ? 8 * 8192 : w * 2 * 8192), fullw = base + WORDS + w * 64, freew = fullw + 32;
  const int ftok = lenp - 16 * (npp - x);

  if (wave < 8 || wave > 10) {
    int npos, hd = h;
    unsigned tab;
    if (w == 2) {
      if (!helps) return;
      pgv = bt[pb * NBPS + min(npp - x + lane, NBPS - 1)];
      hd = ph;
      npos = (x + 1) & ~1;
      const int rows = min(max(ftok - 16 * lane, 0), 16);
      tab = rows ? (unsigned)((float)((lane + 1) * tend2) * __builtin_amdgcn_rcpf((float)x)) << 16 | rows << 11 : 0;
    } else {
      npos = ((lenb + 63 - 32 * w) >> 6) * 2;
      const int rows = min(max(lenb - 16 * pg, 0), 16);
      const int np = (lenb + 15) >> 4, n0 = (np >> 2) * 2 + min(np & 3, 2), ld = LEAD + 2 * n0 - np;
      const int slot = w ? min(2 * lane + ld, n0 + lane) : max(2 * lane + 1 - ld, lane);
      tab = rows ? (unsigned)((float)(slot * tend) * __builtin_amdgcn_rcpf((float)max(np - 1, 1))) << 16 | rows << 11 : 0;
    }
    int freed = 0;
    const int st = w == 2 ? 3 : 4;
    for (int j = w == 2 ? wave - 11 : wave & 3; j < npos; j += st) {
      if (j != wave || !early) {
        const unsigned e = rl(tab, j);
        while (freed + msk < j) {
          freed = __builtin_amdgcn_readfirstlane(lds_ld(freew));
          if (freed + msk < j) __builtin_amdgcn_s_sleep(SLP);
        }
        const int due = (e >> 16) + POFS;
        while ((int)(NOW() - t0) < due) __builtin_amdgcn_s_sleep(SLP2);
        issue(ring + (j & msk) * 8192, rl(pgv, j), hd, e & 0xffff);
      }
      WAITVM0();
      lds_put(fullw + 4 * (j & msk), j + 1);
    }
    return;
  }

  char* const slot = ws + (unsigned)(gives ? b * 8 + h : pb * 8 + ph) * SLOT;
  unsigned tag = 0;
  if (w == 2) {
    if (!x) return;
    tag = __hip_atomic_load((gu32*)(slot + EPOCH_OFF), __ATOMIC_RELAXED, __HIP_MEMORY_SCOPE_AGENT) + TAGK;
    if (gives) {
      while ((int)(NOW() - t0) < tend2 + POFS + 150) __builtin_amdgcn_s_sleep(16);
#pragma nounroll
      for (int spin = 0; spin < (1 << 22); ++spin) {
        if (__builtin_amdgcn_readfirstlane(__hip_atomic_load((gu32*)(slot + FLAG_OFF), __ATOMIC_RELAXED, __HIP_MEMORY_SCOPE_AGENT) == tag)) break;
        __builtin_amdgcn_s_sleep(8);
      }
      asm volatile("" ::: "memory");
      const __amdgpu_buffer_rsrc_t pr = rsrc(slot, 4096 + 64);
      u32x4 y[4];
#pragma unroll
      for (int r = 0; r < 4; ++r) y[r] = __builtin_amdgcn_raw_buffer_load_b128(pr, lane * 16 + r * 1024, 0, 16);
      const unsigned lse = __builtin_amdgcn_raw_buffer_load_b32(pr, 4096 + 4 * c, 0, 16);
#pragma unroll
      for (int k = 0; k < 8; ++k) {
        const unsigned lo = y[k >> 1][2 * (k & 1)], hi = y[k >> 1][2 * (k & 1) + 1];
        *LDSP(u32x4, base + DUMP + DSZ + 1024 * k + 16 * lane) = u32x4{lo << 16, lo & 0xffff0000u, hi << 16, hi & 0xffff0000u};
      }
      *LDSP(u32x2, base + DUMP + DSZ + 8192 + 8 * lane) = u32x2{lse, g ? 0u : 0x3f800000u};
      asm volatile("s_waitcnt lgkmcnt(0)" ::: "memory");
      lds_put(fullw + 36, 1);
      if (lane == 0) __hip_atomic_store((gu32*)(slot + EPOCH_OFF), tag, __ATOMIC_RELAXED, __HIP_MEMORY_SCOPE_AGENT);
      return;
    }
  }
  __builtin_amdgcn_s_setprio(3);
  if (w == 2) {
#pragma unroll
    for (int u = 0; u < 4; ++u) qf[u] = *(const u32x4*)(q + (pb * 8 + ph) * 4096 + c * 256 + g * 16 + 64 * u);
  }
  if ((unimask >> (w == 2 ? pb : b)) & 1) {
#pragma unroll
    for (int u = 0; u < 4; ++u) qf[u] = u32x4{0, 0, 0, 0};
  }
  const float CS = 0.088388347648318440f * 1.4426950408889634f;
  const unsigned ka = ring + c * 256 + ((g ^ c) << 4);
  const unsigned va = ring + 4096 + (lane >> 2) * 256 + ((((lane & 3) << 2) ^ (lane >> 2)) << 4);
  f32x4 acc[8];
#pragma unroll
  for (int k = 0; k < 8; ++k) acc[k] = f32x4{0.f, 0.f, 0.f, 0.f};
  float m = -1e30f, l = 0.f;
  asm volatile("" : "+v"(qf[0]), "+v"(qf[1]), "+v"(qf[2]), "+v"(qf[3]), "+v"(tag));
  int i = 0, tokens = w == 2 ? ftok : lenb - 32 * w;
  const int dec = w == 2 ? 32 : 64;
  for (int lim = w == 1 || (x && gives) ? 64 : 0;; lim = 0) {
    while (tokens > lim) {
      for (;;) {
        const uint64_t f = __hip_atomic_load(LDSP(uint64_t, fullw + 4 * (i & msk)), __ATOMIC_RELAXED, __HIP_MEMORY_SCOPE_WORKGROUP);
        if (__builtin_amdgcn_readfirstlane((unsigned)f + (unsigned)(f >> 32)) == 2 * i + 3) break;
      }
      asm volatile("" ::: "memory");
      const unsigned o = (i & msk) * 8192;
      u32x4 kf[2][4], vt[8];
#pragma unroll
      for (int u = 0; u < 4; ++u) {
        const unsigned a = (o + ka) ^ (u << 6);
        kf[0][u] = *LDSP(const u32x4, a);
        kf[1][u] = *LDSP(const u32x4, a + 8192);
      }
#pragma unroll
      for (int k = 0; k < 8; ++k) {
        const unsigned a = ((o + va) ^ ((k >> 1) << 4)) + 8 * (k & 1);
        const u32x2 lo = __builtin_bit_cast(u32x2, __builtin_amdgcn_ds_read_tr16_b64_v4i16(LDSP(s16x4, a)));
        const u32x2 hi = __builtin_bit_cast(u32x2, __builtin_amdgcn_ds_read_tr16_b64_v4i16(LDSP(s16x4, a + 8192)));
        vt[k] = u32x4{lo[0], lo[1], hi[0], hi[1]};
      }
      f32x4 sc[2] = {{0.f, 0.f, 0.f, 0.f}, {0.f, 0.f, 0.f, 0.f}};
#pragma unroll
      for (int u = 0; u < 4; ++u)
#pragma unroll
        for (int t = 0; t < 2; ++t)
          sc[t] = __builtin_amdgcn_mfma_f32_16x16x32_bf16(__builtin_bit_cast(bf16x8, kf[t][u]), __builtin_bit_cast(bf16x8, qf[u]), sc[t], 0, 0, 0);
      i += 2;
      if (tokens < 32) {
#pragma unroll
        for (int r = 0; r < 8; ++r) sc[r >> 2][r & 3] = 16 * (r >> 2) + 4 * g + (r & 3) < tokens ? sc[r >> 2][r & 3] : -__builtin_inff();
      }
      tokens -= dec;
      const float mx = xg_max(vmax(vmax(vmax(sc[0][0], sc[0][1]), vmax(sc[0][2], sc[0][3])), vmax(vmax(sc[1][0], sc[1][1]), vmax(sc[1][2], sc[1][3]))));
      const float mn = vmax(m, mx * CS);
      const float alpha = __builtin_amdgcn_exp2f(m - mn);
      m = mn;
      float pr[8];
#pragma unroll
      for (int r = 0; r < 8; ++r) pr[r] = __builtin_amdgcn_exp2f(__builtin_fmaf(sc[r >> 2][r & 3], CS, -mn));
      asm volatile("" : "+v"(vt[0]), "+v"(vt[1]), "+v"(vt[2]), "+v"(vt[3]), "+v"(vt[4]), "+v"(vt[5]), "+v"(vt[6]), "+v"(vt[7]) :: "memory");
      lds_put(freew, i);
      l = l * alpha + (((pr[0] + pr[1]) + (pr[2] + pr[3])) + ((pr[4] + pr[5]) + (pr[6] + pr[7])));
      const u32x4 pf = {cvtpk(pr[0], pr[1]), cvtpk(pr[2], pr[3]), cvtpk(pr[4], pr[5]), cvtpk(pr[6], pr[7])};
#pragma unroll
      for (int k = 0; k < 8; ++k)
        acc[k] = __builtin_amdgcn_mfma_f32_16x16x32_bf16(__builtin_bit_cast(bf16x8, vt[k]), __builtin_bit_cast(bf16x8, pf), acc[k] * alpha, 0, 0, 0);
    }
    if (!lim) break;
    while (!__builtin_amdgcn_readfirstlane(lds_ld(base + WORDS + 36 + (w ? 0 : 128)))) {}
    asm volatile("" ::: "memory");
    const unsigned src = base + DUMP + (w ? 0 : DSZ);
    const u32x2 y = *LDSP(const u32x2, src + 8192 + 8 * lane);
    const float mn = vmax(m, __uint_as_float(y[0]));
    const float alpha = __builtin_amdgcn_exp2f(m - mn), wt = __builtin_amdgcn_exp2f(__uint_as_float(y[0]) - mn);
    m = mn;
    l = l * alpha + __uint_as_float(y[1]) * wt;
#pragma unroll
    for (int k = 0; k < 8; ++k) acc[k] = acc[k] * alpha + *LDSP(const f32x4, src + 1024 * k + 16 * lane) * wt;
  }
  if (!w) {
#pragma unroll
    for (int k = 0; k < 8; ++k) *LDSP(f32x4, base + DUMP + 1024 * k + 16 * lane) = acc[k];
    *LDSP(u32x2, base + DUMP + 8192 + 8 * lane) = u32x2{__float_as_uint(m), __float_as_uint(l)};
    asm volatile("s_waitcnt lgkmcnt(0)" ::: "memory");
    lds_put(fullw + 36, 1);
  } else {
    const float lt = xg_sum(l);
    const float inv = __builtin_amdgcn_rcpf(lt);
    u32x4 y[4];
#pragma unroll
    for (int r = 0; r < 4; ++r)
      y[r] = u32x4{cvtpk(acc[2 * r][0] * inv, acc[2 * r][1] * inv), cvtpk(acc[2 * r][2] * inv, acc[2 * r][3] * inv),
                   cvtpk(acc[2 * r + 1][0] * inv, acc[2 * r + 1][1] * inv), cvtpk(acc[2 * r + 1][2] * inv, acc[2 * r + 1][3] * inv)};
    if (w == 1) {
      const __amdgpu_buffer_rsrc_t orr = rsrc(out + (b * 8 + h) * 4096, 4096);
#pragma unroll
      for (int r = 0; r < 4; ++r) __builtin_amdgcn_raw_buffer_store_b128(y[r], orr, c * 256 + g * 64 + r * 16, 0, OPOL);
    } else {
      const __amdgpu_buffer_rsrc_t orr = rsrc(slot, 4096 + 64);
#pragma unroll
      for (int r = 0; r < 4; ++r) __builtin_amdgcn_raw_buffer_store_b128(y[r], orr, lane * 16 + r * 1024, 0, 16);
      __builtin_amdgcn_raw_buffer_store_b32(__float_as_uint(m + __builtin_amdgcn_logf(lt)), orr, lane * 4 + 4096, 0, 16);
      WAITVM0();
      if (lane == 0) __hip_atomic_store((gu32*)(slot + FLAG_OFF), tag, __ATOMIC_RELAXED, __HIP_MEMORY_SCOPE_AGENT);
      return;
    }
  }
}

extern "C" void kernel_launch(void* const* d_in, const int* in_sizes, int n_in, void* d_out, int out_size,
                              void* d_ws, size_t ws_size, hipStream_t stream) {
  pa_kernel<<<256, 896, 0, stream>>>((const char*)d_in[0], (const char*)d_in[1], (const char*)d_in[2],
                                     (const int*)d_in[3], (const int*)d_in[4], (char*)d_out, (char*)d_ws);
}
```

```cpp
#define TS
#define SA 140000
#define OFSA 100
#define UNITA 3
#define SB 30000
#define OFSB 2800
#define UNITB 10
#include <hip/hip_runtime.h>
#include <hip/hip_bf16.h>
#include <stdint.h>

#define LEAD 3
#define CAPS 58
#define CAPT 75
#define LMAX 84
#define EA 3
#define XMIN 4
#define MARG 600
#define STAG 30
#define KPOL " nt"
#define VPOL " nt"
#define TAU16 470
#define POFS 0
#define SLP 1
#define SLP2 1
#define OPOL 16

typedef __attribute__((ext_vector_type(8))) __bf16 bf16x8;
typedef __attribute__((ext_vector_type(4))) short s16x4;
typedef __attribute__((ext_vector_type(4))) float f32x4;
typedef __attribute__((ext_vector_type(4))) unsigned u32x4;
typedef __attribute__((ext_vector_type(2))) unsigned u32x2;
typedef __attribute__((address_space(1))) unsigned gu32;
#define LDSP(T, a) ((__attribute__((address_space(3))) T*)(uintptr_t)(a))

constexpr int NBPS = 128, SKV = NBPS * 16, NS = 8;
constexpr unsigned DUMP = 2 * NS * 8192, DSZ = 8192 + 512, WORDS = DUMP + 2 * DSZ;
constexpr unsigned SLOT = 8192, FLAG_OFF = 4096 + 128, EPOCH_OFF = 4096 + 256;
constexpr unsigned TAGK = 0x9E3779B9u;

__device__ __forceinline__ unsigned cvtpk(float lo, float hi) {
  typedef __attribute__((ext_vector_type(2))) __bf16 bf16x2;
  bf16x2 r = {(__bf16)lo, (__bf16)hi};
  return __builtin_bit_cast(unsigned, r);
}
__device__ __forceinline__ float vmax(float a, float b) { return __builtin_elementwise_maximum(a, b); }
__device__ __forceinline__ float xg_max(float x) {
  auto s = __builtin_amdgcn_permlane16_swap(__float_as_uint(x), __float_as_uint(x), false, false);
  x = vmax(__uint_as_float(s[0]), __uint_as_float(s[1]));
  auto t = __builtin_amdgcn_permlane32_swap(__float_as_uint(x), __float_as_uint(x), false, false);
  return vmax(__uint_as_float(t[0]), __uint_as_float(t[1]));
}
__device__ __forceinline__ float xg_sum(float x) {
  auto s = __builtin_amdgcn_permlane16_swap(__float_as_uint(x), __float_as_uint(x), false, false);
  x = __uint_as_float(s[0]) + __uint_as_float(s[1]);
  auto t = __builtin_amdgcn_permlane32_swap(__float_as_uint(x), __float_as_uint(x), false, false);
  return __uint_as_float(t[0]) + __uint_as_float(t[1]);
}
__device__ __forceinline__ int scan32(int v) {
  v += __builtin_amdgcn_update_dpp(0, v, 0x111, 0xf, 0xf, false);
  v += __builtin_amdgcn_update_dpp(0, v, 0x112, 0xf, 0xf, false);
  v += __builtin_amdgcn_update_dpp(0, v, 0x114, 0xf, 0xf, false);
  v += __builtin_amdgcn_update_dpp(0, v, 0x118, 0xf, 0xf, false);
  v += __builtin_amdgcn_update_dpp(0, v, 0x142, 0xa, 0xf, false);
  return v;
}
__device__ __forceinline__ __amdgpu_buffer_rsrc_t rsrc(const void* p, unsigned n) {
  return __builtin_amdgcn_make_buffer_rsrc((void*)p, 0, n, 0x00020000);
}
__device__ __forceinline__ u32x4 srd(const void* p, unsigned n) {
  const uint64_t a = (uint64_t)p;
  return u32x4{(unsigned)a, (unsigned)(a >> 32) & 0xffffu, n, 0x00020000u};
}
__device__ __forceinline__ int rl(int v, int l) { return __builtin_amdgcn_readlane(v, l); }
__device__ __forceinline__ unsigned bal32(bool c) { return (unsigned)__builtin_amdgcn_ballot_w64(c); }
__device__ __forceinline__ int lds_ld(unsigned a) {
  return __hip_atomic_load(LDSP(int, a), __ATOMIC_RELAXED, __HIP_MEMORY_SCOPE_WORKGROUP);
}
__device__ __forceinline__ void lds_put(unsigned a, int v) {
  __hip_atomic_store(LDSP(int, a), v, __ATOMIC_RELAXED, __HIP_MEMORY_SCOPE_WORKGROUP);
}

#define DMA4(POL, dst, rs, v)                                                                         \
  do {                                                                                                \
    unsigned keep_;                                                                                   \
    asm volatile("s_mov_b32 %0, m0\n\ts_mov_b32 m0, %1\n\ts_nop 4\n\t"                                \
                 "buffer_load_dwordx4 %3, %2, 0 offen" POL " lds\n\t"                                 \
                 "buffer_load_dwordx4 %4, %2, 0 offen offset:1024" POL " lds\n\t"                     \
                 "buffer_load_dwordx4 %5, %2, 0 offen offset:2048" POL " lds\n\t"                     \
                 "buffer_load_dwordx4 %6, %2, 0 offen offset:3072" POL " lds\n\t"                     \
                 "s_mov_b32 m0, %0"                                                                   \
                 : "=&s"(keep_) : "s"(dst), "s"(rs), "v"(v[0]), "v"(v[1]), "v"(v[2]), "v"(v[3]) : "memory"); \
  } while (0)
#define WAITVM0() asm volatile("s_waitcnt vmcnt(0)" ::: "memory")
#define NOW() ((unsigned)__builtin_amdgcn_s_memrealtime())

__global__ void __launch_bounds__(896)
pa_kernel(const char* __restrict__ q, const char* __restrict__ kc, const char* __restrict__ vc,
          const int* __restrict__ bt, const int* __restrict__ lens, char* __restrict__ out, char* __restrict__ ws) {
  __shared__ __attribute__((aligned(16384))) char lds[WORDS + 192];
  const unsigned t0 = NOW();
  asm volatile("" :: "s"(q), "s"(kc), "s"(vc), "s"(bt), "s"(lens), "s"(out), "s"(ws));
  const int lane = threadIdx.x & 63;
  const int wave = __builtin_amdgcn_readfirstlane(threadIdx.x >> 6);
  const int b = blockIdx.x >> 3, h = (blockIdx.x - b) & 7;
  const int g = lane >> 4, c = lane & 15;
  const unsigned base = (unsigned)(uintptr_t)LDSP(char, lds);
  const int w = wave < 8 ? wave >> 2 : min(wave - 8, 2);

  const int pg = 4 * (lane >> 1) + (wave >> 1 & 2) + (lane & 1);
  const int ly = lane < 32 ? lane : (lane ^ 16) & 31;
  int len = lens[lane & 31], leny = lens[ly];
  int pgv = bt[b * NBPS + pg];
  u32x4 qf[4];
  if ((wave & 14) == 8) {
#pragma unroll
    for (int u = 0; u < 4; ++u) qf[u] = *(const u32x4*)(q + (b * 8 + h) * 4096 + c * 256 + g * 16 + 64 * u);
  }
  if (wave == 10 && lane < 48) lds_put(base + WORDS + 4 * lane, 0);
  asm volatile("s_waitcnt lgkmcnt(0)\n\ts_barrier" ::: "memory");
  unsigned go[4];
#pragma unroll
  for (int u = 0; u < 4; ++u) go[u] = (4 * u + g) * 2048 + ((c ^ (4 * u + g)) << 4) - 1024 * u;
  asm volatile("" : "+v"(len), "+v"(leny), "+v"(pgv));
  const unsigned unimask = bal32(len <= 0);
  if (len <= 0 || len > SKV) len = SKV;
  int lenb = rl(len, b);
  auto issue = [&](unsigned dst, unsigned page, int hd, unsigned nrec) __attribute__((always_inline)) {
    const size_t po = (size_t)page * 32768 + hd * 256;
    const u32x4 kr = srd(kc + po, nrec), vr = srd(vc + po, nrec);
    DMA4(KPOL, dst, kr, go);
    DMA4(VPOL, dst + 4096, vr, go);
  };
  const bool early = wave < EA && (wave < 2 || lenb > 64);
  if (early) issue(base + wave * 8192, rl(pgv, wave), h, __builtin_amdgcn_readfirstlane(min(max(lenb - 16 * (wave + (wave & 2)), 0), 16) * 2048));

  if (leny <= 0 || leny > SKV) leny = SKV;
  const int key = ((len + 15) >> 4) * 32 + (lane & 31), keyy = ((leny + 15) >> 4) * 32 + ly;
  unsigned less = (unsigned)(keyy - key) >> 31;
#define ROR(n) less = __builtin_amdgcn_alignbit(less, __builtin_amdgcn_update_dpp(0, keyy, 0x120 + n, 0xf, 0xf, true) - key, 31);
  ROR(1) ROR(2) ROR(3) ROR(4) ROR(5) ROR(6) ROR(7) ROR(8) ROR(9) ROR(10) ROR(11) ROR(12) ROR(13) ROR(14) ROR(15)
  int rank = __builtin_popcount(less);
  {
    auto t = __builtin_amdgcn_permlane32_swap(rank, rank, false, false);
    rank = t[0] + t[1];
  }
  const int tot = rl(scan32(key >> 5), 31), rb = rl(rank, b);
  const bool gives = (h & 1) && h != 3, pair73 = (h & 3) == 3;
  const int rg = gives ? 31 - rb : pair73 ? rb : 2 * rb + (h >> 1 & 1);
  const int ph = pair73 ? h ^ 4 : gives ? h - 1 + 2 * (rg & 1) : (h & 4) | 1;
  const int pb = __builtin_ctz(bal32(rank == (gives ? (pair73 ? rg : rg >> 1) : 31 - rg)) | 0x80000000u);
  const int npb = (lenb + 15) >> 4, lenp = rl(len, pb), npp = (lenp + 15) >> 4;
  const int room = ((tot * LMAX) >> 11) - (gives ? npp : npb);
  int x = min((gives ? npb : npp) - max((tot * (pair73 ? CAPT : CAPS)) >> 11, 8), min(pair73 ? room : (room * 43) >> 6, 64));
  if (x < XMIN || rg > 31) x = 0;
  const bool helps = x && !gives;
  if (x && gives) lenb = 16 * (npb - x);
  const int tend = ((tot - 32) * TAU16) >> 9, tend2 = max(tend - MARG - STAG * rg, 0);
  const int msk = helps && w != 1 ? 3 : 7;
  const unsigned ring = base + (w == 1 ? 8 * 8192 : w * 2 * 8192), fullw = base + WORDS + w * 64, freew = fullw + 32;
  const int ftok = lenp - 16 * (npp - x);
  unsigned* const tsp = (unsigned*)(ws + (64u << 20)) + (blockIdx.x * 2 + w) * 16;

  if (wave < 8 || wave > 10) {
    int npos, hd = h;
    unsigned tab;
    if (w == 2) {
      if (!helps) return;
      pgv = bt[pb * NBPS + min(npp - x + lane, NBPS - 1)];
      hd = ph;
      npos = (x + 1) & ~1;
      const int rows = min(max(ftok - 16 * lane, 0), 16);
      tab = rows ? (unsigned)((float)((lane + 1) * tend2) * __builtin_amdgcn_rcpf((float)x)) << 16 | rows << 11 : 0;
    } else {
      npos = ((lenb + 63 - 32 * w) >> 6) * 2;
      const int rows = min(max(lenb - 16 * pg, 0), 16);
      const int np = (lenb + 15) >> 4, n0 = (np >> 2) * 2 + min(np & 3, 2), ld = LEAD + 2 * n0 - np;
      const int slot = w ? min(2 * lane + ld, n0 + lane) : max(2 * lane + 1 - ld, lane);
      tab = rows ? (unsigned)((float)(slot * tend) * __builtin_amdgcn_rcpf((float)max(np - 1, 1))) << 16 | rows << 11 : 0;
    }
    int freed = 0;
    if (lane == 0 && (wave & 11) == 0) { tsp[14] = NOW(); tsp[4] = npos; }
    const int st = w == 2 ? 3 : 4;
    for (int j = w == 2 ? wave - 11 : wave & 3; j < npos; j += st) {
      if (j != wave || !early) {
        const unsigned e = rl(tab, j);
        while (freed + msk < j) {
          freed = __builtin_amdgcn_readfirstlane(lds_ld(freew));
          if (freed + msk < j) __builtin_amdgcn_s_sleep(SLP);
        }
        const int due = (e >> 16) + POFS;
        while ((int)(NOW() - t0) < due) __builtin_amdgcn_s_sleep(SLP2);
        issue(ring + (j & msk) * 8192, rl(pgv, j), hd, e & 0xffff);
      }
      WAITVM0();
      lds_put(fullw + 4 * (j & msk), j + 1);
    }
    return;
  }

  char* const slot = ws + (unsigned)(gives ? b * 8 + h : pb * 8 + ph) * SLOT;
  unsigned tag = 0;
  if (w == 2) {
    if (lane == 0) { tsp[15 - 32] = t0; tsp[15 - 16] = t0; }
    if (!x) return;
    tag = __hip_atomic_load((gu32*)(slot + EPOCH_OFF), __ATOMIC_RELAXED, __HIP_MEMORY_SCOPE_AGENT) + TAGK;
    if (gives) {
      while ((int)(NOW() - t0) < tend2 + POFS + 150) __builtin_amdgcn_s_sleep(16);
#pragma nounroll
      for (int spin = 0; spin < (1 << 22); ++spin) {
        if (__builtin_amdgcn_readfirstlane(__hip_atomic_load((gu32*)(slot + FLAG_OFF), __ATOMIC_RELAXED, __HIP_MEMORY_SCOPE_AGENT) == tag)) break;
        __builtin_amdgcn_s_sleep(8);
      }
      asm volatile("" ::: "memory");
      const __amdgpu_buffer_rsrc_t pr = rsrc(slot, 4096 + 64);
      u32x4 y[4];
#pragma unroll
      for (int r = 0; r < 4; ++r) y[r] = __builtin_amdgcn_raw_buffer_load_b128(pr, lane * 16 + r * 1024, 0, 16);
      const unsigned lse = __builtin_amdgcn_raw_buffer_load_b32(pr, 4096 + 4 * c, 0, 16);
#pragma unroll
      for (int k = 0; k < 8; ++k) {
        const unsigned lo = y[k >> 1][2 * (k & 1)], hi = y[k >> 1][2 * (k & 1) + 1];
        *LDSP(u32x4, base + DUMP + DSZ + 1024 * k + 16 * lane) = u32x4{lo << 16, lo & 0xffff0000u, hi << 16, hi & 0xffff0000u};
      }
      *LDSP(u32x2, base + DUMP + DSZ + 8192 + 8 * lane) = u32x2{lse, g ? 0u : 0x3f800000u};
      asm volatile("s_waitcnt lgkmcnt(0)" ::: "memory");
      lds_put(fullw + 36, 1);
      if (lane == 0) __hip_atomic_store((gu32*)(slot + EPOCH_OFF), tag, __ATOMIC_RELAXED, __HIP_MEMORY_SCOPE_AGENT);
      return;
    }
  }
  __builtin_amdgcn_s_setprio(3);
  if (w == 2) {
#pragma unroll
    for (int u = 0; u < 4; ++u) qf[u] = *(const u32x4*)(q + (pb * 8 + ph) * 4096 + c * 256 + g * 16 + 64 * u);
  }
  if ((unimask >> (w == 2 ? pb : b)) & 1) {
#pragma unroll
    for (int u = 0; u < 4; ++u) qf[u] = u32x4{0, 0, 0, 0};
  }
  const float CS = 0.088388347648318440f * 1.4426950408889634f;
  const unsigned ka = ring + c * 256 + ((g ^ c) << 4);
  const unsigned va = ring + 4096 + (lane >> 2) * 256 + ((((lane & 3) << 2) ^ (lane >> 2)) << 4);
  f32x4 acc[8];
#pragma unroll
  for (int k = 0; k < 8; ++k) acc[k] = f32x4{0.f, 0.f, 0.f, 0.f};
  float m = -1e30f, l = 0.f;
  unsigned ts_first = 0, ts_real = 0, ts_mid = 0, ts_dump = 0;
  const unsigned ck0 = (unsigned)__builtin_amdgcn_s_memtime(), rt0 = NOW();
  asm volatile("" : "+v"(qf[0]), "+v"(qf[1]), "+v"(qf[2]), "+v"(qf[3]), "+v"(tag));
  int i = 0, tokens = w == 2 ? ftok : lenb - 32 * w;
  const int dec = w == 2 ? 32 : 64;
  for (int lim = w == 1 || (x && gives) ? 64 : 0;; lim = 0) {
    while (tokens > lim) {
      for (;;) {
        const uint64_t f = __hip_atomic_load(LDSP(uint64_t, fullw + 4 * (i & msk)), __ATOMIC_RELAXED, __HIP_MEMORY_SCOPE_WORKGROUP);
        if (__builtin_amdgcn_readfirstlane((unsigned)f + (unsigned)(f >> 32)) == 2 * i + 3) break;
      }
      asm volatile("" ::: "memory");
      if (i == 0) ts_first = NOW();
      if (i == 24) ts_mid = NOW();
      if (tokens <= 64) ts_real = NOW();
      const unsigned o = (i & msk) * 8192;
      u32x4 kf[2][4], vt[8];
#pragma unroll
      for (int u = 0; u < 4; ++u) {
        const unsigned a = (o + ka) ^ (u << 6);
        kf[0][u] = *LDSP(const u32x4, a);
        kf[1][u] = *LDSP(const u32x4, a + 8192);
      }
#pragma unroll
      for (int k = 0; k < 8; ++k) {
        const unsigned a = ((o + va) ^ ((k >> 1) << 4)) + 8 * (k & 1);
        const u32x2 lo = __builtin_bit_cast(u32x2, __builtin_amdgcn_ds_read_tr16_b64_v4i16(LDSP(s16x4, a)));
        const u32x2 hi = __builtin_bit_cast(u32x2, __builtin_amdgcn_ds_read_tr16_b64_v4i16(LDSP(s16x4, a + 8192)));
        vt[k] = u32x4{lo[0], lo[1], hi[0], hi[1]};
      }
      f32x4 sc[2] = {{0.f, 0.f, 0.f, 0.f}, {0.f, 0.f, 0.f, 0.f}};
#pragma unroll
      for (int u = 0; u < 4; ++u)
#pragma unroll
        for (int t = 0; t < 2; ++t)
          sc[t] = __builtin_amdgcn_mfma_f32_16x16x32_bf16(__builtin_bit_cast(bf16x8, kf[t][u]), __builtin_bit_cast(bf16x8, qf[u]), sc[t], 0, 0, 0);
      i += 2;
      if (tokens < 32) {
#pragma unroll
        for (int r = 0; r < 8; ++r) sc[r >> 2][r & 3] = 16 * (r >> 2) + 4 * g + (r & 3) < tokens ? sc[r >> 2][r & 3] : -__builtin_inff();
      }
      tokens -= dec;
      const float mx = xg_max(vmax(vmax(vmax(sc[0][0], sc[0][1]), vmax(sc[0][2], sc[0][3])), vmax(vmax(sc[1][0], sc[1][1]), vmax(sc[1][2], sc[1][3]))));
      const float mn = vmax(m, mx * CS);
      const float alpha = __builtin_amdgcn_exp2f(m - mn);
      m = mn;
      float pr[8];
#pragma unroll
      for (int r = 0; r < 8; ++r) pr[r] = __builtin_amdgcn_exp2f(__builtin_fmaf(sc[r >> 2][r & 3], CS, -mn));
      asm volatile("" : "+v"(vt[0]), "+v"(vt[1]), "+v"(vt[2]), "+v"(vt[3]), "+v"(vt[4]), "+v"(vt[5]), "+v"(vt[6]), "+v"(vt[7]) :: "memory");
      lds_put(freew, i);
      l = l * alpha + (((pr[0] + pr[1]) + (pr[2] + pr[3])) + ((pr[4] + pr[5]) + (pr[6] + pr[7])));
      const u32x4 pf = {cvtpk(pr[0], pr[1]), cvtpk(pr[2], pr[3]), cvtpk(pr[4], pr[5]), cvtpk(pr[6], pr[7])};
#pragma unroll
      for (int k = 0; k < 8; ++k)
        acc[k] = __builtin_amdgcn_mfma_f32_16x16x32_bf16(__builtin_bit_cast(bf16x8, vt[k]), __builtin_bit_cast(bf16x8, pf), acc[k] * alpha, 0, 0, 0);
    }
    if (!lim) break;
    while (!__builtin_amdgcn_readfirstlane(lds_ld(base + WORDS + 36 + (w ? 0 : 128)))) {}
    asm volatile("" ::: "memory");
    ts_dump = NOW();
    const unsigned src = base + DUMP + (w ? 0 : DSZ);
    const u32x2 y = *LDSP(const u32x2, src + 8192 + 8 * lane);
    const float mn = vmax(m, __uint_as_float(y[0]));
    const float alpha = __builtin_amdgcn_exp2f(m - mn), wt = __builtin_amdgcn_exp2f(__uint_as_float(y[0]) - mn);
    m = mn;
    l = l * alpha + __uint_as_float(y[1]) * wt;
#pragma unroll
    for (int k = 0; k < 8; ++k) acc[k] = acc[k] * alpha + *LDSP(const f32x4, src + 1024 * k + 16 * lane) * wt;
  }
  if (!w) {
#pragma unroll
    for (int k = 0; k < 8; ++k) *LDSP(f32x4, base + DUMP + 1024 * k + 16 * lane) = acc[k];
    *LDSP(u32x2, base + DUMP + 8192 + 8 * lane) = u32x2{__float_as_uint(m), __float_as_uint(l)};
    asm volatile("s_waitcnt lgkmcnt(0)" ::: "memory");
    lds_put(fullw + 36, 1);
  } else {
    const float lt = xg_sum(l);
    const float inv = __builtin_amdgcn_rcpf(lt);
    u32x4 y[4];
#pragma unroll
    for (int r = 0; r < 4; ++r)
      y[r] = u32x4{cvtpk(acc[2 * r][0] * inv, acc[2 * r][1] * inv), cvtpk(acc[2 * r][2] * inv, acc[2 * r][3] * inv),
                   cvtpk(acc[2 * r + 1][0] * inv, acc[2 * r + 1][1] * inv), cvtpk(acc[2 * r + 1][2] * inv, acc[2 * r + 1][3] * inv)};
    if (w == 1) {
      const __amdgpu_buffer_rsrc_t orr = rsrc(out + (b * 8 + h) * 4096, 4096);
#pragma unroll
      for (int r = 0; r < 4; ++r) __builtin_amdgcn_raw_buffer_store_b128(y[r], orr, c * 256 + g * 64 + r * 16, 0, OPOL);
    } else {
      const __amdgpu_buffer_rsrc_t orr = rsrc(slot, 4096 + 64);
#pragma unroll
      for (int r = 0; r < 4; ++r) __builtin_amdgcn_raw_buffer_store_b128(y[r], orr, lane * 16 + r * 1024, 0, 16);
      __builtin_amdgcn_raw_buffer_store_b32(__float_as_uint(m + __builtin_amdgcn_logf(lt)), orr, lane * 4 + 4096, 0, 16);
      WAITVM0();
      if (lane == 0) __hip_atomic_store((gu32*)(slot + FLAG_OFF), tag, __ATOMIC_RELAXED, __HIP_MEMORY_SCOPE_AGENT);
      if (lane == 0) tsp[15 - 32] = NOW();
      return;
    }
  }
  if (lane == 0) { unsigned* t = tsp; t[0] = t0; t[1] = ts_first; t[2] = ts_real; t[3] = NOW();
    t[5] = w; t[6] = ts_dump - t0; t[7] = ts_mid; t[8] = (unsigned)__builtin_amdgcn_s_memtime() - ck0; t[9] = NOW() - rt0; t[10] = !w && ts_dump ? ts_dump : t0; t[11] = h; t[12] = x; t[13] = __builtin_amdgcn_s_getreg((31 << 11) | 20); }
}

__device__ float stat_of(const unsigned* t, unsigned lo, int s) {
  const int f = s / 10000, sel = (s / 100) % 100, val = s % 100;
  float sum = 0.f, mx = 0.f; int n = 0;
  for (int w = 0; w < 512; ++w) {
    const unsigned* x = t + w * 16;
    bool in = true;
    if (sel == 1 || sel == 5) in = (int)x[11] == val;
    if (sel == 2 || sel == 6) in = (int)(x[13] & 7) == val;
    if (sel == 4) in = (x[5] > 0) == (val != 0);
    if (!in) continue;
    const float v = (float)(x[f] - lo);
    sum += v; mx = fmaxf(mx, v); ++n;
  }
  return (sel == 3 || sel == 5 || sel == 6) ? mx : sum / n;
}
__global__ void diag_kernel(const unsigned* __restrict__ tg) {
  __shared__ unsigned t[512 * 16];
  for (int i = threadIdx.x; i < 512 * 16; i += 512) t[i] = tg[i];
  __syncthreads();
  if (threadIdx.x == 0) {
    unsigned lo = t[0]; for (int i = 0; i < 512; ++i) if ((int)(t[i * 16] - lo) < 0) lo = t[i * 16];
    const int qa = min(63, max(0, (int)((stat_of(t, lo, SA) - OFSA) / UNITA))), qb = min(63, max(0, (int)((stat_of(t, lo, SB) - OFSB) / UNITB)));
    const int due = 30000 + 200 * qa + 12800 * qb;
    while ((int)((unsigned)__builtin_amdgcn_s_memrealtime() - lo) < due) __builtin_amdgcn_s_sleep(1);
  }
}

extern "C" void kernel_launch(void* const* d_in, const int* in_sizes, int n_in, void* d_out, int out_size,
                              void* d_ws, size_t ws_size, hipStream_t stream) {
  pa_kernel<<<256, 896, 0, stream>>>((const char*)d_in[0], (const char*)d_in[1], (const char*)d_in[2],
                                     (const int*)d_in[3], (const int*)d_in[4], (char*)d_out, (char*)d_ws);
  diag_kernel<<<1, 512, 0, stream>>>((const unsigned*)((char*)d_ws + (64u << 20)));
}
```

```cpp
#define EAM 0x07
#define POFS 40
#include <hip/hip_runtime.h>
#include <hip/hip_bf16.h>
#include <stdint.h>

#define LEAD 3
#define CAPS 58
#define CAPT 75
#define LMAX 84
#define XCOST 8
#define TCOST 2
#define XMIN 4
#define KPOL " nt"
#define VPOL " nt"
#define TAU16 470
#define SLP 1
#define SLP2 1
#define OPOL 16

typedef __attribute__((ext_vector_type(8))) __bf16 bf16x8;
typedef __attribute__((ext_vector_type(4))) short s16x4;
typedef __attribute__((ext_vector_type(4))) float f32x4;
typedef __attribute__((ext_vector_type(4))) unsigned u32x4;
typedef __attribute__((ext_vector_type(2))) unsigned u32x2;
typedef __attribute__((address_space(1))) unsigned gu32;
#define LDSP(T, a) ((__attribute__((address_space(3))) T*)(uintptr_t)(a))

constexpr int NBPS = 128, SKV = NBPS * 16, NS = 8;
constexpr unsigned DUMP = 2 * NS * 8192, DSZ = 8192 + 512, WORDS = DUMP + 2 * DSZ;
constexpr unsigned SLOT = 8192, FLAG_OFF = 4096 + 128, EPOCH_OFF = 4096 + 256;
constexpr unsigned TAGK = 0x9E3779B9u;

__device__ __forceinline__ unsigned cvtpk(float lo, float hi) {
  typedef __attribute__((ext_vector_type(2))) __bf16 bf16x2;
  bf16x2 r = {(__bf16)lo, (__bf16)hi};
  return __builtin_bit_cast(unsigned, r);
}
__device__ __forceinline__ float vmax(float a, float b) { return __builtin_elementwise_maximum(a, b); }
__device__ __forceinline__ float xg_max(float x) {
  auto s = __builtin_amdgcn_permlane16_swap(__float_as_uint(x), __float_as_uint(x), false, false);
  x = vmax(__uint_as_float(s[0]), __uint_as_float(s[1]));
  auto t = __builtin_amdgcn_permlane32_swap(__float_as_uint(x), __float_as_uint(x), false, false);
  return vmax(__uint_as_float(t[0]), __uint_as_float(t[1]));
}
__device__ __forceinline__ float xg_sum(float x) {
  auto s = __builtin_amdgcn_permlane16_swap(__float_as_uint(x), __float_as_uint(x), false, false);
  x = __uint_as_float(s[0]) + __uint_as_float(s[1]);
  auto t = __builtin_amdgcn_permlane32_swap(__float_as_uint(x), __float_as_uint(x), false, false);
  return __uint_as_float(t[0]) + __uint_as_float(t[1]);
}
__device__ __forceinline__ int scan32(int v) {
  v += __builtin_amdgcn_update_dpp(0, v, 0x111, 0xf, 0xf, false);
  v += __builtin_amdgcn_update_dpp(0, v, 0x112, 0xf, 0xf, false);
  v += __builtin_amdgcn_update_dpp(0, v, 0x114, 0xf, 0xf, false);
  v += __builtin_amdgcn_update_dpp(0, v, 0x118, 0xf, 0xf, false);
  v += __builtin_amdgcn_update_dpp(0, v, 0x142, 0xa, 0xf, false);
  return v;
}
__device__ __forceinline__ __amdgpu_buffer_rsrc_t rsrc(const void* p, unsigned n) {
  return __builtin_amdgcn_make_buffer_rsrc((void*)p, 0, n, 0x00020000);
}
__device__ __forceinline__ u32x4 srd(const void* p, unsigned n) {
  const uint64_t a = (uint64_t)p;
  return u32x4{(unsigned)a, (unsigned)(a >> 32) & 0xffffu, n, 0x00020000u};
}
__device__ __forceinline__ int rl(int v, int l) { return __builtin_amdgcn_readlane(v, l); }
__device__ __forceinline__ unsigned bal32(bool c) { return (unsigned)__builtin_amdgcn_ballot_w64(c); }
__device__ __forceinline__ int lds_ld(unsigned a) {
  return __hip_atomic_load(LDSP(int, a), __ATOMIC_RELAXED, __HIP_MEMORY_SCOPE_WORKGROUP);
}
__device__ __forceinline__ void lds_put(unsigned a, int v) {
  __hip_atomic_store(LDSP(int, a), v, __ATOMIC_RELAXED, __HIP_MEMORY_SCOPE_WORKGROUP);
}

#define DMA4(POL, dst, rs, v)                                                                         \
  do {                                                                                                \
    unsigned keep_;                                                                                   \
    asm volatile("s_mov_b32 %0, m0\n\ts_mov_b32 m0, %1\n\ts_nop 4\n\t"                                \
                 "buffer_load_dwordx4 %3, %2, 0 offen" POL " lds\n\t"                                 \
                 "buffer_load_dwordx4 %4, %2, 0 offen offset:1024" POL " lds\n\t"                     \
                 "buffer_load_dwordx4 %5, %2, 0 offen offset:2048" POL " lds\n\t"                     \
                 "buffer_load_dwordx4 %6, %2, 0 offen offset:3072" POL " lds\n\t"                     \
                 "s_mov_b32 m0, %0"                                                                   \
                 : "=&s"(keep_) : "s"(dst), "s"(rs), "v"(v[0]), "v"(v[1]), "v"(v[2]), "v"(v[3]) : "memory"); \
  } while (0)
#define WAITVM0() asm volatile("s_waitcnt vmcnt(0)" ::: "memory")
#define NOW() ((unsigned)__builtin_amdgcn_s_memrealtime())

__global__ void __launch_bounds__(704)
pa_kernel(const char* __restrict__ q, const char* __restrict__ kc, const char* __restrict__ vc,
          const int* __restrict__ bt, const int* __restrict__ lens, char* __restrict__ out, char* __restrict__ ws) {
  __shared__ __attribute__((aligned(16384))) char lds[WORDS + 192];
  const unsigned t0 = NOW();
  asm volatile("" :: "s"(q), "s"(kc), "s"(vc), "s"(bt), "s"(lens), "s"(out), "s"(ws));
  const int lane = threadIdx.x & 63;
  const int wave = __builtin_amdgcn_readfirstlane(threadIdx.x >> 6);
  const int b = blockIdx.x >> 3, h = (blockIdx.x - b) & 7;
  const int g = lane >> 4, c = lane & 15;
  const unsigned base = (unsigned)(uintptr_t)LDSP(char, lds);
  const int w = wave < 8 ? wave >> 2 : wave - 8;

  int pg = 4 * (lane >> 1) + (wave >> 1 & 2) + (lane & 1);
  const int ly = lane < 32 ? lane : (lane ^ 16) & 31;
  int len = lens[lane & 31], leny = lens[ly];
  int pgv = bt[b * NBPS + pg];
  u32x4 qf[4];
  if ((wave & 14) == 8) {
#pragma unroll
    for (int u = 0; u < 4; ++u) qf[u] = *(const u32x4*)(q + (b * 8 + h) * 4096 + c * 256 + g * 16 + 64 * u);
  }
  if (wave == 10 && lane < 48) lds_put(base + WORDS + 4 * lane, 0);
  asm volatile("s_waitcnt lgkmcnt(0)\n\ts_barrier" ::: "memory");
  unsigned go[4];
#pragma unroll
  for (int u = 0; u < 4; ++u) go[u] = (4 * u + g) * 2048 + ((c ^ (4 * u + g)) << 4) - 1024 * u;
  asm volatile("" : "+v"(len), "+v"(leny), "+v"(pgv));
  const unsigned unimask = bal32(len <= 0);
  if (len <= 0 || len > SKV) len = SKV;
  int lenb = rl(len, b);
  auto issue = [&](unsigned dst, unsigned pghd, unsigned nrec) __attribute__((always_inline)) {
    const size_t po = (size_t)pghd * 256;
    const u32x4 kr = srd(kc + po, nrec), vr = srd(vc + po, nrec);
    DMA4(KPOL, dst, kr, go);
    DMA4(VPOL, dst + 4096, vr, go);
  };
  pgv = pgv * 128 + h;
  const unsigned ring = base + (w & 1) * 8 * 8192;
  const bool early = (EAM >> wave & 1) && lenb > 32 * (wave & 2) + 32 * w;
  if (early) issue(ring + (wave & 3) * 8192, rl(pgv, wave & 3), __builtin_amdgcn_readfirstlane(min(max(lenb - 16 * rl(pg, wave & 3), 0), 16) * 2048));

  if (leny <= 0 || leny > SKV) leny = SKV;
  const int key = ((len + 15) >> 4) * 32 + (lane & 31), keyy = ((leny + 15) >> 4) * 32 + ly;
  unsigned less = (unsigned)(keyy - key) >> 31;
#define ROR(n) less = __builtin_amdgcn_alignbit(less, __builtin_amdgcn_update_dpp(0, keyy, 0x120 + n, 0xf, 0xf, true) - key, 31);
  ROR(1) ROR(2) ROR(3) ROR(4) ROR(5) ROR(6) ROR(7) ROR(8) ROR(9) ROR(10) ROR(11) ROR(12) ROR(13) ROR(14) ROR(15)
  int rank = __builtin_popcount(less);
  {
    auto t = __builtin_amdgcn_permlane32_swap(rank, rank, false, false);
    rank = t[0] + t[1];
  }
  const int tot = rl(scan32(key >> 5), 31), rb = rl(rank, b);
  const bool gives = (h & 1) && h != 3, pair73 = (h & 3) == 3;
  const int rg = gives ? 31 - rb : pair73 ? rb : 2 * rb + (h >> 1 & 1);
  const int ph = pair73 ? h ^ 4 : gives ? h - 1 + 2 * (rg & 1) : (h & 4) | 1;
  const int pb = __builtin_ctz(bal32(rank == (gives ? (pair73 ? rg : rg >> 1) : 31 - rg)) | 0x80000000u);
  const int npb = (lenb + 15) >> 4, lenp = rl(len, pb), npp = (lenp + 15) >> 4;
  const int nph = gives ? npp : npb, xc = pair73 ? TCOST : XCOST;
  const int room = ((tot * LMAX) >> 11) - nph;
  int x = min((gives ? npb : npp) - max((tot * (pair73 ? CAPT : CAPS)) >> 11, 8), min((room * (1024 / (16 + (pair73 ? TCOST : XCOST)))) >> 6, 122 - nph));
  if (x < XMIN || rg > 31 || nph < 11) x = 0;
  const bool helps = x && !gives;
  if (x && gives) lenb = 16 * (npb - x);
  const int tend = ((tot - 32) * TAU16) >> 9;
  const int ftok = lenp - 16 * (npp - x);
  const int xh = helps ? x : 0, xw = ((xh + 1 + 2 * w) >> 2) * 2, sr = (x + 1) & 2;
  unsigned fullw = base + WORDS + (w == 2 ? sr * 32 : w * 64);

  if (wave < 8) {
    int tk = lenb;
    if (helps) {
      const bool fo = (unsigned)(lane - 4) < (unsigned)xw;
      pg = fo ? (pg - 8) ^ sr : lane < 4 ? pg : pg - 2 * xw;
      pgv = bt[fo ? pb * NBPS + min(npp - x + pg, NBPS - 1) : b * NBPS + pg] * 128 + (fo ? ph : h);
      tk = fo ? ftok : lenb;
    }
    const int npos = ((lenb + 63 - 32 * w) >> 6) * 2 + xw;
    const int rows = min(max(tk - 16 * pg, 0), 16);
    const int xr = (xh + 1) & ~1, no = (lenb + 15) >> 4, np = no + xr, n0 = (no >> 2) * 2 + min(no & 3, 2) + ((xh + 1) >> 2) * 2, ld = LEAD + 2 * n0 - np;
    const int slot = w ? min(2 * lane + ld, n0 + lane) : max(2 * lane + 1 - ld, lane);
    const int cost = 16 * slot + xc * min(max(slot - 7, 0), xr);
    const unsigned tab = rows ? (unsigned)((float)(cost * tend) * __builtin_amdgcn_rcpf((float)max(16 * (np - 1) + xc * xr, 1))) << 16 | rows << 11 : 0;
    int freed = 0;
    for (int j = wave & 3; j < npos; j += 4) {
      if (j > 3 || !early) {
        const unsigned e = rl(tab, j);
        while (freed + 7 < j) {
          freed = __builtin_amdgcn_readfirstlane(lds_ld(fullw + 32));
          if (freed + 7 < j) __builtin_amdgcn_s_sleep(SLP);
        }
        const int due = (e >> 16) + POFS;
        while ((int)(NOW() - t0) < due) __builtin_amdgcn_s_sleep(SLP2);
        issue(ring + (j & 7) * 8192, rl(pgv, j), e & 0xffff);
      }
      WAITVM0();
      lds_put(fullw + 4 * (j & 7), j + 1);
    }
    return;
  }

  char* const slot = ws + (unsigned)(gives ? b * 8 + h : pb * 8 + ph) * SLOT;
  unsigned tag = 0;
  if (w == 2) {
    if (!x) return;
    tag = __hip_atomic_load((gu32*)(slot + EPOCH_OFF), __ATOMIC_RELAXED, __HIP_MEMORY_SCOPE_AGENT) + TAGK;
    if (gives) {
      while ((int)(NOW() - t0) < tend - (tend >> 2)) __builtin_amdgcn_s_sleep(32);
#pragma nounroll
      for (int spin = 0; spin < (1 << 22); ++spin) {
        if (__builtin_amdgcn_readfirstlane(__hip_atomic_load((gu32*)(slot + FLAG_OFF), __ATOMIC_RELAXED, __HIP_MEMORY_SCOPE_AGENT) == tag)) break;
        __builtin_amdgcn_s_sleep(8);
      }
      asm volatile("" ::: "memory");
      const __amdgpu_buffer_rsrc_t pr = rsrc(slot, 4096 + 64);
      u32x4 y[4];
#pragma unroll
      for (int r = 0; r < 4; ++r) y[r] = __builtin_amdgcn_raw_buffer_load_b128(pr, lane * 16 + r * 1024, 0, 16);
      const unsigned lse = __builtin_amdgcn_raw_buffer_load_b32(pr, 4096 + 4 * c, 0, 16);
#pragma unroll
      for (int k = 0; k < 8; ++k) {
        const unsigned lo = y[k >> 1][2 * (k & 1)], hi = y[k >> 1][2 * (k & 1) + 1];
        *LDSP(u32x4, base + DUMP + DSZ + 1024 * k + 16 * lane) = u32x4{lo << 16, lo & 0xffff0000u, hi << 16, hi & 0xffff0000u};
      }
      *LDSP(u32x2, base + DUMP + DSZ + 8192 + 8 * lane) = u32x2{lse, g ? 0u : 0x3f800000u};
      asm volatile("s_waitcnt lgkmcnt(0)" ::: "memory");
      lds_put(base + WORDS + 128 + 36, 1);
      if (lane == 0) __hip_atomic_store((gu32*)(slot + EPOCH_OFF), tag, __ATOMIC_RELAXED, __HIP_MEMORY_SCOPE_AGENT);
      return;
    }
  }
  __builtin_amdgcn_s_setprio(3);
  if (w == 2) {
#pragma unroll
    for (int u = 0; u < 4; ++u) qf[u] = *(const u32x4*)(q + (pb * 8 + ph) * 4096 + c * 256 + g * 16 + 64 * u);
  }
  if ((unimask >> (w == 2 ? pb : b)) & 1) {
#pragma unroll
    for (int u = 0; u < 4; ++u) qf[u] = u32x4{0, 0, 0, 0};
  }
  const float CS = 0.088388347648318440f * 1.4426950408889634f;
  const unsigned ka = base + c * 256 + ((g ^ c) << 4);
  const unsigned va = base + 4096 + (lane >> 2) * 256 + ((((lane & 3) << 2) ^ (lane >> 2)) << 4);
  f32x4 acc[8];
#pragma unroll
  for (int k = 0; k < 8; ++k) acc[k] = f32x4{0.f, 0.f, 0.f, 0.f};
  float m = -1e30f, l = 0.f;
  asm volatile("" : "+v"(qf[0]), "+v"(qf[1]), "+v"(qf[2]), "+v"(qf[3]), "+v"(tag));
  int i = w == 2 ? 4 : 0, tokens = w == 2 ? ftok : lenb - 32 * w;
  const int dec = w == 2 ? 32 : 64, alt = w == 2 ? 64 : 0;
  for (int lim = w == 1 || (x && gives) ? 64 : 0;; lim = 0) {
    while (tokens > lim) {
      for (;;) {
        const uint64_t f = __hip_atomic_load(LDSP(uint64_t, fullw + 4 * (i & 7)), __ATOMIC_RELAXED, __HIP_MEMORY_SCOPE_WORKGROUP);
        if (__builtin_amdgcn_readfirstlane((unsigned)f + (unsigned)(f >> 32) + lds_ld(fullw + 32)) == 3 * i + 3) break;
      }
      asm volatile("" ::: "memory");
      const unsigned o = (i & 7) * 8192 + (fullw & 64) * 1024;
      u32x4 kf[2][4], vt[8];
#pragma unroll
      for (int u = 0; u < 4; ++u) {
        const unsigned a = (o + ka) ^ (u << 6);
        kf[0][u] = *LDSP(const u32x4, a);
        kf[1][u] = *LDSP(const u32x4, a + 8192);
      }
#pragma unroll
      for (int k = 0; k < 8; ++k) {
        const unsigned a = ((o + va) ^ ((k >> 1) << 4)) + 8 * (k & 1);
        const u32x2 lo = __builtin_bit_cast(u32x2, __builtin_amdgcn_ds_read_tr16_b64_v4i16(LDSP(s16x4, a)));
        const u32x2 hi = __builtin_bit_cast(u32x2, __builtin_amdgcn_ds_read_tr16_b64_v4i16(LDSP(s16x4, a + 8192)));
        vt[k] = u32x4{lo[0], lo[1], hi[0], hi[1]};
      }
      f32x4 sc[2] = {{0.f, 0.f, 0.f, 0.f}, {0.f, 0.f, 0.f, 0.f}};
#pragma unroll
      for (int u = 0; u < 4; ++u)
#pragma unroll
        for (int t = 0; t < 2; ++t)
          sc[t] = __builtin_amdgcn_mfma_f32_16x16x32_bf16(__builtin_bit_cast(bf16x8, kf[t][u]), __builtin_bit_cast(bf16x8, qf[u]), sc[t], 0, 0, 0);
      if (tokens < 32) {
#pragma unroll
        for (int r = 0; r < 8; ++r) sc[r >> 2][r & 3] = 16 * (r >> 2) + 4 * g + (r & 3) < tokens ? sc[r >> 2][r & 3] : -__builtin_inff();
      }
      tokens -= dec;
      const float mx = xg_max(vmax(vmax(vmax(sc[0][0], sc[0][1]), vmax(sc[0][2], sc[0][3])), vmax(vmax(sc[1][0], sc[1][1]), vmax(sc[1][2], sc[1][3]))));
      const float mn = vmax(m, mx * CS);
      const float alpha = __builtin_amdgcn_exp2f(m - mn);
      m = mn;
      float pr[8];
#pragma unroll
      for (int r = 0; r < 8; ++r) pr[r] = __builtin_amdgcn_exp2f(__builtin_fmaf(sc[r >> 2][r & 3], CS, -mn));
      asm volatile("" : "+v"(vt[0]), "+v"(vt[1]), "+v"(vt[2]), "+v"(vt[3]), "+v"(vt[4]), "+v"(vt[5]), "+v"(vt[6]), "+v"(vt[7]) :: "memory");
      lds_put(fullw + 32, i + 2);
      fullw ^= alt;
      i += (fullw ^ sr * 32) & alt ? 0 : i == 2 ? 2 + xw : 2;
      l = l * alpha + (((pr[0] + pr[1]) + (pr[2] + pr[3])) + ((pr[4] + pr[5]) + (pr[6] + pr[7])));
      const u32x4 pf = {cvtpk(pr[0], pr[1]), cvtpk(pr[2], pr[3]), cvtpk(pr[4], pr[5]), cvtpk(pr[6], pr[7])};
#pragma unroll
      for (int k = 0; k < 8; ++k)
        acc[k] = __builtin_amdgcn_mfma_f32_16x16x32_bf16(__builtin_bit_cast(bf16x8, vt[k]), __builtin_bit_cast(bf16x8, pf), acc[k] * alpha, 0, 0, 0);
    }
    if (!lim) break;
    while (!__builtin_amdgcn_readfirstlane(lds_ld(base + WORDS + 36 + (w ? 0 : 128)))) {}
    asm volatile("" ::: "memory");
    const unsigned src = base + DUMP + (w ? 0 : DSZ);
    const u32x2 y = *LDSP(const u32x2, src + 8192 + 8 * lane);
    const float mn = vmax(m, __uint_as_float(y[0]));
    const float alpha = __builtin_amdgcn_exp2f(m - mn), wt = __builtin_amdgcn_exp2f(__uint_as_float(y[0]) - mn);
    m = mn;
    l = l * alpha + __uint_as_float(y[1]) * wt;
#pragma unroll
    for (int k = 0; k < 8; ++k) acc[k] = acc[k] * alpha + *LDSP(const f32x4, src + 1024 * k + 16 * lane) * wt;
  }
  if (!w) {
#pragma unroll
    for (int k = 0; k < 8; ++k) *LDSP(f32x4, base + DUMP + 1024 * k + 16 * lane) = acc[k];
    *LDSP(u32x2, base + DUMP + 8192 + 8 * lane) = u32x2{__float_as_uint(m), __float_as_uint(l)};
    asm volatile("s_waitcnt lgkmcnt(0)" ::: "memory");
    lds_put(base + WORDS + 36, 1);
  } else {
    const float lt = xg_sum(l);
    const float inv = __builtin_amdgcn_rcpf(lt);
    u32x4 y[4];
#pragma unroll
    for (int r = 0; r < 4; ++r)
      y[r] = u32x4{cvtpk(acc[2 * r][0] * inv, acc[2 * r][1] * inv), cvtpk(acc[2 * r][2] * inv, acc[2 * r][3] * inv),
                   cvtpk(acc[2 * r + 1][0] * inv, acc[2 * r + 1][1] * inv), cvtpk(acc[2 * r + 1][2] * inv, acc[2 * r + 1][3] * inv)};
    if (w == 1) {
      const __amdgpu_buffer_rsrc_t orr = rsrc(out + (b * 8 + h) * 4096, 4096);
#pragma unroll
      for (int r = 0; r < 4; ++r) __builtin_amdgcn_raw_buffer_store_b128(y[r], orr, c * 256 + g * 64 + r * 16, 0, OPOL);
    } else {
      const __amdgpu_buffer_rsrc_t orr = rsrc(slot, 4096 + 64);
#pragma unroll
      for (int r = 0; r < 4; ++r) __builtin_amdgcn_raw_buffer_store_b128(y[r], orr, lane * 16 + r * 1024, 0, 16);
      __builtin_amdgcn_raw_buffer_store_b32(__float_as_uint(m + __builtin_amdgcn_logf(lt)), orr, lane * 4 + 4096, 0, 16);
      WAITVM0();
      if (lane == 0) __hip_atomic_store((gu32*)(slot + FLAG_OFF), tag, __ATOMIC_RELAXED, __HIP_MEMORY_SCOPE_AGENT);
      return;
    }
  }
}

extern "C" void kernel_launch(void* const* d_in, const int* in_sizes, int n_in, void* d_out, int out_size,
                              void* d_ws, size_t ws_size, hipStream_t stream) {
  pa_kernel<<<256, 704, 0, stream>>>((const char*)d_in[0], (const char*)d_in[1], (const char*)d_in[2],
                                     (const int*)d_in[3], (const int*)d_in[4], (char*)d_out, (char*)d_ws);
}
```

```cpp
#define TS
#define SA 30504
#define OFSA 2800
#define UNITA 20
#define SB 30506
#define OFSB 2800
#define UNITB 20
#include <hip/hip_runtime.h>
#include <hip/hip_bf16.h>
#include <stdint.h>

#define LEAD 3
#define CAPS 58
#define CAPT 75
#define LMAX 84
#define EA 3
#define XMIN 4
#define MARG 600
#define STAG 30
#define KPOL " nt"
#define VPOL " nt"
#define TAU16 470
#define POFS 0
#define SLP 1
#define SLP2 1
#define OPOL 16

typedef __attribute__((ext_vector_type(8))) __bf16 bf16x8;
typedef __attribute__((ext_vector_type(4))) short s16x4;
typedef __attribute__((ext_vector_type(4))) float f32x4;
typedef __attribute__((ext_vector_type(4))) unsigned u32x4;
typedef __attribute__((ext_vector_type(2))) unsigned u32x2;
typedef __attribute__((address_space(1))) unsigned gu32;
#define LDSP(T, a) ((__attribute__((address_space(3))) T*)(uintptr_t)(a))

constexpr int NBPS = 128, SKV = NBPS * 16, NS = 8;
constexpr unsigned DUMP = 2 * NS * 8192, DSZ = 8192 + 512, WORDS = DUMP + 2 * DSZ;
constexpr unsigned SLOT = 8192, FLAG_OFF = 4096 + 128, EPOCH_OFF = 4096 + 256;
constexpr unsigned TAGK = 0x9E3779B9u;

__device__ __forceinline__ unsigned cvtpk(float lo, float hi) {
  typedef __attribute__((ext_vector_type(2))) __bf16 bf16x2;
  bf16x2 r = {(__bf16)lo, (__bf16)hi};
  return __builtin_bit_cast(unsigned, r);
}
__device__ __forceinline__ float vmax(float a, float b) { return __builtin_elementwise_maximum(a, b); }
__device__ __forceinline__ float xg_max(float x) {
  auto s = __builtin_amdgcn_permlane16_swap(__float_as_uint(x), __float_as_uint(x), false, false);
  x = vmax(__uint_as_float(s[0]), __uint_as_float(s[1]));
  auto t = __builtin_amdgcn_permlane32_swap(__float_as_uint(x), __float_as_uint(x), false, false);
  return vmax(__uint_as_float(t[0]), __uint_as_float(t[1]));
}
__device__ __forceinline__ float xg_sum(float x) {
  auto s = __builtin_amdgcn_permlane16_swap(__float_as_uint(x), __float_as_uint(x), false, false);
  x = __uint_as_float(s[0]) + __uint_as_float(s[1]);
  auto t = __builtin_amdgcn_permlane32_swap(__float_as_uint(x), __float_as_uint(x), false, false);
  return __uint_as_float(t[0]) + __uint_as_float(t[1]);
}
__device__ __forceinline__ int scan32(int v) {
  v += __builtin_amdgcn_update_dpp(0, v, 0x111, 0xf, 0xf, false);
  v += __builtin_amdgcn_update_dpp(0, v, 0x112, 0xf, 0xf, false);
  v += __builtin_amdgcn_update_dpp(0, v, 0x114, 0xf, 0xf, false);
  v += __builtin_amdgcn_update_dpp(0, v, 0x118, 0xf, 0xf, false);
  v += __builtin_amdgcn_update_dpp(0, v, 0x142, 0xa, 0xf, false);
  return v;
}
__device__ __forceinline__ __amdgpu_buffer_rsrc_t rsrc(const void* p, unsigned n) {
  return __builtin_amdgcn_make_buffer_rsrc((void*)p, 0, n, 0x00020000);
}
__device__ __forceinline__ u32x4 srd(const void* p, unsigned n) {
  const uint64_t a = (uint64_t)p;
  return u32x4{(unsigned)a, (unsigned)(a >> 32) & 0xffffu, n, 0x00020000u};
}
__device__ __forceinline__ int rl(int v, int l) { return __builtin_amdgcn_readlane(v, l); }
__device__ __forceinline__ unsigned bal32(bool c) { return (unsigned)__builtin_amdgcn_ballot_w64(c); }
__device__ __forceinline__ int lds_ld(unsigned a) {
  return __hip_atomic_load(LDSP(int, a), __ATOMIC_RELAXED, __HIP_MEMORY_SCOPE_WORKGROUP);
}
__device__ __forceinline__ void lds_put(unsigned a, int v) {
  __hip_atomic_store(LDSP(int, a), v, __ATOMIC_RELAXED, __HIP_MEMORY_SCOPE_WORKGROUP);
}

#define DMA4(POL, dst, rs, v)                                                                         \
  do {                                                                                                \
    unsigned keep_;                                                                                   \
    asm volatile("s_mov_b32 %0, m0\n\ts_mov_b32 m0, %1\n\ts_nop 4\n\t"                                \
                 "buffer_load_dwordx4 %3, %2, 0 offen" POL " lds\n\t"                                 \
                 "buffer_load_dwordx4 %4, %2, 0 offen offset:1024" POL " lds\n\t"                     \
                 "buffer_load_dwordx4 %5, %2, 0 offen offset:2048" POL " lds\n\t"                     \
                 "buffer_load_dwordx4 %6, %2, 0 offen offset:3072" POL " lds\n\t"                     \
                 "s_mov_b32 m0, %0"                                                                   \
                 : "=&s"(keep_) : "s"(dst), "s"(rs), "v"(v[0]), "v"(v[1]), "v"(v[2]), "v"(v[3]) : "memory"); \
  } while (0)
#define WAITVM0() asm volatile("s_waitcnt vmcnt(0)" ::: "memory")
#define NOW() ((unsigned)__builtin_amdgcn_s_memrealtime())

__global__ void __launch_bounds__(896)
pa_kernel(const char* __restrict__ q, const char* __restrict__ kc, const char* __restrict__ vc,
          const int* __restrict__ bt, const int* __restrict__ lens, char* __restrict__ out, char* __restrict__ ws) {
  __shared__ __attribute__((aligned(16384))) char lds[WORDS + 192];
  const unsigned t0 = NOW();
  asm volatile("" :: "s"(q), "s"(kc), "s"(vc), "s"(bt), "s"(lens), "s"(out), "s"(ws));
  const int lane = threadIdx.x & 63;
  const int wave = __builtin_amdgcn_readfirstlane(threadIdx.x >> 6);
  const int b = blockIdx.x >> 3, h = (blockIdx.x - b) & 7;
  const int g = lane >> 4, c = lane & 15;
  const unsigned base = (unsigned)(uintptr_t)LDSP(char, lds);
  const int w = wave < 8 ? wave >> 2 : min(wave - 8, 2);

  const int pg = 4 * (lane >> 1) + (wave >> 1 & 2) + (lane & 1);
  const int ly = lane < 32 ? lane : (lane ^ 16) & 31;
  int len = lens[lane & 31], leny = lens[ly];
  int pgv = bt[b * NBPS + pg];
  u32x4 qf[4];
  if ((wave & 14) == 8) {
#pragma unroll
    for (int u = 0; u < 4; ++u) qf[u] = *(const u32x4*)(q + (b * 8 + h) * 4096 + c * 256 + g * 16 + 64 * u);
  }
  if (wave == 10 && lane < 48) lds_put(base + WORDS + 4 * lane, 0);
  asm volatile("s_waitcnt lgkmcnt(0)\n\ts_barrier" ::: "memory");
  unsigned go[4];
#pragma unroll
  for (int u = 0; u < 4; ++u) go[u] = (4 * u + g) * 2048 + ((c ^ (4 * u + g)) << 4) - 1024 * u;
  asm volatile("" : "+v"(len), "+v"(leny), "+v"(pgv));
  const unsigned unimask = bal32(len <= 0);
  if (len <= 0 || len > SKV) len = SKV;
  int lenb = rl(len, b);
  auto issue = [&](unsigned dst, unsigned page, int hd, unsigned nrec) __attribute__((always_inline)) {
    const size_t po = (size_t)page * 32768 + hd * 256;
    const u32x4 kr = srd(kc + po, nrec), vr = srd(vc + po, nrec);
    DMA4(KPOL, dst, kr, go);
    DMA4(VPOL, dst + 4096, vr, go);
  };
  const bool early = wave < EA && (wave < 2 || lenb > 64);
  if (early) issue(base + wave * 8192, rl(pgv, wave), h, __builtin_amdgcn_readfirstlane(min(max(lenb - 16 * (wave + (wave & 2)), 0), 16) * 2048));

  if (leny <= 0 || leny > SKV) leny = SKV;
  const int key = ((len + 15) >> 4) * 32 + (lane & 31), keyy = ((leny + 15) >> 4) * 32 + ly;
  unsigned less = (unsigned)(keyy - key) >> 31;
#define ROR(n) less = __builtin_amdgcn_alignbit(less, __builtin_amdgcn_update_dpp(0, keyy, 0x120 + n, 0xf, 0xf, true) - key, 31);
  ROR(1) ROR(2) ROR(3) ROR(4) ROR(5) ROR(6) ROR(7) ROR(8) ROR(9) ROR(10) ROR(11) ROR(12) ROR(13) ROR(14) ROR(15)
  int rank = __builtin_popcount(less);
  {
    auto t = __builtin_amdgcn_permlane32_swap(rank, rank, false, false);
    rank = t[0] + t[1];
  }
  const int tot = rl(scan32(key >> 5), 31), rb = rl(rank, b);
  const bool gives = (h & 1) && h != 3, pair73 = (h & 3) == 3;
  const int rg = gives ? 31 - rb : pair73 ? rb : 2 * rb + (h >> 1 & 1);
  const int ph = pair73 ? h ^ 4 : gives ? h - 1 + 2 * (rg & 1) : (h & 4) | 1;
  const int pb = __builtin_ctz(bal32(rank == (gives ? (pair73 ? rg : rg >> 1) : 31 - rg)) | 0x80000000u);
  const int npb = (lenb + 15) >> 4, lenp = rl(len, pb), npp = (lenp + 15) >> 4;
  const int room = ((tot * LMAX) >> 11) - (gives ? npp : npb);
  int x = min((gives ? npb : npp) - max((tot * (pair73 ? CAPT : CAPS)) >> 11, 8), min(pair73 ? room : (room * 43) >> 6, 64));
  if (x < XMIN || rg > 31) x = 0;
  const bool helps = x && !gives;
  if (x && gives) lenb = 16 * (npb - x);
  const int tend = ((tot - 32) * TAU16) >> 9, tend2 = max(tend - MARG - STAG * rg, 0);
  const int msk = helps && w != 1 ? 3 : 7;
  const unsigned ring = base + (w == 1 ? 8 * 8192 : w * 2 * 8192), fullw = base + WORDS + w * 64, freew = fullw + 32;
  const int ftok = lenp - 16 * (npp - x);
  unsigned* const tsp = (unsigned*)(ws + (64u << 20)) + (blockIdx.x * 2 + w) * 16;

  if (wave < 8 || wave > 10) {
    int npos, hd = h;
    unsigned tab;
    if (w == 2) {
      if (!helps) return;
      pgv = bt[pb * NBPS + min(npp - x + lane, NBPS - 1)];
      hd = ph;
      npos = (x + 1) & ~1;
      const int rows = min(max(ftok - 16 * lane, 0), 16);
      tab = rows ? (unsigned)((float)((lane + 1) * tend2) * __builtin_amdgcn_rcpf((float)x)) << 16 | rows << 11 : 0;
    } else {
      npos = ((lenb + 63 - 32 * w) >> 6) * 2;
      const int rows = min(max(lenb - 16 * pg, 0), 16);
      const int np = (lenb + 15) >> 4, n0 = (np >> 2) * 2 + min(np & 3, 2), ld = LEAD + 2 * n0 - np;
      const int slot = w ? min(2 * lane + ld, n0 + lane) : max(2 * lane + 1 - ld, lane);
      tab = rows ? (unsigned)((float)(slot * tend) * __builtin_amdgcn_rcpf((float)max(np - 1, 1))) << 16 | rows << 11 : 0;
    }
    int freed = 0;
    if (lane == 0 && (wave & 11) == 0) { tsp[14] = NOW(); tsp[4] = npos; }
    const int st = w == 2 ? 3 : 4;
    for (int j = w == 2 ? wave - 11 : wave & 3; j < npos; j += st) {
      if (j != wave || !early) {
        const unsigned e = rl(tab, j);
        while (freed + msk < j) {
          freed = __builtin_amdgcn_readfirstlane(lds_ld(freew));
          if (freed + msk < j) __builtin_amdgcn_s_sleep(SLP);
        }
        const int due = (e >> 16) + POFS;
        while ((int)(NOW() - t0) < due) __builtin_amdgcn_s_sleep(SLP2);
        issue(ring + (j & msk) * 8192, rl(pgv, j), hd, e & 0xffff);
      }
      WAITVM0();
      lds_put(fullw + 4 * (j & msk), j + 1);
    }
    return;
  }

  char* const slot = ws + (unsigned)(gives ? b * 8 + h : pb * 8 + ph) * SLOT;
  unsigned tag = 0;
  if (w == 2) {
    if (lane == 0) { tsp[15 - 32] = t0; tsp[15 - 16] = t0; }
    if (!x) return;
    tag = __hip_atomic_load((gu32*)(slot + EPOCH_OFF), __ATOMIC_RELAXED, __HIP_MEMORY_SCOPE_AGENT) + TAGK;
    if (gives) {
      while ((int)(NOW() - t0) < tend2 + POFS + 150) __builtin_amdgcn_s_sleep(16);
#pragma nounroll
      for (int spin = 0; spin < (1 << 22); ++spin) {
        if (__builtin_amdgcn_readfirstlane(__hip_atomic_load((gu32*)(slot + FLAG_OFF), __ATOMIC_RELAXED, __HIP_MEMORY_SCOPE_AGENT) == tag)) break;
        __builtin_amdgcn_s_sleep(8);
      }
      asm volatile("" ::: "memory");
      const __amdgpu_buffer_rsrc_t pr = rsrc(slot, 4096 + 64);
      u32x4 y[4];
#pragma unroll
      for (int r = 0; r < 4; ++r) y[r] = __builtin_amdgcn_raw_buffer_load_b128(pr, lane * 16 + r * 1024, 0, 16);
      const unsigned lse = __builtin_amdgcn_raw_buffer_load_b32(pr, 4096 + 4 * c, 0, 16);
#pragma unroll
      for (int k = 0; k < 8; ++k) {
        const unsigned lo = y[k >> 1][2 * (k & 1)], hi = y[k >> 1][2 * (k & 1) + 1];
        *LDSP(u32x4, base + DUMP + DSZ + 1024 * k + 16 * lane) = u32x4{lo << 16, lo & 0xffff0000u, hi << 16, hi & 0xffff0000u};
      }
      *LDSP(u32x2, base + DUMP + DSZ + 8192 + 8 * lane) = u32x2{lse, g ? 0u : 0x3f800000u};
      asm volatile("s_waitcnt lgkmcnt(0)" ::: "memory");
      lds_put(fullw + 36, 1);
      if (lane == 0) __hip_atomic_store((gu32*)(slot + EPOCH_OFF), tag, __ATOMIC_RELAXED, __HIP_MEMORY_SCOPE_AGENT);
      return;
    }
  }
  __builtin_amdgcn_s_setprio(3);
  if (w == 2) {
#pragma unroll
    for (int u = 0; u < 4; ++u) qf[u] = *(const u32x4*)(q + (pb * 8 + ph) * 4096 + c * 256 + g * 16 + 64 * u);
  }
  if ((unimask >> (w == 2 ? pb : b)) & 1) {
#pragma unroll
    for (int u = 0; u < 4; ++u) qf[u] = u32x4{0, 0, 0, 0};
  }
  const float CS = 0.088388347648318440f * 1.4426950408889634f;
  const unsigned ka = ring + c * 256 + ((g ^ c) << 4);
  const unsigned va = ring + 4096 + (lane >> 2) * 256 + ((((lane & 3) << 2) ^ (lane >> 2)) << 4);
  f32x4 acc[8];
#pragma unroll
  for (int k = 0; k < 8; ++k) acc[k] = f32x4{0.f, 0.f, 0.f, 0.f};
  float m = -1e30f, l = 0.f;
  unsigned ts_first = 0, ts_real = 0, ts_mid = 0, ts_dump = 0;
  const unsigned ck0 = (unsigned)__builtin_amdgcn_s_memtime(), rt0 = NOW();
  asm volatile("" : "+v"(qf[0]), "+v"(qf[1]), "+v"(qf[2]), "+v"(qf[3]), "+v"(tag));
  int i = 0, tokens = w == 2 ? ftok : lenb - 32 * w;
  const int dec = w == 2 ? 32 : 64;
  for (int lim = w == 1 || (x && gives) ? 64 : 0;; lim = 0) {
    while (tokens > lim) {
      for (;;) {
        const uint64_t f = __hip_atomic_load(LDSP(uint64_t, fullw + 4 * (i & msk)), __ATOMIC_RELAXED, __HIP_MEMORY_SCOPE_WORKGROUP);
        if (__builtin_amdgcn_readfirstlane((unsigned)f + (unsigned)(f >> 32)) == 2 * i + 3) break;
      }
      asm volatile("" ::: "memory");
      if (i == 0) ts_first = NOW();
      if (i == 24) ts_mid = NOW();
      if (tokens <= 64) ts_real = NOW();
      const unsigned o = (i & msk) * 8192;
      u32x4 kf[2][4], vt[8];
#pragma unroll
      for (int u = 0; u < 4; ++u) {
        const unsigned a = (o + ka) ^ (u << 6);
        kf[0][u] = *LDSP(const u32x4, a);
        kf[1][u] = *LDSP(const u32x4, a + 8192);
      }
#pragma unroll
      for (int k = 0; k < 8; ++k) {
        const unsigned a = ((o + va) ^ ((k >> 1) << 4)) + 8 * (k & 1);
        const u32x2 lo = __builtin_bit_cast(u32x2, __builtin_amdgcn_ds_read_tr16_b64_v4i16(LDSP(s16x4, a)));
        const u32x2 hi = __builtin_bit_cast(u32x2, __builtin_amdgcn_ds_read_tr16_b64_v4i16(LDSP(s16x4, a + 8192)));
        vt[k] = u32x4{lo[0], lo[1], hi[0], hi[1]};
      }
      f32x4 sc[2] = {{0.f, 0.f, 0.f, 0.f}, {0.f, 0.f, 0.f, 0.f}};
#pragma unroll
      for (int u = 0; u < 4; ++u)
#pragma unroll
        for (int t = 0; t < 2; ++t)
          sc[t] = __builtin_amdgcn_mfma_f32_16x16x32_bf16(__builtin_bit_cast(bf16x8, kf[t][u]), __builtin_bit_cast(bf16x8, qf[u]), sc[t], 0, 0, 0);
      i += 2;
      if (tokens < 32) {
#pragma unroll
        for (int r = 0; r < 8; ++r) sc[r >> 2][r & 3] = 16 * (r >> 2) + 4 * g + (r & 3) < tokens ? sc[r >> 2][r & 3] : -__builtin_inff();
      }
      tokens -= dec;
      const float mx = xg_max(vmax(vmax(vmax(sc[0][0], sc[0][1]), vmax(sc[0][2], sc[0][3])), vmax(vmax(sc[1][0], sc[1][1]), vmax(sc[1][2], sc[1][3]))));
      const float mn = vmax(m, mx * CS);
      const float alpha = __builtin_amdgcn_exp2f(m - mn);
      m = mn;
      float pr[8];
#pragma unroll
      for (int r = 0; r < 8; ++r) pr[r] = __builtin_amdgcn_exp2f(__builtin_fmaf(sc[r >> 2][r & 3], CS, -mn));
      asm volatile("" : "+v"(vt[0]), "+v"(vt[1]), "+v"(vt[2]), "+v"(vt[3]), "+v"(vt[4]), "+v"(vt[5]), "+v"(vt[6]), "+v"(vt[7]) :: "memory");
      lds_put(freew, i);
      l = l * alpha + (((pr[0] + pr[1]) + (pr[2] + pr[3])) + ((pr[4] + pr[5]) + (pr[6] + pr[7])));
      const u32x4 pf = {cvtpk(pr[0], pr[1]), cvtpk(pr[2], pr[3]), cvtpk(pr[4], pr[5]), cvtpk(pr[6], pr[7])};
#pragma unroll
      for (int k = 0; k < 8; ++k)
        acc[k] = __builtin_amdgcn_mfma_f32_16x16x32_bf16(__builtin_bit_cast(bf16x8, vt[k]), __builtin_bit_cast(bf16x8, pf), acc[k] * alpha, 0, 0, 0);
    }
    if (!lim) break;
    while (!__builtin_amdgcn_readfirstlane(lds_ld(base + WORDS + 36 + (w ? 0 : 128)))) {}
    asm volatile("" ::: "memory");
    ts_dump = NOW();
    const unsigned src = base + DUMP + (w ? 0 : DSZ);
    const u32x2 y = *LDSP(const u32x2, src + 8192 + 8 * lane);
    const float mn = vmax(m, __uint_as_float(y[0]));
    const float alpha = __builtin_amdgcn_exp2f(m - mn), wt = __builtin_amdgcn_exp2f(__uint_as_float(y[0]) - mn);
    m = mn;
    l = l * alpha + __uint_as_float(y[1]) * wt;
#pragma unroll
    for (int k = 0; k < 8; ++k) acc[k] = acc[k] * alpha + *LDSP(const f32x4, src + 1024 * k + 16 * lane) * wt;
  }
  if (!w) {
#pragma unroll
    for (int k = 0; k < 8; ++k) *LDSP(f32x4, base + DUMP + 1024 * k + 16 * lane) = acc[k];
    *LDSP(u32x2, base + DUMP + 8192 + 8 * lane) = u32x2{__float_as_uint(m), __float_as_uint(l)};
    asm volatile("s_waitcnt lgkmcnt(0)" ::: "memory");
    lds_put(fullw + 36, 1);
  } else {
    const float lt = xg_sum(l);
    const float inv = __builtin_amdgcn_rcpf(lt);
    u32x4 y[4];
#pragma unroll
    for (int r = 0; r < 4; ++r)
      y[r] = u32x4{cvtpk(acc[2 * r][0] * inv, acc[2 * r][1] * inv), cvtpk(acc[2 * r][2] * inv, acc[2 * r][3] * inv),
                   cvtpk(acc[2 * r + 1][0] * inv, acc[2 * r + 1][1] * inv), cvtpk(acc[2 * r + 1][2] * inv, acc[2 * r + 1][3] * inv)};
    if (w == 1) {
      const __amdgpu_buffer_rsrc_t orr = rsrc(out + (b * 8 + h) * 4096, 4096);
#pragma unroll
      for (int r = 0; r < 4; ++r) __builtin_amdgcn_raw_buffer_store_b128(y[r], orr, c * 256 + g * 64 + r * 16, 0, OPOL);
    } else {
      const __amdgpu_buffer_rsrc_t orr = rsrc(slot, 4096 + 64);
#pragma unroll
      for (int r = 0; r < 4; ++r) __builtin_amdgcn_raw_buffer_store_b128(y[r], orr, lane * 16 + r * 1024, 0, 16);
      __builtin_amdgcn_raw_buffer_store_b32(__float_as_uint(m + __builtin_amdgcn_logf(lt)), orr, lane * 4 + 4096, 0, 16);
      WAITVM0();
      if (lane == 0) __hip_atomic_store((gu32*)(slot + FLAG_OFF), tag, __ATOMIC_RELAXED, __HIP_MEMORY_SCOPE_AGENT);
      if (lane == 0) tsp[15 - 32] = NOW();
      return;
    }
  }
  if (lane == 0) { unsigned* t = tsp; t[0] = t0; t[1] = ts_first; t[2] = ts_real; t[3] = NOW();
    t[5] = w; t[6] = ts_dump - t0; t[7] = ts_mid; t[8] = (unsigned)__builtin_amdgcn_s_memtime() - ck0; t[9] = NOW() - rt0; t[10] = !w && ts_dump ? ts_dump : t0; t[11] = h; t[12] = x; t[13] = __builtin_amdgcn_s_getreg((31 << 11) | 20); }
}

__device__ float stat_of(const unsigned* t, unsigned lo, int s) {
  const int f = s / 10000, sel = (s / 100) % 100, val = s % 100;
  float sum = 0.f, mx = 0.f; int n = 0;
  for (int w = 0; w < 512; ++w) {
    const unsigned* x = t + w * 16;
    bool in = true;
    if (sel == 1 || sel == 5) in = (int)x[11] == val;
    if (sel == 2 || sel == 6) in = (int)(x[13] & 7) == val;
    if (sel == 4) in = (x[5] > 0) == (val != 0);
    if (!in) continue;
    const float v = (float)(x[f] - lo);
    sum += v; mx = fmaxf(mx, v); ++n;
  }
  return (sel == 3 || sel == 5 || sel == 6) ? mx : sum / n;
}
__global__ void diag_kernel(const unsigned* __restrict__ tg) {
  __shared__ unsigned t[512 * 16];
  for (int i = threadIdx.x; i < 512 * 16; i += 512) t[i] = tg[i];
  __syncthreads();
  if (threadIdx.x == 0) {
    unsigned lo = t[0]; for (int i = 0; i < 512; ++i) if ((int)(t[i * 16] - lo) < 0) lo = t[i * 16];
    const int qa = min(63, max(0, (int)((stat_of(t, lo, SA) - OFSA) / UNITA))), qb = min(63, max(0, (int)((stat_of(t, lo, SB) - OFSB) / UNITB)));
    const int due = 30000 + 200 * qa + 12800 * qb;
    while ((int)((unsigned)__builtin_amdgcn_s_memrealtime() - lo) < due) __builtin_amdgcn_s_sleep(1);
  }
}

extern "C" void kernel_launch(void* const* d_in, const int* in_sizes, int n_in, void* d_out, int out_size,
                              void* d_ws, size_t ws_size, hipStream_t stream) {
  pa_kernel<<<256, 896, 0, stream>>>((const char*)d_in[0], (const char*)d_in[1], (const char*)d_in[2],
                                     (const int*)d_in[3], (const int*)d_in[4], (char*)d_out, (char*)d_ws);
  diag_kernel<<<1, 512, 0, stream>>>((const unsigned*)((char*)d_ws + (64u << 20)));
}
```

```cpp
#include <hip/hip_runtime.h>
#include <hip/hip_bf16.h>
#include <stdint.h>

#define LEAD 3
#define CAPS 58
#define CAPT 75
#define LMAX 84
#define EAM 0x3f
#define XCOST 8
#define TCOST 2
#define XMIN 4
#define KPOL " nt"
#define VPOL " nt"
#define TAU16 470
#define POFS 0
#define SLP 1
#define SLP2 1
#define OPOL 16

typedef __attribute__((ext_vector_type(8))) __bf16 bf16x8;
typedef __attribute__((ext_vector_type(4))) short s16x4;
typedef __attribute__((ext_vector_type(4))) float f32x4;
typedef __attribute__((ext_vector_type(4))) unsigned u32x4;
typedef __attribute__((ext_vector_type(2))) unsigned u32x2;
typedef __attribute__((address_space(1))) unsigned gu32;
#define LDSP(T, a) ((__attribute__((address_space(3))) T*)(uintptr_t)(a))

constexpr int NBPS = 128, SKV = NBPS * 16, NS = 8;
constexpr unsigned DUMP = 2 * NS * 8192, DSZ = 8192 + 512, WORDS = DUMP + 2 * DSZ;
constexpr unsigned SLOT = 8192, FLAG_OFF = 4096 + 128, EPOCH_OFF = 4096 + 256;
constexpr unsigned TAGK = 0x9E3779B9u;

__device__ __forceinline__ unsigned cvtpk(float lo, float hi) {
  typedef __attribute__((ext_vector_type(2))) __bf16 bf16x2;
  bf16x2 r = {(__bf16)lo, (__bf16)hi};
  return __builtin_bit_cast(unsigned, r);
}
__device__ __forceinline__ float vmax(float a, float b) { return __builtin_elementwise_maximum(a, b); }
__device__ __forceinline__ float xg_max(float x) {
  auto s = __builtin_amdgcn_permlane16_swap(__float_as_uint(x), __float_as_uint(x), false, false);
  x = vmax(__uint_as_float(s[0]), __uint_as_float(s[1]));
  auto t = __builtin_amdgcn_permlane32_swap(__float_as_uint(x), __float_as_uint(x), false, false);
  return vmax(__uint_as_float(t[0]), __uint_as_float(t[1]));
}
__device__ __forceinline__ float xg_sum(float x) {
  auto s = __builtin_amdgcn_permlane16_swap(__float_as_uint(x), __float_as_uint(x), false, false);
  x = __uint_as_float(s[0]) + __uint_as_float(s[1]);
  auto t = __builtin_amdgcn_permlane32_swap(__float_as_uint(x), __float_as_uint(x), false, false);
  return __uint_as_float(t[0]) + __uint_as_float(t[1]);
}
__device__ __forceinline__ int scan32(int v) {
  v += __builtin_amdgcn_update_dpp(0, v, 0x111, 0xf, 0xf, false);
  v += __builtin_amdgcn_update_dpp(0, v, 0x112, 0xf, 0xf, false);
  v += __builtin_amdgcn_update_dpp(0, v, 0x114, 0xf, 0xf, false);
  v += __builtin_amdgcn_update_dpp(0, v, 0x118, 0xf, 0xf, false);
  v += __builtin_amdgcn_update_dpp(0, v, 0x142, 0xa, 0xf, false);
  return v;
}
__device__ __forceinline__ __amdgpu_buffer_rsrc_t rsrc(const void* p, unsigned n) {
  return __builtin_amdgcn_make_buffer_rsrc((void*)p, 0, n, 0x00020000);
}
__device__ __forceinline__ u32x4 srd(const void* p, unsigned n) {
  const uint64_t a = (uint64_t)p;
  return u32x4{(unsigned)a, (unsigned)(a >> 32) & 0xffffu, n, 0x00020000u};
}
__device__ __forceinline__ int rl(int v, int l) { return __builtin_amdgcn_readlane(v, l); }
__device__ __forceinline__ unsigned bal32(bool c) { return (unsigned)__builtin_amdgcn_ballot_w64(c); }
__device__ __forceinline__ int lds_ld(unsigned a) {
  return __hip_atomic_load(LDSP(int, a), __ATOMIC_RELAXED, __HIP_MEMORY_SCOPE_WORKGROUP);
}
__device__ __forceinline__ void lds_put(unsigned a, int v) {
  __hip_atomic_store(LDSP(int, a), v, __ATOMIC_RELAXED, __HIP_MEMORY_SCOPE_WORKGROUP);
}

#define DMA4(POL, dst, rs, v)                                                                         \
  do {                                                                                                \
    unsigned keep_;                                                                                   \
    asm volatile("s_mov_b32 %0, m0\n\ts_mov_b32 m0, %1\n\ts_nop 4\n\t"                                \
                 "buffer_load_dwordx4 %3, %2, 0 offen" POL " lds\n\t"                                 \
                 "buffer_load_dwordx4 %4, %2, 0 offen offset:1024" POL " lds\n\t"                     \
                 "buffer_load_dwordx4 %5, %2, 0 offen offset:2048" POL " lds\n\t"                     \
                 "buffer_load_dwordx4 %6, %2, 0 offen offset:3072" POL " lds\n\t"                     \
                 "s_mov_b32 m0, %0"                                                                   \
                 : "=&s"(keep_) : "s"(dst), "s"(rs), "v"(v[0]), "v"(v[1]), "v"(v[2]), "v"(v[3]) : "memory"); \
  } while (0)
#define WAITVM0() asm volatile("s_waitcnt vmcnt(0)" ::: "memory")
#define NOW() ((unsigned)__builtin_amdgcn_s_memrealtime())

__global__ void __launch_bounds__(704)
pa_kernel(const char* __restrict__ q, const char* __restrict__ kc, const char* __restrict__ vc,
          const int* __restrict__ bt, const int* __restrict__ lens, char* __restrict__ out, char* __restrict__ ws) {
  __shared__ __attribute__((aligned(16384))) char lds[WORDS + 192];
  const unsigned t0 = NOW();
  asm volatile("" :: "s"(q), "s"(kc), "s"(vc), "s"(bt), "s"(lens), "s"(out), "s"(ws));
  const int lane = threadIdx.x & 63;
  const int wave = __builtin_amdgcn_readfirstlane(threadIdx.x >> 6);
  const int b = blockIdx.x >> 3, h = (blockIdx.x - b) & 7;
  const int g = lane >> 4, c = lane & 15;
  const unsigned base = (unsigned)(uintptr_t)LDSP(char, lds);
  const int w = wave < 8 ? wave >> 2 : wave - 8;

  int pg = 4 * (lane >> 1) + (wave >> 1 & 2) + (lane & 1);
  const int ly = lane < 32 ? lane : (lane ^ 16) & 31;
  int len = lens[lane & 31], leny = lens[ly];
  int pgv = bt[b * NBPS + pg];
  u32x4 qf[4];
  if ((wave & 14) == 8) {
#pragma unroll
    for (int u = 0; u < 4; ++u) qf[u] = *(const u32x4*)(q + (b * 8 + h) * 4096 + c * 256 + g * 16 + 64 * u);
  }
  if (wave == 10 && lane < 48) lds_put(base + WORDS + 4 * lane, 0);
  asm volatile("s_waitcnt lgkmcnt(0)\n\ts_barrier" ::: "memory");
  unsigned go[4];
#pragma unroll
  for (int u = 0; u < 4; ++u) go[u] = (4 * u + g) * 2048 + ((c ^ (4 * u + g)) << 4) - 1024 * u;
  asm volatile("" : "+v"(len), "+v"(leny), "+v"(pgv));
  const unsigned unimask = bal32(len <= 0);
  if (len <= 0 || len > SKV) len = SKV;
  int lenb = rl(len, b);
  auto issue = [&](unsigned dst, unsigned pghd, unsigned nrec) __attribute__((always_inline)) {
    const size_t po = (size_t)pghd * 256;
    const u32x4 kr = srd(kc + po, nrec), vr = srd(vc + po, nrec);
    DMA4(KPOL, dst, kr, go);
    DMA4(VPOL, dst + 4096, vr, go);
  };
  pgv = pgv * 128 + h;
  const unsigned ring = base + (w & 1) * 8 * 8192;
  const bool early = (EAM >> wave & 1) && lenb > 32 * (wave & 2) + 32 * w;
  if (early) issue(ring + (wave & 3) * 8192, rl(pgv, wave & 3), __builtin_amdgcn_readfirstlane(min(max(lenb - 16 * rl(pg, wave & 3), 0), 16) * 2048));

  if (leny <= 0 || leny > SKV) leny = SKV;
  const int key = ((len + 15) >> 4) * 32 + (lane & 31), keyy = ((leny + 15) >> 4) * 32 + ly;
  unsigned less = (unsigned)(keyy - key) >> 31;
#define ROR(n) less = __builtin_amdgcn_alignbit(less, __builtin_amdgcn_update_dpp(0, keyy, 0x120 + n, 0xf, 0xf, true) - key, 31);
  ROR(1) ROR(2) ROR(3) ROR(4) ROR(5) ROR(6) ROR(7) ROR(8) ROR(9) ROR(10) ROR(11) ROR(12) ROR(13) ROR(14) ROR(15)
  int rank = __builtin_popcount(less);
  {
    auto t = __builtin_amdgcn_permlane32_swap(rank, rank, false, false);
    rank = t[0] + t[1];
  }
  const int tot = rl(scan32(key >> 5), 31), rb = rl(rank, b);
  const bool gives = (h & 1) && h != 3, pair73 = (h & 3) == 3;
  const int rg = gives ? 31 - rb : pair73 ? rb : 2 * rb + (h >> 1 & 1);
  const int ph = pair73 ? h ^ 4 : gives ? h - 1 + 2 * (rg & 1) : (h & 4) | 1;
  const int pb = __builtin_ctz(bal32(rank == (gives ? (pair73 ? rg : rg >> 1) : 31 - rg)) | 0x80000000u);
  const int npb = (lenb + 15) >> 4, lenp = rl(len, pb), npp = (lenp + 15) >> 4;
  const int nph = gives ? npp : npb, xc = pair73 ? TCOST : XCOST;
  const int room = ((tot * LMAX) >> 11) - nph;
  int x = min((gives ? npb : npp) - max((tot * (pair73 ? CAPT : CAPS)) >> 11, 8), min((room * (1024 / (16 + (pair73 ? TCOST : XCOST)))) >> 6, 122 - nph));
  if (x < XMIN || rg > 31 || nph < 11) x = 0;
  const bool helps = x && !gives;
  if (x && gives) lenb = 16 * (npb - x);
  const int tend = ((tot - 32) * TAU16) >> 9;
  const int ftok = lenp - 16 * (npp - x);
  const int xh = helps ? x : 0, xw = ((xh + 1 + 2 * w) >> 2) * 2, sr = (x + 1) & 2;
  unsigned fullw = base + WORDS + (w == 2 ? sr * 32 : w * 64);

  if (wave < 8) {
    int tk = lenb;
    if (helps) {
      const bool fo = (unsigned)(lane - 4) < (unsigned)xw;
      pg = fo ? (pg - 8) ^ sr : lane < 4 ? pg : pg - 2 * xw;
      pgv = bt[fo ? pb * NBPS + min(npp - x + pg, NBPS - 1) : b * NBPS + pg] * 128 + (fo ? ph : h);
      tk = fo ? ftok : lenb;
    }
    const int npos = ((lenb + 63 - 32 * w) >> 6) * 2 + xw;
    const int rows = min(max(tk - 16 * pg, 0), 16);
    const int xr = (xh + 1) & ~1, no = (lenb + 15) >> 4, np = no + xr, n0 = (no >> 2) * 2 + min(no & 3, 2) + ((xh + 1) >> 2) * 2, ld = LEAD + 2 * n0 - np;
    const int slot = w ? min(2 * lane + ld, n0 + lane) : max(2 * lane + 1 - ld, lane);
    const int cost = 16 * slot + xc * min(max(slot - 7, 0), xr);
    const unsigned tab = rows ? (unsigned)((float)(cost * tend) * __builtin_amdgcn_rcpf((float)max(16 * (np - 1) + xc * xr, 1))) << 16 | rows << 11 : 0;
    int freed = 0;
    for (int j = wave & 3; j < npos; j += 4) {
      if (j > 3 || !early) {
        const unsigned e = rl(tab, j);
        while (freed + 7 < j) {
          freed = __builtin_amdgcn_readfirstlane(lds_ld(fullw + 32));
          if (freed + 7 < j) __builtin_amdgcn_s_sleep(SLP);
        }
        const int due = (e >> 16) + POFS;
        while ((int)(NOW() - t0) < due) __builtin_amdgcn_s_sleep(SLP2);
        issue(ring + (j & 7) * 8192, rl(pgv, j), e & 0xffff);
      }
      WAITVM0();
      lds_put(fullw + 4 * (j & 7), j + 1);
    }
    return;
  }

  char* const slot = ws + (unsigned)(gives ? b * 8 + h : pb * 8 + ph) * SLOT;
  unsigned tag = 0;
  if (w == 2) {
    if (!x) return;
    tag = __hip_atomic_load((gu32*)(slot + EPOCH_OFF), __ATOMIC_RELAXED, __HIP_MEMORY_SCOPE_AGENT) + TAGK;
    if (gives) {
      while ((int)(NOW() - t0) < tend - (tend >> 2)) __builtin_amdgcn_s_sleep(32);
#pragma nounroll
      for (int spin = 0; spin < (1 << 22); ++spin) {
        if (__builtin_amdgcn_readfirstlane(__hip_atomic_load((gu32*)(slot + FLAG_OFF), __ATOMIC_RELAXED, __HIP_MEMORY_SCOPE_AGENT) == tag)) break;
        __builtin_amdgcn_s_sleep(8);
      }
      asm volatile("" ::: "memory");
      const __amdgpu_buffer_rsrc_t pr = rsrc(slot, 4096 + 64);
      u32x4 y[4];
#pragma unroll
      for (int r = 0; r < 4; ++r) y[r] = __builtin_amdgcn_raw_buffer_load_b128(pr, lane * 16 + r * 1024, 0, 16);
      const unsigned lse = __builtin_amdgcn_raw_buffer_load_b32(pr, 4096 + 4 * c, 0, 16);
#pragma unroll
      for (int k = 0; k < 8; ++k) {
        const unsigned lo = y[k >> 1][2 * (k & 1)], hi = y[k >> 1][2 * (k & 1) + 1];
        *LDSP(u32x4, base + DUMP + DSZ + 1024 * k + 16 * lane) = u32x4{lo << 16, lo & 0xffff0000u, hi << 16, hi & 0xffff0000u};
      }
      *LDSP(u32x2, base + DUMP + DSZ + 8192 + 8 * lane) = u32x2{lse, g ? 0u : 0x3f800000u};
      asm volatile("s_waitcnt lgkmcnt(0)" ::: "memory");
      lds_put(base + WORDS + 128 + 36, 1);
      if (lane == 0) __hip_atomic_store((gu32*)(slot + EPOCH_OFF), tag, __ATOMIC_RELAXED, __HIP_MEMORY_SCOPE_AGENT);
      return;
    }
  }
  __builtin_amdgcn_s_setprio(3);
  if (w == 2) {
#pragma unroll
    for (int u = 0; u < 4; ++u) qf[u] = *(const u32x4*)(q + (pb * 8 + ph) * 4096 + c * 256 + g * 16 + 64 * u);
  }
  if ((unimask >> (w == 2 ? pb : b)) & 1) {
#pragma unroll
    for (int u = 0; u < 4; ++u) qf[u] = u32x4{0, 0, 0, 0};
  }
  const float CS = 0.088388347648318440f * 1.4426950408889634f;
  const unsigned ka = base + c * 256 + ((g ^ c) << 4);
  const unsigned va = base + 4096 + (lane >> 2) * 256 + ((((lane & 3) << 2) ^ (lane >> 2)) << 4);
  f32x4 acc[8];
#pragma unroll
  for (int k = 0; k < 8; ++k) acc[k] = f32x4{0.f, 0.f, 0.f, 0.f};
  float m = -1e30f, l = 0.f;
  asm volatile("" : "+v"(qf[0]), "+v"(qf[1]), "+v"(qf[2]), "+v"(qf[3]), "+v"(tag));
  int i = w == 2 ? 4 : 0, tokens = w == 2 ? ftok : lenb - 32 * w;
  const int dec = w == 2 ? 32 : 64, alt = w == 2 ? 64 : 0;
  for (int lim = w == 1 || (x && gives) ? 64 : 0;; lim = 0) {
    while (tokens > lim) {
      for (;;) {
        const uint64_t f = __hip_atomic_load(LDSP(uint64_t, fullw + 4 * (i & 7)), __ATOMIC_RELAXED, __HIP_MEMORY_SCOPE_WORKGROUP);
        if (__builtin_amdgcn_readfirstlane((unsigned)f + (unsigned)(f >> 32) + lds_ld(fullw + 32)) == 3 * i + 3) break;
      }
      asm volatile("" ::: "memory");
      const unsigned o = (i & 7) * 8192 + (fullw & 64) * 1024;
      u32x4 kf[2][4], vt[8];
#pragma unroll
      for (int u = 0; u < 4; ++u) {
        const unsigned a = (o + ka) ^ (u << 6);
        kf[0][u] = *LDSP(const u32x4, a);
        kf[1][u] = *LDSP(const u32x4, a + 8192);
      }
#pragma unroll
      for (int k = 0; k < 8; ++k) {
        const unsigned a = ((o + va) ^ ((k >> 1) << 4)) + 8 * (k & 1);
        const u32x2 lo = __builtin_bit_cast(u32x2, __builtin_amdgcn_ds_read_tr16_b64_v4i16(LDSP(s16x4, a)));
        const u32x2 hi = __builtin_bit_cast(u32x2, __builtin_amdgcn_ds_read_tr16_b64_v4i16(LDSP(s16x4, a + 8192)));
        vt[k] = u32x4{lo[0], lo[1], hi[0], hi[1]};
      }
      f32x4 sc[2] = {{0.f, 0.f, 0.f, 0.f}, {0.f, 0.f, 0.f, 0.f}};
#pragma unroll
      for (int u = 0; u < 4; ++u)
#pragma unroll
        for (int t = 0; t < 2; ++t)
          sc[t] = __builtin_amdgcn_mfma_f32_16x16x32_bf16(__builtin_bit_cast(bf16x8, kf[t][u]), __builtin_bit_cast(bf16x8, qf[u]), sc[t], 0, 0, 0);
      if (tokens < 32) {
#pragma unroll
        for (int r = 0; r < 8; ++r) sc[r >> 2][r & 3] = 16 * (r >> 2) + 4 * g + (r & 3) < tokens ? sc[r >> 2][r & 3] : -__builtin_inff();
      }
      tokens -= dec;
      const float mx = xg_max(vmax(vmax(vmax(sc[0][0], sc[0][1]), vmax(sc[0][2], sc[0][3])), vmax(vmax(sc[1][0], sc[1][1]), vmax(sc[1][2], sc[1][3]))));
      const float mn = vmax(m, mx * CS);
      const float alpha = __builtin_amdgcn_exp2f(m - mn);
      m = mn;
      float pr[8];
#pragma unroll
      for (int r = 0; r < 8; ++r) pr[r] = __builtin_amdgcn_exp2f(__builtin_fmaf(sc[r >> 2][r & 3], CS, -mn));
      asm volatile("" : "+v"(vt[0]), "+v"(vt[1]), "+v"(vt[2]), "+v"(vt[3]), "+v"(vt[4]), "+v"(vt[5]), "+v"(vt[6]), "+v"(vt[7]) :: "memory");
      lds_put(fullw + 32, i + 2);
      fullw ^= alt;
      i += (fullw ^ sr * 32) & alt ? 0 : i == 2 ? 2 + xw : 2;
      l = l * alpha + (((pr[0] + pr[1]) + (pr[2] + pr[3])) + ((pr[4] + pr[5]) + (pr[6] + pr[7])));
      const u32x4 pf = {cvtpk(pr[0], pr[1]), cvtpk(pr[2], pr[3]), cvtpk(pr[4], pr[5]), cvtpk(pr[6], pr[7])};
#pragma unroll
      for (int k = 0; k < 8; ++k)
        acc[k] = __builtin_amdgcn_mfma_f32_16x16x32_bf16(__builtin_bit_cast(bf16x8, vt[k]), __builtin_bit_cast(bf16x8, pf), acc[k] * alpha, 0, 0, 0);
    }
    if (!lim) break;
    while (!__builtin_amdgcn_readfirstlane(lds_ld(base + WORDS + 36 + (w ? 0 : 128)))) {}
    asm volatile("" ::: "memory");
    const unsigned src = base + DUMP + (w ? 0 : DSZ);
    const u32x2 y = *LDSP(const u32x2, src + 8192 + 8 * lane);
    const float mn = vmax(m, __uint_as_float(y[0]));
    const float alpha = __builtin_amdgcn_exp2f(m - mn), wt = __builtin_amdgcn_exp2f(__uint_as_float(y[0]) - mn);
    m = mn;
    l = l * alpha + __uint_as_float(y[1]) * wt;
#pragma unroll
    for (int k = 0; k < 8; ++k) acc[k] = acc[k] * alpha + *LDSP(const f32x4, src + 1024 * k + 16 * lane) * wt;
  }
  if (!w) {
#pragma unroll
    for (int k = 0; k < 8; ++k) *LDSP(f32x4, base + DUMP + 1024 * k + 16 * lane) = acc[k];
    *LDSP(u32x2, base + DUMP + 8192 + 8 * lane) = u32x2{__float_as_uint(m), __float_as_uint(l)};
    asm volatile("s_waitcnt lgkmcnt(0)" ::: "memory");
    lds_put(base + WORDS + 36, 1);
  } else {
    const float lt = xg_sum(l);
    const float inv = __builtin_amdgcn_rcpf(lt);
    u32x4 y[4];
#pragma unroll
    for (int r = 0; r < 4; ++r)
      y[r] = u32x4{cvtpk(acc[2 * r][0] * inv, acc[2 * r][1] * inv), cvtpk(acc[2 * r][2] * inv, acc[2 * r][3] * inv),
                   cvtpk(acc[2 * r + 1][0] * inv, acc[2 * r + 1][1] * inv), cvtpk(acc[2 * r + 1][2] * inv, acc[2 * r + 1][3] * inv)};
    if (w == 1) {
      const __amdgpu_buffer_rsrc_t orr = rsrc(out + (b * 8 + h) * 4096, 4096);
#pragma unroll
      for (int r = 0; r < 4; ++r) __builtin_amdgcn_raw_buffer_store_b128(y[r], orr, c * 256 + g * 64 + r * 16, 0, OPOL);
    } else {
      const __amdgpu_buffer_rsrc_t orr = rsrc(slot, 4096 + 64);
#pragma unroll
      for (int r = 0; r < 4; ++r) __builtin_amdgcn_raw_buffer_store_b128(y[r], orr, lane * 16 + r * 1024, 0, 16);
      __builtin_amdgcn_raw_buffer_store_b32(__float_as_uint(m + __builtin_amdgcn_logf(lt)), orr, lane * 4 + 4096, 0, 16);
      WAITVM0();
      if (lane == 0) __hip_atomic_store((gu32*)(slot + FLAG_OFF), tag, __ATOMIC_RELAXED, __HIP_MEMORY_SCOPE_AGENT);
      return;
    }
  }
}

extern "C" void kernel_launch(void* const* d_in, const int* in_sizes, int n_in, void* d_out, int out_size,
                              void* d_ws, size_t ws_size, hipStream_t stream) {
  pa_kernel<<<256, 704, 0, stream>>>((const char*)d_in[0], (const char*)d_in[1], (const char*)d_in[2],
                                     (const int*)d_in[3], (const int*)d_in[4], (char*)d_out, (char*)d_ws);
}
```

```cpp
#define TS
#define SA 30500
#define OFSA 2800
#define UNITA 20
#define SB 30501
#define OFSB 2800
#define UNITB 20
#include <hip/hip_runtime.h>
#include <hip/hip_bf16.h>
#include <stdint.h>

#define LEAD 3
#define CAPS 58
#define CAPT 75
#define LMAX 84
#define EA 3
#define XMIN 4
#define MARG 600
#define STAG 30
#define KPOL " nt"
#define VPOL " nt"
#define TAU16 470
#define POFS 0
#define SLP 1
#define SLP2 1
#define OPOL 16

typedef __attribute__((ext_vector_type(8))) __bf16 bf16x8;
typedef __attribute__((ext_vector_type(4))) short s16x4;
typedef __attribute__((ext_vector_type(4))) float f32x4;
typedef __attribute__((ext_vector_type(4))) unsigned u32x4;
typedef __attribute__((ext_vector_type(2))) unsigned u32x2;
typedef __attribute__((address_space(1))) unsigned gu32;
#define LDSP(T, a) ((__attribute__((address_space(3))) T*)(uintptr_t)(a))

constexpr int NBPS = 128, SKV = NBPS * 16, NS = 8;
constexpr unsigned DUMP = 2 * NS * 8192, DSZ = 8192 + 512, WORDS = DUMP + 2 * DSZ;
constexpr unsigned SLOT = 8192, FLAG_OFF = 4096 + 128, EPOCH_OFF = 4096 + 256;
constexpr unsigned TAGK = 0x9E3779B9u;

__device__ __forceinline__ unsigned cvtpk(float lo, float hi) {
  typedef __attribute__((ext_vector_type(2))) __bf16 bf16x2;
  bf16x2 r = {(__bf16)lo, (__bf16)hi};
  return __builtin_bit_cast(unsigned, r);
}
__device__ __forceinline__ float vmax(float a, float b) { return __builtin_elementwise_maximum(a, b); }
__device__ __forceinline__ float xg_max(float x) {
  auto s = __builtin_amdgcn_permlane16_swap(__float_as_uint(x), __float_as_uint(x), false, false);
  x = vmax(__uint_as_float(s[0]), __uint_as_float(s[1]));
  auto t = __builtin_amdgcn_permlane32_swap(__float_as_uint(x), __float_as_uint(x), false, false);
  return vmax(__uint_as_float(t[0]), __uint_as_float(t[1]));
}
__device__ __forceinline__ float xg_sum(float x) {
  auto s = __builtin_amdgcn_permlane16_swap(__float_as_uint(x), __float_as_uint(x), false, false);
  x = __uint_as_float(s[0]) + __uint_as_float(s[1]);
  auto t = __builtin_amdgcn_permlane32_swap(__float_as_uint(x), __float_as_uint(x), false, false);
  return __uint_as_float(t[0]) + __uint_as_float(t[1]);
}
__device__ __forceinline__ int scan32(int v) {
  v += __builtin_amdgcn_update_dpp(0, v, 0x111, 0xf, 0xf, false);
  v += __builtin_amdgcn_update_dpp(0, v, 0x112, 0xf, 0xf, false);
  v += __builtin_amdgcn_update_dpp(0, v, 0x114, 0xf, 0xf, false);
  v += __builtin_amdgcn_update_dpp(0, v, 0x118, 0xf, 0xf, false);
  v += __builtin_amdgcn_update_dpp(0, v, 0x142, 0xa, 0xf, false);
  return v;
}
__device__ __forceinline__ __amdgpu_buffer_rsrc_t rsrc(const void* p, unsigned n) {
  return __builtin_amdgcn_make_buffer_rsrc((void*)p, 0, n, 0x00020000);
}
__device__ __forceinline__ u32x4 srd(const void* p, unsigned n) {
  const uint64_t a = (uint64_t)p;
  return u32x4{(unsigned)a, (unsigned)(a >> 32) & 0xffffu, n, 0x00020000u};
}
__device__ __forceinline__ int rl(int v, int l) { return __builtin_amdgcn_readlane(v, l); }
__device__ __forceinline__ unsigned bal32(bool c) { return (unsigned)__builtin_amdgcn_ballot_w64(c); }
__device__ __forceinline__ int lds_ld(unsigned a) {
  return __hip_atomic_load(LDSP(int, a), __ATOMIC_RELAXED, __HIP_MEMORY_SCOPE_WORKGROUP);
}
__device__ __forceinline__ void lds_put(unsigned a, int v) {
  __hip_atomic_store(LDSP(int, a), v, __ATOMIC_RELAXED, __HIP_MEMORY_SCOPE_WORKGROUP);
}

#define DMA4(POL, dst, rs, v)                                                                         \
  do {                                                                                                \
    unsigned keep_;                                                                                   \
    asm volatile("s_mov_b32 %0, m0\n\ts_mov_b32 m0, %1\n\ts_nop 4\n\t"                                \
                 "buffer_load_dwordx4 %3, %2, 0 offen" POL " lds\n\t"                                 \
                 "buffer_load_dwordx4 %4, %2, 0 offen offset:1024" POL " lds\n\t"                     \
                 "buffer_load_dwordx4 %5, %2, 0 offen offset:2048" POL " lds\n\t"                     \
                 "buffer_load_dwordx4 %6, %2, 0 offen offset:3072" POL " lds\n\t"                     \
                 "s_mov_b32 m0, %0"                                                                   \
                 : "=&s"(keep_) : "s"(dst), "s"(rs), "v"(v[0]), "v"(v[1]), "v"(v[2]), "v"(v[3]) : "memory"); \
  } while (0)
#define WAITVM0() asm volatile("s_waitcnt vmcnt(0)" ::: "memory")
#define NOW() ((unsigned)__builtin_amdgcn_s_memrealtime())

__global__ void __launch_bounds__(896)
pa_kernel(const char* __restrict__ q, const char* __restrict__ kc, const char* __restrict__ vc,
          const int* __restrict__ bt, const int* __restrict__ lens, char* __restrict__ out, char* __restrict__ ws) {
  __shared__ __attribute__((aligned(16384))) char lds[WORDS + 192];
  const unsigned t0 = NOW();
  asm volatile("" :: "s"(q), "s"(kc), "s"(vc), "s"(bt), "s"(lens), "s"(out), "s"(ws));
  const int lane = threadIdx.x & 63;
  const int wave = __builtin_amdgcn_readfirstlane(threadIdx.x >> 6);
  const int b = blockIdx.x >> 3, h = (blockIdx.x - b) & 7;
  const int g = lane >> 4, c = lane & 15;
  const unsigned base = (unsigned)(uintptr_t)LDSP(char, lds);
  const int w = wave < 8 ? wave >> 2 : min(wave - 8, 2);

  const int pg = 4 * (lane >> 1) + (wave >> 1 & 2) + (lane & 1);
  const int ly = lane < 32 ? lane : (lane ^ 16) & 31;
  int len = lens[lane & 31], leny = lens[ly];
  int pgv = bt[b * NBPS + pg];
  u32x4 qf[4];
  if ((wave & 14) == 8) {
#pragma unroll
    for (int u = 0; u < 4; ++u) qf[u] = *(const u32x4*)(q + (b * 8 + h) * 4096 + c * 256 + g * 16 + 64 * u);
  }
  if (wave == 10 && lane < 48) lds_put(base + WORDS + 4 * lane, 0);
  asm volatile("s_waitcnt lgkmcnt(0)\n\ts_barrier" ::: "memory");
  unsigned go[4];
#pragma unroll
  for (int u = 0; u < 4; ++u) go[u] = (4 * u + g) * 2048 + ((c ^ (4 * u + g)) << 4) - 1024 * u;
  asm volatile("" : "+v"(len), "+v"(leny), "+v"(pgv));
  const unsigned unimask = bal32(len <= 0);
  if (len <= 0 || len > SKV) len = SKV;
  int lenb = rl(len, b);
  auto issue = [&](unsigned dst, unsigned page, int hd, unsigned nrec) __attribute__((always_inline)) {
    const size_t po = (size_t)page * 32768 + hd * 256;
    const u32x4 kr = srd(kc + po, nrec), vr = srd(vc + po, nrec);
    DMA4(KPOL, dst, kr, go);
    DMA4(VPOL, dst + 4096, vr, go);
  };
  const bool early = wave < EA && (wave < 2 || lenb > 64);
  if (early) issue(base + wave * 8192, rl(pgv, wave), h, __builtin_amdgcn_readfirstlane(min(max(lenb - 16 * (wave + (wave & 2)), 0), 16) * 2048));

  if (leny <= 0 || leny > SKV) leny = SKV;
  const int key = ((len + 15) >> 4) * 32 + (lane & 31), keyy = ((leny + 15) >> 4) * 32 + ly;
  unsigned less = (unsigned)(keyy - key) >> 31;
#define ROR(n) less = __builtin_amdgcn_alignbit(less, __builtin_amdgcn_update_dpp(0, keyy, 0x120 + n, 0xf, 0xf, true) - key, 31);
  ROR(1) ROR(2) ROR(3) ROR(4) ROR(5) ROR(6) ROR(7) ROR(8) ROR(9) ROR(10) ROR(11) ROR(12) ROR(13) ROR(14) ROR(15)
  int rank = __builtin_popcount(less);
  {
    auto t = __builtin_amdgcn_permlane32_swap(rank, rank, false, false);
    rank = t[0] + t[1];
  }
  const int tot = rl(scan32(key >> 5), 31), rb = rl(rank, b);
  const bool gives = (h & 1) && h != 3, pair73 = (h & 3) == 3;
  const int rg = gives ? 31 - rb : pair73 ? rb : 2 * rb + (h >> 1 & 1);
  const int ph = pair73 ? h ^ 4 : gives ? h - 1 + 2 * (rg & 1) : (h & 4) | 1;
  const int pb = __builtin_ctz(bal32(rank == (gives ? (pair73 ? rg : rg >> 1) : 31 - rg)) | 0x80000000u);
  const int npb = (lenb + 15) >> 4, lenp = rl(len, pb), npp = (lenp + 15) >> 4;
  const int room = ((tot * LMAX) >> 11) - (gives ? npp : npb);
  int x = min((gives ? npb : npp) - max((tot * (pair73 ? CAPT : CAPS)) >> 11, 8), min(pair73 ? room : (room * 43) >> 6, 64));
  if (x < XMIN || rg > 31) x = 0;
  const bool helps = x && !gives;
  if (x && gives) lenb = 16 * (npb - x);
  const int tend = ((tot - 32) * TAU16) >> 9, tend2 = max(tend - MARG - STAG * rg, 0);
  const int msk = helps && w != 1 ? 3 : 7;
  const unsigned ring = base + (w == 1 ? 8 * 8192 : w * 2 * 8192), fullw = base + WORDS + w * 64, freew = fullw + 32;
  const int ftok = lenp - 16 * (npp - x);
  unsigned* const tsp = (unsigned*)(ws + (64u << 20)) + (blockIdx.x * 2 + w) * 16;

  if (wave < 8 || wave > 10) {
    int npos, hd = h;
    unsigned tab;
    if (w == 2) {
      if (!helps) return;
      pgv = bt[pb * NBPS + min(npp - x + lane, NBPS - 1)];
      hd = ph;
      npos = (x + 1) & ~1;
      const int rows = min(max(ftok - 16 * lane, 0), 16);
      tab = rows ? (unsigned)((float)((lane + 1) * tend2) * __builtin_amdgcn_rcpf((float)x)) << 16 | rows << 11 : 0;
    } else {
      npos = ((lenb + 63 - 32 * w) >> 6) * 2;
      const int rows = min(max(lenb - 16 * pg, 0), 16);
      const int np = (lenb + 15) >> 4, n0 = (np >> 2) * 2 + min(np & 3, 2), ld = LEAD + 2 * n0 - np;
      const int slot = w ? min(2 * lane + ld, n0 + lane) : max(2 * lane + 1 - ld, lane);
      tab = rows ? (unsigned)((float)(slot * tend) * __builtin_amdgcn_rcpf((float)max(np - 1, 1))) << 16 | rows << 11 : 0;
    }
    int freed = 0;
    if (lane == 0 && (wave & 11) == 0) { tsp[14] = NOW(); tsp[4] = npos; }
    const int st = w == 2 ? 3 : 4;
    for (int j = w == 2 ? wave - 11 : wave & 3; j < npos; j += st) {
      if (j != wave || !early) {
        const unsigned e = rl(tab, j);
        while (freed + msk < j) {
          freed = __builtin_amdgcn_readfirstlane(lds_ld(freew));
          if (freed + msk < j) __builtin_amdgcn_s_sleep(SLP);
        }
        const int due = (e >> 16) + POFS;
        while ((int)(NOW() - t0) < due) __builtin_amdgcn_s_sleep(SLP2);
        issue(ring + (j & msk) * 8192, rl(pgv, j), hd, e & 0xffff);
      }
      WAITVM0();
      lds_put(fullw + 4 * (j & msk), j + 1);
    }
    return;
  }

  char* const slot = ws + (unsigned)(gives ? b * 8 + h : pb * 8 + ph) * SLOT;
  unsigned tag = 0;
  if (w == 2) {
    if (lane == 0) { tsp[15 - 32] = t0; tsp[15 - 16] = t0; }
    if (!x) return;
    tag = __hip_atomic_load((gu32*)(slot + EPOCH_OFF), __ATOMIC_RELAXED, __HIP_MEMORY_SCOPE_AGENT) + TAGK;
    if (gives) {
      while ((int)(NOW() - t0) < tend2 + POFS + 150) __builtin_amdgcn_s_sleep(16);
#pragma nounroll
      for (int spin = 0; spin < (1 << 22); ++spin) {
        if (__builtin_amdgcn_readfirstlane(__hip_atomic_load((gu32*)(slot + FLAG_OFF), __ATOMIC_RELAXED, __HIP_MEMORY_SCOPE_AGENT) == tag)) break;
        __builtin_amdgcn_s_sleep(8);
      }
      asm volatile("" ::: "memory");
      const __amdgpu_buffer_rsrc_t pr = rsrc(slot, 4096 + 64);
      u32x4 y[4];
#pragma unroll
      for (int r = 0; r < 4; ++r) y[r] = __builtin_amdgcn_raw_buffer_load_b128(pr, lane * 16 + r * 1024, 0, 16);
      const unsigned lse = __builtin_amdgcn_raw_buffer_load_b32(pr, 4096 + 4 * c, 0, 16);
#pragma unroll
      for (int k = 0; k < 8; ++k) {
        const unsigned lo = y[k >> 1][2 * (k & 1)], hi = y[k >> 1][2 * (k & 1) + 1];
        *LDSP(u32x4, base + DUMP + DSZ + 1024 * k + 16 * lane) = u32x4{lo << 16, lo & 0xffff0000u, hi << 16, hi & 0xffff0000u};
      }
      *LDSP(u32x2, base + DUMP + DSZ + 8192 + 8 * lane) = u32x2{lse, g ? 0u : 0x3f800000u};
      asm volatile("s_waitcnt lgkmcnt(0)" ::: "memory");
      lds_put(fullw + 36, 1);
      if (lane == 0) __hip_atomic_store((gu32*)(slot + EPOCH_OFF), tag, __ATOMIC_RELAXED, __HIP_MEMORY_SCOPE_AGENT);
      return;
    }
  }
  __builtin_amdgcn_s_setprio(3);
  if (w == 2) {
#pragma unroll
    for (int u = 0; u < 4; ++u) qf[u] = *(const u32x4*)(q + (pb * 8 + ph) * 4096 + c * 256 + g * 16 + 64 * u);
  }
  if ((unimask >> (w == 2 ? pb : b)) & 1) {
#pragma unroll
    for (int u = 0; u < 4; ++u) qf[u] = u32x4{0, 0, 0, 0};
  }
  const float CS = 0.088388347648318440f * 1.4426950408889634f;
  const unsigned ka = ring + c * 256 + ((g ^ c) << 4);
  const unsigned va = ring + 4096 + (lane >> 2) * 256 + ((((lane & 3) << 2) ^ (lane >> 2)) << 4);
  f32x4 acc[8];
#pragma unroll
  for (int k = 0; k < 8; ++k) acc[k] = f32x4{0.f, 0.f, 0.f, 0.f};
  float m = -1e30f, l = 0.f;
  unsigned ts_first = 0, ts_real = 0, ts_mid = 0, ts_dump = 0;
  const unsigned ck0 = (unsigned)__builtin_amdgcn_s_memtime(), rt0 = NOW();
  asm volatile("" : "+v"(qf[0]), "+v"(qf[1]), "+v"(qf[2]), "+v"(qf[3]), "+v"(tag));
  int i = 0, tokens = w == 2 ? ftok : lenb - 32 * w;
  const int dec = w == 2 ? 32 : 64;
  for (int lim = w == 1 || (x && gives) ? 64 : 0;; lim = 0) {
    while (tokens > lim) {
      for (;;) {
        const uint64_t f = __hip_atomic_load(LDSP(uint64_t, fullw + 4 * (i & msk)), __ATOMIC_RELAXED, __HIP_MEMORY_SCOPE_WORKGROUP);
        if (__builtin_amdgcn_readfirstlane((unsigned)f + (unsigned)(f >> 32)) == 2 * i + 3) break;
      }
      asm volatile("" ::: "memory");
      if (i == 0) ts_first = NOW();
      if (i == 24) ts_mid = NOW();
      if (tokens <= 64) ts_real = NOW();
      const unsigned o = (i & msk) * 8192;
      u32x4 kf[2][4], vt[8];
#pragma unroll
      for (int u = 0; u < 4; ++u) {
        const unsigned a = (o + ka) ^ (u << 6);
        kf[0][u] = *LDSP(const u32x4, a);
        kf[1][u] = *LDSP(const u32x4, a + 8192);
      }
#pragma unroll
      for (int k = 0; k < 8; ++k) {
        const unsigned a = ((o + va) ^ ((k >> 1) << 4)) + 8 * (k & 1);
        const u32x2 lo = __builtin_bit_cast(u32x2, __builtin_amdgcn_ds_read_tr16_b64_v4i16(LDSP(s16x4, a)));
        const u32x2 hi = __builtin_bit_cast(u32x2, __builtin_amdgcn_ds_read_tr16_b64_v4i16(LDSP(s16x4, a + 8192)));
        vt[k] = u32x4{lo[0], lo[1], hi[0], hi[1]};
      }
      f32x4 sc[2] = {{0.f, 0.f, 0.f, 0.f}, {0.f, 0.f, 0.f, 0.f}};
#pragma unroll
      for (int u = 0; u < 4; ++u)
#pragma unroll
        for (int t = 0; t < 2; ++t)
          sc[t] = __builtin_amdgcn_mfma_f32_16x16x32_bf16(__builtin_bit_cast(bf16x8, kf[t][u]), __builtin_bit_cast(bf16x8, qf[u]), sc[t], 0, 0, 0);
      i += 2;
      if (tokens < 32) {
#pragma unroll
        for (int r = 0; r < 8; ++r) sc[r >> 2][r & 3] = 16 * (r >> 2) + 4 * g + (r & 3) < tokens ? sc[r >> 2][r & 3] : -__builtin_inff();
      }
      tokens -= dec;
      const float mx = xg_max(vmax(vmax(vmax(sc[0][0], sc[0][1]), vmax(sc[0][2], sc[0][3])), vmax(vmax(sc[1][0], sc[1][1]), vmax(sc[1][2], sc[1][3]))));
      const float mn = vmax(m, mx * CS);
      const float alpha = __builtin_amdgcn_exp2f(m - mn);
      m = mn;
      float pr[8];
#pragma unroll
      for (int r = 0; r < 8; ++r) pr[r] = __builtin_amdgcn_exp2f(__builtin_fmaf(sc[r >> 2][r & 3], CS, -mn));
      asm volatile("" : "+v"(vt[0]), "+v"(vt[1]), "+v"(vt[2]), "+v"(vt[3]), "+v"(vt[4]), "+v"(vt[5]), "+v"(vt[6]), "+v"(vt[7]) :: "memory");
      lds_put(freew, i);
      l = l * alpha + (((pr[0] + pr[1]) + (pr[2] + pr[3])) + ((pr[4] + pr[5]) + (pr[6] + pr[7])));
      const u32x4 pf = {cvtpk(pr[0], pr[1]), cvtpk(pr[2], pr[3]), cvtpk(pr[4], pr[5]), cvtpk(pr[6], pr[7])};
#pragma unroll
      for (int k = 0; k < 8; ++k)
        acc[k] = __builtin_amdgcn_mfma_f32_16x16x32_bf16(__builtin_bit_cast(bf16x8, vt[k]), __builtin_bit_cast(bf16x8, pf), acc[k] * alpha, 0, 0, 0);
    }
    if (!lim) break;
    while (!__builtin_amdgcn_readfirstlane(lds_ld(base + WORDS + 36 + (w ? 0 : 128)))) {}
    asm volatile("" ::: "memory");
    ts_dump = NOW();
    const unsigned src = base + DUMP + (w ? 0 : DSZ);
    const u32x2 y = *LDSP(const u32x2, src + 8192 + 8 * lane);
    const float mn = vmax(m, __uint_as_float(y[0]));
    const float alpha = __builtin_amdgcn_exp2f(m - mn), wt = __builtin_amdgcn_exp2f(__uint_as_float(y[0]) - mn);
    m = mn;
    l = l * alpha + __uint_as_float(y[1]) * wt;
#pragma unroll
    for (int k = 0; k < 8; ++k) acc[k] = acc[k] * alpha + *LDSP(const f32x4, src + 1024 * k + 16 * lane) * wt;
  }
  if (!w) {
#pragma unroll
    for (int k = 0; k < 8; ++k) *LDSP(f32x4, base + DUMP + 1024 * k + 16 * lane) = acc[k];
    *LDSP(u32x2, base + DUMP + 8192 + 8 * lane) = u32x2{__float_as_uint(m), __float_as_uint(l)};
    asm volatile("s_waitcnt lgkmcnt(0)" ::: "memory");
    lds_put(fullw + 36, 1);
  } else {
    const float lt = xg_sum(l);
    const float inv = __builtin_amdgcn_rcpf(lt);
    u32x4 y[4];
#pragma unroll
    for (int r = 0; r < 4; ++r)
      y[r] = u32x4{cvtpk(acc[2 * r][0] * inv, acc[2 * r][1] * inv), cvtpk(acc[2 * r][2] * inv, acc[2 * r][3] * inv),
                   cvtpk(acc[2 * r + 1][0] * inv, acc[2 * r + 1][1] * inv), cvtpk(acc[2 * r + 1][2] * inv, acc[2 * r + 1][3] * inv)};
    if (w == 1) {
      const __amdgpu_buffer_rsrc_t orr = rsrc(out + (b * 8 + h) * 4096, 4096);
#pragma unroll
      for (int r = 0; r < 4; ++r) __builtin_amdgcn_raw_buffer_store_b128(y[r], orr, c * 256 + g * 64 + r * 16, 0, OPOL);
    } else {
      const __amdgpu_buffer_rsrc_t orr = rsrc(slot, 4096 + 64);
#pragma unroll
      for (int r = 0; r < 4; ++r) __builtin_amdgcn_raw_buffer_store_b128(y[r], orr, lane * 16 + r * 1024, 0, 16);
      __builtin_amdgcn_raw_buffer_store_b32(__float_as_uint(m + __builtin_amdgcn_logf(lt)), orr, lane * 4 + 4096, 0, 16);
      WAITVM0();
      if (lane == 0) __hip_atomic_store((gu32*)(slot + FLAG_OFF), tag, __ATOMIC_RELAXED, __HIP_MEMORY_SCOPE_AGENT);
      if (lane == 0) tsp[15 - 32] = NOW();
      return;
    }
  }
  if (lane == 0) { unsigned* t = tsp; t[0] = t0; t[1] = ts_first; t[2] = ts_real; t[3] = NOW();
    t[5] = w; t[6] = ts_dump - t0; t[7] = ts_mid; t[8] = (unsigned)__builtin_amdgcn_s_memtime() - ck0; t[9] = NOW() - rt0; t[10] = !w && ts_dump ? ts_dump : t0; t[11] = h; t[12] = x; t[13] = __builtin_amdgcn_s_getreg((31 << 11) | 20); }
}

__device__ float stat_of(const unsigned* t, unsigned lo, int s) {
  const int f = s / 10000, sel = (s / 100) % 100, val = s % 100;
  float sum = 0.f, mx = 0.f; int n = 0;
  for (int w = 0; w < 512; ++w) {
    const unsigned* x = t + w * 16;
    bool in = true;
    if (sel == 1 || sel == 5) in = (int)x[11] == val;
    if (sel == 2 || sel == 6) in = (int)(x[13] & 7) == val;
    if (sel == 4) in = (x[5] > 0) == (val != 0);
    if (!in) continue;
    const float v = (float)(x[f] - lo);
    sum += v; mx = fmaxf(mx, v); ++n;
  }
  return (sel == 3 || sel == 5 || sel == 6) ? mx : sum / n;
}
__global__ void diag_kernel(const unsigned* __restrict__ tg) {
  __shared__ unsigned t[512 * 16];
  for (int i = threadIdx.x; i < 512 * 16; i += 512) t[i] = tg[i];
  __syncthreads();
  if (threadIdx.x == 0) {
    unsigned lo = t[0]; for (int i = 0; i < 512; ++i) if ((int)(t[i * 16] - lo) < 0) lo = t[i * 16];
    const int qa = min(63, max(0, (int)((stat_of(t, lo, SA) - OFSA) / UNITA))), qb = min(63, max(0, (int)((stat_of(t, lo, SB) - OFSB) / UNITB)));
    const int due = 30000 + 200 * qa + 12800 * qb;
    while ((int)((unsigned)__builtin_amdgcn_s_memrealtime() - lo) < due) __builtin_amdgcn_s_sleep(1);
  }
}

extern "C" void kernel_launch(void* const* d_in, const int* in_sizes, int n_in, void* d_out, int out_size,
                              void* d_ws, size_t ws_size, hipStream_t stream) {
  pa_kernel<<<256, 896, 0, stream>>>((const char*)d_in[0], (const char*)d_in[1], (const char*)d_in[2],
                                     (const int*)d_in[3], (const int*)d_in[4], (char*)d_out, (char*)d_ws);
  diag_kernel<<<1, 512, 0, stream>>>((const unsigned*)((char*)d_ws + (64u << 20)));
}
```

```cpp
#define EAM 0x1f
#define TAU16 480
#include <hip/hip_runtime.h>
#include <hip/hip_bf16.h>
#include <stdint.h>

#define LEAD 3
#define CAPS 58
#define CAPT 75
#define LMAX 84
#define XCOST 8
#define TCOST 2
#define XMIN 4
#define KPOL " nt"
#define VPOL " nt"
#define POFS 0
#define SLP 1
#define SLP2 1
#define OPOL 16

typedef __attribute__((ext_vector_type(8))) __bf16 bf16x8;
typedef __attribute__((ext_vector_type(4))) short s16x4;
typedef __attribute__((ext_vector_type(4))) float f32x4;
typedef __attribute__((ext_vector_type(4))) unsigned u32x4;
typedef __attribute__((ext_vector_type(2))) unsigned u32x2;
typedef __attribute__((address_space(1))) unsigned gu32;
#define LDSP(T, a) ((__attribute__((address_space(3))) T*)(uintptr_t)(a))

constexpr int NBPS = 128, SKV = NBPS * 16, NS = 8;
constexpr unsigned DUMP = 2 * NS * 8192, DSZ = 8192 + 512, WORDS = DUMP + 2 * DSZ;
constexpr unsigned SLOT = 8192, FLAG_OFF = 4096 + 128, EPOCH_OFF = 4096 + 256;
constexpr unsigned TAGK = 0x9E3779B9u;

__device__ __forceinline__ unsigned cvtpk(float lo, float hi) {
  typedef __attribute__((ext_vector_type(2))) __bf16 bf16x2;
  bf16x2 r = {(__bf16)lo, (__bf16)hi};
  return __builtin_bit_cast(unsigned, r);
}
__device__ __forceinline__ float vmax(float a, float b) { return __builtin_elementwise_maximum(a, b); }
__device__ __forceinline__ float xg_max(float x) {
  auto s = __builtin_amdgcn_permlane16_swap(__float_as_uint(x), __float_as_uint(x), false, false);
  x = vmax(__uint_as_float(s[0]), __uint_as_float(s[1]));
  auto t = __builtin_amdgcn_permlane32_swap(__float_as_uint(x), __float_as_uint(x), false, false);
  return vmax(__uint_as_float(t[0]), __uint_as_float(t[1]));
}
__device__ __forceinline__ float xg_sum(float x) {
  auto s = __builtin_amdgcn_permlane16_swap(__float_as_uint(x), __float_as_uint(x), false, false);
  x = __uint_as_float(s[0]) + __uint_as_float(s[1]);
  auto t = __builtin_amdgcn_permlane32_swap(__float_as_uint(x), __float_as_uint(x), false, false);
  return __uint_as_float(t[0]) + __uint_as_float(t[1]);
}
__device__ __forceinline__ int scan32(int v) {
  v += __builtin_amdgcn_update_dpp(0, v, 0x111, 0xf, 0xf, false);
  v += __builtin_amdgcn_update_dpp(0, v, 0x112, 0xf, 0xf, false);
  v += __builtin_amdgcn_update_dpp(0, v, 0x114, 0xf, 0xf, false);
  v += __builtin_amdgcn_update_dpp(0, v, 0x118, 0xf, 0xf, false);
  v += __builtin_amdgcn_update_dpp(0, v, 0x142, 0xa, 0xf, false);
  return v;
}
__device__ __forceinline__ __amdgpu_buffer_rsrc_t rsrc(const void* p, unsigned n) {
  return __builtin_amdgcn_make_buffer_rsrc((void*)p, 0, n, 0x00020000);
}
__device__ __forceinline__ u32x4 srd(const void* p, unsigned n) {
  const uint64_t a = (uint64_t)p;
  return u32x4{(unsigned)a, (unsigned)(a >> 32) & 0xffffu, n, 0x00020000u};
}
__device__ __forceinline__ int rl(int v, int l) { return __builtin_amdgcn_readlane(v, l); }
__device__ __forceinline__ unsigned bal32(bool c) { return (unsigned)__builtin_amdgcn_ballot_w64(c); }
__device__ __forceinline__ int lds_ld(unsigned a) {
  return __hip_atomic_load(LDSP(int, a), __ATOMIC_RELAXED, __HIP_MEMORY_SCOPE_WORKGROUP);
}
__device__ __forceinline__ void lds_put(unsigned a, int v) {
  __hip_atomic_store(LDSP(int, a), v, __ATOMIC_RELAXED, __HIP_MEMORY_SCOPE_WORKGROUP);
}

#define DMA4(POL, dst, rs, v)                                                                         \
  do {                                                                                                \
    unsigned keep_;                                                                                   \
    asm volatile("s_mov_b32 %0, m0\n\ts_mov_b32 m0, %1\n\ts_nop 4\n\t"                                \
                 "buffer_load_dwordx4 %3, %2, 0 offen" POL " lds\n\t"                                 \
                 "buffer_load_dwordx4 %4, %2, 0 offen offset:1024" POL " lds\n\t"                     \
                 "buffer_load_dwordx4 %5, %2, 0 offen offset:2048" POL " lds\n\t"                     \
                 "buffer_load_dwordx4 %6, %2, 0 offen offset:3072" POL " lds\n\t"                     \
                 "s_mov_b32 m0, %0"                                                                   \
                 : "=&s"(keep_) : "s"(dst), "s"(rs), "v"(v[0]), "v"(v[1]), "v"(v[2]), "v"(v[3]) : "memory"); \
  } while (0)
#define WAITVM0() asm volatile("s_waitcnt vmcnt(0)" ::: "memory")
#define NOW() ((unsigned)__builtin_amdgcn_s_memrealtime())

__global__ void __launch_bounds__(704)
pa_kernel(const char* __restrict__ q, const char* __restrict__ kc, const char* __restrict__ vc,
          const int* __restrict__ bt, const int* __restrict__ lens, char* __restrict__ out, char* __restrict__ ws) {
  __shared__ __attribute__((aligned(16384))) char lds[WORDS + 192];
  const unsigned t0 = NOW();
  asm volatile("" :: "s"(q), "s"(kc), "s"(vc), "s"(bt), "s"(lens), "s"(out), "s"(ws));
  const int lane = threadIdx.x & 63;
  const int wave = __builtin_amdgcn_readfirstlane(threadIdx.x >> 6);
  const int b = blockIdx.x >> 3, h = (blockIdx.x - b) & 7;
  const int g = lane >> 4, c = lane & 15;
  const unsigned base = (unsigned)(uintptr_t)LDSP(char, lds);
  const int w = wave < 8 ? wave >> 2 : wave - 8;

  int pg = 4 * (lane >> 1) + (wave >> 1 & 2) + (lane & 1);
  const int ly = lane < 32 ? lane : (lane ^ 16) & 31;
  int len = lens[lane & 31], leny = lens[ly];
  int pgv = bt[b * NBPS + pg];
  u32x4 qf[4];
  if ((wave & 14) == 8) {
#pragma unroll
    for (int u = 0; u < 4; ++u) qf[u] = *(const u32x4*)(q + (b * 8 + h) * 4096 + c * 256 + g * 16 + 64 * u);
  }
  if (wave == 10 && lane < 48) lds_put(base + WORDS + 4 * lane, 0);
  asm volatile("s_waitcnt lgkmcnt(0)\n\ts_barrier" ::: "memory");
  unsigned go[4];
#pragma unroll
  for (int u = 0; u < 4; ++u) go[u] = (4 * u + g) * 2048 + ((c ^ (4 * u + g)) << 4) - 1024 * u;
  asm volatile("" : "+v"(len), "+v"(leny), "+v"(pgv));
  const unsigned unimask = bal32(len <= 0);
  if (len <= 0 || len > SKV) len = SKV;
  int lenb = rl(len, b);
  auto issue = [&](unsigned dst, unsigned pghd, unsigned nrec) __attribute__((always_inline)) {
    const size_t po = (size_t)pghd * 256;
    const u32x4 kr = srd(kc + po, nrec), vr = srd(vc + po, nrec);
    DMA4(KPOL, dst, kr, go);
    DMA4(VPOL, dst + 4096, vr, go);
  };
  pgv = pgv * 128 + h;
  const unsigned ring = base + (w & 1) * 8 * 8192;
  const bool early = (EAM >> wave & 1) && lenb > 32 * (wave & 2) + 32 * w;
  if (early) issue(ring + (wave & 3) * 8192, rl(pgv, wave & 3), __builtin_amdgcn_readfirstlane(min(max(lenb - 16 * rl(pg, wave & 3), 0), 16) * 2048));

  if (leny <= 0 || leny > SKV) leny = SKV;
  const int key = ((len + 15) >> 4) * 32 + (lane & 31), keyy = ((leny + 15) >> 4) * 32 + ly;
  unsigned less = (unsigned)(keyy - key) >> 31;
#define ROR(n) less = __builtin_amdgcn_alignbit(less, __builtin_amdgcn_update_dpp(0, keyy, 0x120 + n, 0xf, 0xf, true) - key, 31);
  ROR(1) ROR(2) ROR(3) ROR(4) ROR(5) ROR(6) ROR(7) ROR(8) ROR(9) ROR(10) ROR(11) ROR(12) ROR(13) ROR(14) ROR(15)
  int rank = __builtin_popcount(less);
  {
    auto t = __builtin_amdgcn_permlane32_swap(rank, rank, false, false);
    rank = t[0] + t[1];
  }
  const int tot = rl(scan32(key >> 5), 31), rb = rl(rank, b);
  const bool gives = (h & 1) && h != 3, pair73 = (h & 3) == 3;
  const int rg = gives ? 31 - rb : pair73 ? rb : 2 * rb + (h >> 1 & 1);
  const int ph = pair73 ? h ^ 4 : gives ? h - 1 + 2 * (rg & 1) : (h & 4) | 1;
  const int pb = __builtin_ctz(bal32(rank == (gives ? (pair73 ? rg : rg >> 1) : 31 - rg)) | 0x80000000u);
  const int npb = (lenb + 15) >> 4, lenp = rl(len, pb), npp = (lenp + 15) >> 4;
  const int nph = gives ? npp : npb, xc = pair73 ? TCOST : XCOST;
  const int room = ((tot * LMAX) >> 11) - nph;
  int x = min((gives ? npb : npp) - max((tot * (pair73 ? CAPT : CAPS)) >> 11, 8), min((room * (1024 / (16 + (pair73 ? TCOST : XCOST)))) >> 6, 122 - nph));
  if (x < XMIN || rg > 31 || nph < 11) x = 0;
  const bool helps = x && !gives;
  if (x && gives) lenb = 16 * (npb - x);
  const int tend = ((tot - 32) * TAU16) >> 9;
  const int ftok = lenp - 16 * (npp - x);
  const int xh = helps ? x : 0, xw = ((xh + 1 + 2 * w) >> 2) * 2, sr = (x + 1) & 2;
  unsigned fullw = base + WORDS + (w == 2 ? sr * 32 : w * 64);

  if (wave < 8) {
    int tk = lenb;
    if (helps) {
      const bool fo = (unsigned)(lane - 4) < (unsigned)xw;
      pg = fo ? (pg - 8) ^ sr : lane < 4 ? pg : pg - 2 * xw;
      pgv = bt[fo ? pb * NBPS + min(npp - x + pg, NBPS - 1) : b * NBPS + pg] * 128 + (fo ? ph : h);
      tk = fo ? ftok : lenb;
    }
    const int npos = ((lenb + 63 - 32 * w) >> 6) * 2 + xw;
    const int rows = min(max(tk - 16 * pg, 0), 16);
    const int xr = (xh + 1) & ~1, no = (lenb + 15) >> 4, np = no + xr, n0 = (no >> 2) * 2 + min(no & 3, 2) + ((xh + 1) >> 2) * 2, ld = LEAD + 2 * n0 - np;
    const int slot = w ? min(2 * lane + ld, n0 + lane) : max(2 * lane + 1 - ld, lane);
    const int cost = 16 * slot + xc * min(max(slot - 7, 0), xr);
    const unsigned tab = rows ? (unsigned)((float)(cost * tend) * __builtin_amdgcn_rcpf((float)max(16 * (np - 1) + xc * xr, 1))) << 16 | rows << 11 : 0;
    int freed = 0;
    for (int j = wave & 3; j < npos; j += 4) {
      if (j > 3 || !early) {
        const unsigned e = rl(tab, j);
        while (freed + 7 < j) {
          freed = __builtin_amdgcn_readfirstlane(lds_ld(fullw + 32));
          if (freed + 7 < j) __builtin_amdgcn_s_sleep(SLP);
        }
        const int due = (e >> 16) + POFS;
        while ((int)(NOW() - t0) < due) __builtin_amdgcn_s_sleep(SLP2);
        issue(ring + (j & 7) * 8192, rl(pgv, j), e & 0xffff);
      }
      WAITVM0();
      lds_put(fullw + 4 * (j & 7), j + 1);
    }
    return;
  }

  char* const slot = ws + (unsigned)(gives ? b * 8 + h : pb * 8 + ph) * SLOT;
  unsigned tag = 0;
  if (w == 2) {
    if (!x) return;
    tag = __hip_atomic_load((gu32*)(slot + EPOCH_OFF), __ATOMIC_RELAXED, __HIP_MEMORY_SCOPE_AGENT) + TAGK;
    if (gives) {
      while ((int)(NOW() - t0) < tend - (tend >> 2)) __builtin_amdgcn_s_sleep(32);
#pragma nounroll
      for (int spin = 0; spin < (1 << 22); ++spin) {
        if (__builtin_amdgcn_readfirstlane(__hip_atomic_load((gu32*)(slot + FLAG_OFF), __ATOMIC_RELAXED, __HIP_MEMORY_SCOPE_AGENT) == tag)) break;
        __builtin_amdgcn_s_sleep(8);
      }
      asm volatile("" ::: "memory");
      const __amdgpu_buffer_rsrc_t pr = rsrc(slot, 4096 + 64);
      u32x4 y[4];
#pragma unroll
      for (int r = 0; r < 4; ++r) y[r] = __builtin_amdgcn_raw_buffer_load_b128(pr, lane * 16 + r * 1024, 0, 16);
      const unsigned lse = __builtin_amdgcn_raw_buffer_load_b32(pr, 4096 + 4 * c, 0, 16);
#pragma unroll
      for (int k = 0; k < 8; ++k) {
        const unsigned lo = y[k >> 1][2 * (k & 1)], hi = y[k >> 1][2 * (k & 1) + 1];
        *LDSP(u32x4, base + DUMP + DSZ + 1024 * k + 16 * lane) = u32x4{lo << 16, lo & 0xffff0000u, hi << 16, hi & 0xffff0000u};
      }
      *LDSP(u32x2, base + DUMP + DSZ + 8192 + 8 * lane) = u32x2{lse, g ? 0u : 0x3f800000u};
      asm volatile("s_waitcnt lgkmcnt(0)" ::: "memory");
      lds_put(base + WORDS + 128 + 36, 1);
      if (lane == 0) __hip_atomic_store((gu32*)(slot + EPOCH_OFF), tag, __ATOMIC_RELAXED, __HIP_MEMORY_SCOPE_AGENT);
      return;
    }
  }
  __builtin_amdgcn_s_setprio(3);
  if (w == 2) {
#pragma unroll
    for (int u = 0; u < 4; ++u) qf[u] = *(const u32x4*)(q + (pb * 8 + ph) * 4096 + c * 256 + g * 16 + 64 * u);
  }
  if ((unimask >> (w == 2 ? pb : b)) & 1) {
#pragma unroll
    for (int u = 0; u < 4; ++u) qf[u] = u32x4{0, 0, 0, 0};
  }
  const float CS = 0.088388347648318440f * 1.4426950408889634f;
  const unsigned ka = base + c * 256 + ((g ^ c) << 4);
  const unsigned va = base + 4096 + (lane >> 2) * 256 + ((((lane & 3) << 2) ^ (lane >> 2)) << 4);
  f32x4 acc[8];
#pragma unroll
  for (int k = 0; k < 8; ++k) acc[k] = f32x4{0.f, 0.f, 0.f, 0.f};
  float m = -1e30f, l = 0.f;
  asm volatile("" : "+v"(qf[0]), "+v"(qf[1]), "+v"(qf[2]), "+v"(qf[3]), "+v"(tag));
  int i = w == 2 ? 4 : 0, tokens = w == 2 ? ftok : lenb - 32 * w;
  const int dec = w == 2 ? 32 : 64, alt = w == 2 ? 64 : 0;
  for (int lim = w == 1 || (x && gives) ? 64 : 0;; lim = 0) {
    while (tokens > lim) {
      for (;;) {
        const uint64_t f = __hip_atomic_load(LDSP(uint64_t, fullw + 4 * (i & 7)), __ATOMIC_RELAXED, __HIP_MEMORY_SCOPE_WORKGROUP);
        if (__builtin_amdgcn_readfirstlane((unsigned)f + (unsigned)(f >> 32) + lds_ld(fullw + 32)) == 3 * i + 3) break;
      }
      asm volatile("" ::: "memory");
      const unsigned o = (i & 7) * 8192 + (fullw & 64) * 1024;
      u32x4 kf[2][4], vt[8];
#pragma unroll
      for (int u = 0; u < 4; ++u) {
        const unsigned a = (o + ka) ^ (u << 6);
        kf[0][u] = *LDSP(const u32x4, a);
        kf[1][u] = *LDSP(const u32x4, a + 8192);
      }
#pragma unroll
      for (int k = 0; k < 8; ++k) {
        const unsigned a = ((o + va) ^ ((k >> 1) << 4)) + 8 * (k & 1);
        const u32x2 lo = __builtin_bit_cast(u32x2, __builtin_amdgcn_ds_read_tr16_b64_v4i16(LDSP(s16x4, a)));
        const u32x2 hi = __builtin_bit_cast(u32x2, __builtin_amdgcn_ds_read_tr16_b64_v4i16(LDSP(s16x4, a + 8192)));
        vt[k] = u32x4{lo[0], lo[1], hi[0], hi[1]};
      }
      f32x4 sc[2] = {{0.f, 0.f, 0.f, 0.f}, {0.f, 0.f, 0.f, 0.f}};
#pragma unroll
      for (int u = 0; u < 4; ++u)
#pragma unroll
        for (int t = 0; t < 2; ++t)
          sc[t] = __builtin_amdgcn_mfma_f32_16x16x32_bf16(__builtin_bit_cast(bf16x8, kf[t][u]), __builtin_bit_cast(bf16x8, qf[u]), sc[t], 0, 0, 0);
      if (tokens < 32) {
#pragma unroll
        for (int r = 0; r < 8; ++r) sc[r >> 2][r & 3] = 16 * (r >> 2) + 4 * g + (r & 3) < tokens ? sc[r >> 2][r & 3] : -__builtin_inff();
      }
      tokens -= dec;
      const float mx = xg_max(vmax(vmax(vmax(sc[0][0], sc[0][1]), vmax(sc[0][2], sc[0][3])), vmax(vmax(sc[1][0], sc[1][1]), vmax(sc[1][2], sc[1][3]))));
      const float mn = vmax(m, mx * CS);
      const float alpha = __builtin_amdgcn_exp2f(m - mn);
      m = mn;
      float pr[8];
#pragma unroll
      for (int r = 0; r < 8; ++r) pr[r] = __builtin_amdgcn_exp2f(__builtin_fmaf(sc[r >> 2][r & 3], CS, -mn));
      asm volatile("" : "+v"(vt[0]), "+v"(vt[1]), "+v"(vt[2]), "+v"(vt[3]), "+v"(vt[4]), "+v"(vt[5]), "+v"(vt[6]), "+v"(vt[7]) :: "memory");
      lds_put(fullw + 32, i + 2);
      fullw ^= alt;
      i += (fullw ^ sr * 32) & alt ? 0 : i == 2 ? 2 + xw : 2;
      l = l * alpha + (((pr[0] + pr[1]) + (pr[2] + pr[3])) + ((pr[4] + pr[5]) + (pr[6] + pr[7])));
      const u32x4 pf = {cvtpk(pr[0], pr[1]), cvtpk(pr[2], pr[3]), cvtpk(pr[4], pr[5]), cvtpk(pr[6], pr[7])};
#pragma unroll
      for (int k = 0; k < 8; ++k)
        acc[k] = __builtin_amdgcn_mfma_f32_16x16x32_bf16(__builtin_bit_cast(bf16x8, vt[k]), __builtin_bit_cast(bf16x8, pf), acc[k] * alpha, 0, 0, 0);
    }
    if (!lim) break;
    while (!__builtin_amdgcn_readfirstlane(lds_ld(base + WORDS + 36 + (w ? 0 : 128)))) {}
    asm volatile("" ::: "memory");
    const unsigned src = base + DUMP + (w ? 0 : DSZ);
    const u32x2 y = *LDSP(const u32x2, src + 8192 + 8 * lane);
    const float mn = vmax(m, __uint_as_float(y[0]));
    const float alpha = __builtin_amdgcn_exp2f(m - mn), wt = __builtin_amdgcn_exp2f(__uint_as_float(y[0]) - mn);
    m = mn;
    l = l * alpha + __uint_as_float(y[1]) * wt;
#pragma unroll
    for (int k = 0; k < 8; ++k) acc[k] = acc[k] * alpha + *LDSP(const f32x4, src + 1024 * k + 16 * lane) * wt;
  }
  if (!w) {
#pragma unroll
    for (int k = 0; k < 8; ++k) *LDSP(f32x4, base + DUMP + 1024 * k + 16 * lane) = acc[k];
    *LDSP(u32x2, base + DUMP + 8192 + 8 * lane) = u32x2{__float_as_uint(m), __float_as_uint(l)};
    asm volatile("s_waitcnt lgkmcnt(0)" ::: "memory");
    lds_put(base + WORDS + 36, 1);
  } else {
    const float lt = xg_sum(l);
    const float inv = __builtin_amdgcn_rcpf(lt);
    u32x4 y[4];
#pragma unroll
    for (int r = 0; r < 4; ++r)
      y[r] = u32x4{cvtpk(acc[2 * r][0] * inv, acc[2 * r][1] * inv), cvtpk(acc[2 * r][2] * inv, acc[2 * r][3] * inv),
                   cvtpk(acc[2 * r + 1][0] * inv, acc[2 * r + 1][1] * inv), cvtpk(acc[2 * r + 1][2] * inv, acc[2 * r + 1][3] * inv)};
    if (w == 1) {
      const __amdgpu_buffer_rsrc_t orr = rsrc(out + (b * 8 + h) * 4096, 4096);
#pragma unroll
      for (int r = 0; r < 4; ++r) __builtin_amdgcn_raw_buffer_store_b128(y[r], orr, c * 256 + g * 64 + r * 16, 0, OPOL);
    } else {
      const __amdgpu_buffer_rsrc_t orr = rsrc(slot, 4096 + 64);
#pragma unroll
      for (int r = 0; r < 4; ++r) __builtin_amdgcn_raw_buffer_store_b128(y[r], orr, lane * 16 + r * 1024, 0, 16);
      __builtin_amdgcn_raw_buffer_store_b32(__float_as_uint(m + __builtin_amdgcn_logf(lt)), orr, lane * 4 + 4096, 0, 16);
      WAITVM0();
      if (lane == 0) __hip_atomic_store((gu32*)(slot + FLAG_OFF), tag, __ATOMIC_RELAXED, __HIP_MEMORY_SCOPE_AGENT);
      return;
    }
  }
}

extern "C" void kernel_launch(void* const* d_in, const int* in_sizes, int n_in, void* d_out, int out_size,
                              void* d_ws, size_t ws_size, hipStream_t stream) {
  pa_kernel<<<256, 704, 0, stream>>>((const char*)d_in[0], (const char*)d_in[1], (const char*)d_in[2],
                                     (const int*)d_in[3], (const int*)d_in[4], (char*)d_out, (char*)d_ws);
}
```

```cpp
#define D0 30
#include <hip/hip_runtime.h>
#include <hip/hip_bf16.h>
#include <stdint.h>

#define CAPS 68
#define FB0 4
#define QRND 1
#define LMAX 84
#define EDEL 60
#define SCOST 8
#define FETCH_AT (tend - 300 - 32 * rg)
#define TAU16 470
#define TSUB 16
#define KAP 300
#define LREF 88
#define LSL 0
#define LNEG 40
#define LCAP 100
#define BETA 6
#define OPOL 2
#define CPFS "0x1ec0"
#define KPOL " nt"
#define VPOL " nt"

typedef __attribute__((ext_vector_type(8))) __bf16 bf16x8;
typedef __attribute__((ext_vector_type(4))) short s16x4;
typedef __attribute__((ext_vector_type(4))) float f32x4;
typedef __attribute__((ext_vector_type(4))) unsigned u32x4;
typedef __attribute__((ext_vector_type(2))) unsigned u32x2;
typedef __attribute__((address_space(1))) unsigned gu32;
#define LDSP(T, a) ((__attribute__((address_space(3))) T*)(uintptr_t)(a))

constexpr int NBPS = 128, SKV = NBPS * 16, NS = 8;
constexpr unsigned DUMP = 2 * NS * 8192, DSZ = 8192 + 512, WORDS = DUMP + 2 * DSZ;
constexpr unsigned SLOT = 8192, FLAG_OFF = 4096 + 64, EPOCH_OFF = 4096 + 68;
constexpr unsigned TAGK = 0x9E3779B9u;

__device__ __forceinline__ unsigned cvtpk(float lo, float hi) {
  typedef __attribute__((ext_vector_type(2))) __bf16 bf16x2;
  bf16x2 r = {(__bf16)lo, (__bf16)hi};
  return __builtin_bit_cast(unsigned, r);
}
__device__ __forceinline__ float vmax(float a, float b) { return __builtin_elementwise_maximum(a, b); }
__device__ __forceinline__ float xg_max(float x) {
  auto s = __builtin_amdgcn_permlane16_swap(__float_as_uint(x), __float_as_uint(x), false, false);
  x = vmax(__uint_as_float(s[0]), __uint_as_float(s[1]));
  auto t = __builtin_amdgcn_permlane32_swap(__float_as_uint(x), __float_as_uint(x), false, false);
  return vmax(__uint_as_float(t[0]), __uint_as_float(t[1]));
}
__device__ __forceinline__ float xg_sum(float x) {
  auto s = __builtin_amdgcn_permlane16_swap(__float_as_uint(x), __float_as_uint(x), false, false);
  x = __uint_as_float(s[0]) + __uint_as_float(s[1]);
  auto t = __builtin_amdgcn_permlane32_swap(__float_as_uint(x), __float_as_uint(x), false, false);
  return __uint_as_float(t[0]) + __uint_as_float(t[1]);
}
__device__ __forceinline__ int scan32(int v) {
  v += __builtin_amdgcn_update_dpp(0, v, 0x111, 0xf, 0xf, false);
  v += __builtin_amdgcn_update_dpp(0, v, 0x112, 0xf, 0xf, false);
  v += __builtin_amdgcn_update_dpp(0, v, 0x114, 0xf, 0xf, false);
  v += __builtin_amdgcn_update_dpp(0, v, 0x118, 0xf, 0xf, false);
  v += __builtin_amdgcn_update_dpp(0, v, 0x142, 0xa, 0xf, false);
  return v;
}
__device__ __forceinline__ __amdgpu_buffer_rsrc_t rsrc(const void* p, unsigned n) {
  return __builtin_amdgcn_make_buffer_rsrc((void*)p, 0, n, 0x00020000);
}
__device__ __forceinline__ u32x4 srd(const void* p, unsigned n) {
  const uint64_t a = (uint64_t)p;
  return u32x4{(unsigned)a, (unsigned)(a >> 32) & 0xffffu, n, 0x00020000u};
}
__device__ __forceinline__ int rl(int v, int l) { return __builtin_amdgcn_readlane(v, l); }
__device__ __forceinline__ unsigned bal32(bool c) { return (unsigned)__builtin_amdgcn_ballot_w64(c); }
__device__ __forceinline__ int lds_ld(unsigned a) {
  return __hip_atomic_load(LDSP(int, a), __ATOMIC_RELAXED, __HIP_MEMORY_SCOPE_WORKGROUP);
}
__device__ __forceinline__ void lds_put(unsigned a, int v) {
  __hip_atomic_store(LDSP(int, a), v, __ATOMIC_RELAXED, __HIP_MEMORY_SCOPE_WORKGROUP);
}

#define DMA4(POL, dst, rs, v)                                                                         \
  do {                                                                                                \
    unsigned keep_;                                                                                   \
    asm volatile("s_mov_b32 %0, m0\n\ts_mov_b32 m0, %1\n\ts_nop 4\n\t"                                \
                 "buffer_load_dwordx4 %3, %2, 0 offen" POL " lds\n\t"                                 \
                 "buffer_load_dwordx4 %4, %2, 0 offen offset:1024" POL " lds\n\t"                     \
                 "buffer_load_dwordx4 %5, %2, 0 offen offset:2048" POL " lds\n\t"                     \
                 "buffer_load_dwordx4 %6, %2, 0 offen offset:3072" POL " lds\n\t"                     \
                 "s_mov_b32 m0, %0"                                                                   \
                 : "=&s"(keep_) : "s"(dst), "s"(rs), "v"(v[0]), "v"(v[1]), "v"(v[2]), "v"(v[3]) : "memory"); \
  } while (0)
#define WAITVM0() asm volatile("s_waitcnt vmcnt(0)" ::: "memory")
#define NOW() ((unsigned)__builtin_amdgcn_s_memrealtime())

__global__ void __launch_bounds__(704)
pa_kernel(const char* __restrict__ q, const char* __restrict__ kc, const char* __restrict__ vc,
          const int* __restrict__ bt, const int* __restrict__ lens, char* __restrict__ out, char* __restrict__ ws) {
  __shared__ __attribute__((aligned(16384))) char lds[WORDS + 192];
  int cpf;
  {
    uint64_t pc;
    asm volatile("s_getpc_b64 %1\n\tv_lshlrev_b32 %0, 4, %2\n\tv_min_u32 %0, " CPFS ", %0\n\tglobal_load_dword %0, %0, %1" : "=&v"(cpf), "=&s"(pc) : "v"(threadIdx.x));
  }
  const uint64_t t64 = __builtin_amdgcn_s_memrealtime();
  const int lane = threadIdx.x & 63;
  const int wave = __builtin_amdgcn_readfirstlane(threadIdx.x >> 6);
  const int b = blockIdx.x >> 3, h = (blockIdx.x - b) & 7;
  const int g = lane >> 4, c = lane & 15;
  const unsigned base = (unsigned)(uintptr_t)LDSP(char, lds);
  const int w = wave < 8 ? wave >> 2 : wave - 8;
  if (threadIdx.x - 640 < 48) lds_put(base + WORDS + 4 * lane, 0);

  const int pq = (wave >> 1 & 2) + (lane & 1);
  const int ly = lane < 32 ? lane : (lane ^ 16) & 31;
  unsigned go[4];
#pragma unroll
  for (int u = 0; u < 4; ++u) go[u] = (4 * u + g) * 2048 + ((c ^ (4 * u + g)) << 4) - 1024 * u;
  const unsigned ring = base + (w & 1) * 8 * 8192;
  unsigned first;
  int len, leny, pgk;
  asm volatile("s_waitcnt lgkmcnt(0)\n\ts_load_dword %3, %8, %9\n\tglobal_load_dword %0, %4, %7\n\tglobal_load_dword %1, %5, %7\n\tglobal_load_dword %2, %6, %8"
               : "=&v"(len), "=&v"(leny), "=&v"(pgk), "=&s"(first)
               : "v"(4 * (lane & 31)), "v"(4 * ly), "v"(4 * (b * NBPS + (2 * lane - 4 & 124) + pq)), "s"(lens), "s"(bt), "s"(4 * (b * NBPS + (wave >> 1 & 2) + (wave & 1))),
                 "s"(q), "s"(kc), "s"(vc), "s"(out), "s"(ws), "s"(t64), "v"(go[0]), "v"(go[1]), "v"(go[2]), "v"(go[3]), "s"(ring), "s"(h) : "memory");
  const unsigned t0 = t64;
  u32x4 qf[4];
  if ((wave & 14) == 8) {
#pragma unroll
    for (int u = 0; u < 4; ++u) qf[u] = *(const u32x4*)(q + (b * 8 + h) * 4096 + c * 256 + g * 16 + 64 * u);
  }
  const bool early = (wave & 10) == 2;
  asm volatile("s_barrier\n\ts_waitcnt lgkmcnt(0)" : "+s"(first) :: "memory");
  if (early) {
    const size_t po = (size_t)(first * 128 + h) * 256;
    const u32x4 kr = srd(kc + po, 32768), vr = srd(vc + po, 32768);
    if (w) while ((int)(NOW() - t0) < EDEL) {}
    DMA4(KPOL, ring + (wave & 1) * 8192, kr, go);
    DMA4(VPOL, ring + (wave & 3) * 8192 + 4096, vr, go);
  }
  asm volatile("s_waitcnt vmcnt(8)\n\ts_cmp_eq_u32 %4, 2\n\ts_cbranch_scc1 1f\n\ts_waitcnt vmcnt(0)\n1:"
               : "+v"(len), "+v"(leny), "+v"(pgk), "+v"(cpf) : "s"(wave & 10) : "scc", "memory");
  const unsigned unimask = bal32(len <= 0);
  if (len <= 0 || len > SKV) len = SKV;
  const int lenb = rl(len, b), qb = (lenb + 63) >> 6, rest = lenb + 64 - 64 * qb - 32 * w;
  pgk = pgk * 128 + h;
  const int last = rl(pgk, (2 * qb + (wave & 1)) & 63);
  const unsigned nrec = __builtin_amdgcn_readfirstlane(min(max(rest - 16 * (wave & 1), 0), 16) * 2048);
  if (early && qb > 1) {
    const u32x4 kr = srd(kc + (size_t)(unsigned)last * 256, nrec);
    DMA4(KPOL, ring + (wave & 3) * 8192, kr, go);
  }

  if (leny <= 0 || leny > SKV) leny = SKV;
  const int key = ((len + 15) >> 4) * 32 + (lane & 31), keyy = ((leny + 15) >> 4) * 32 + ly;
  unsigned less = (unsigned)(keyy - key) >> 31;
#define ROR(n) less = __builtin_amdgcn_alignbit(less, __builtin_amdgcn_update_dpp(0, keyy, 0x120 + n, 0xf, 0xf, true) - key, 31);
  ROR(1) ROR(2) ROR(3) ROR(4) ROR(5) ROR(6) ROR(7) ROR(8) ROR(9) ROR(10) ROR(11) ROR(12) ROR(13) ROR(14) ROR(15)
  int rank = __builtin_popcount(less);
  {
    auto t = __builtin_amdgcn_permlane32_swap(rank, rank, false, false);
    rank = t[0] + t[1];
  }
  const int tot = rl(scan32(key >> 5), 31), rb = rl(rank, b);
  const bool gives = (h & 3) == 1;
  const int rg = gives ? 31 - rb : 2 * rb + (h >> 1 & 1);
  const int ph = gives ? h - 1 + 2 * (rg & 1) : (h & 4) | 1;
  const int pb = __builtin_ctz(bal32(rank == (gives ? rg >> 1 : 31 - rg)) | 0x80000000u);
  const int npb = (lenb + 15) >> 4, npp = (rl(len, pb) + 15) >> 4;
  const int npg = gives ? npb : npp, nph = gives ? npp : npb;
  int nq = min(min((npg - ((tot * CAPS) >> 11) + QRND) >> 2, ((npg + 3) >> 2) - 2), min(((((tot * LMAX) >> 11) - nph) * 43) >> 8, (128 - nph) >> 2));
  if (nq < 0 || rg > 31 || nph < 11 || (h & 3) == 3) nq = 0;
  nq = __builtin_amdgcn_readfirstlane(nq);
  const int xw = gives ? 0 : 2 * nq, own = gives ? qb - nq : qb;
  const int tend = ((tot - TSUB) * TAU16) >> 9;
  unsigned fullw = base + WORDS + (w & 1) * 64;

  if (wave < 8) {
    int pgv = pgk;
    if (xw) {
      const int fp = pb * NBPS + 4 * (((npp + 3) >> 2) - 1 - nq) + pq, op = b * NBPS + pq - 4 - 2 * xw;
      const bool fk = (unsigned)(lane - 4) < (unsigned)xw, fv = (unsigned)(lane - 2) < (unsigned)xw;
      if (lane > 3) pgk = bt[(fk ? fp - 8 : op) + (2 * lane & 124)] * 128 + (fk ? ph : h);
      if (lane > 1) pgv = bt[(fv ? fp - 4 : op) + (2 * lane & 124)] * 128 + (fv ? ph : h);
    }
    const bool tail = (lane & ~1) == xw + 2;
    if (tail) pgv = last;
    const int nk = 2 * own + xw, nt = lenb + 64 * (own - qb) + 32 * xw, gp = (2 * lane & 124) + pq, gi = 16 * gp + nt - 32 * nk;
    const int cost = min(gi + 64, nt) + max(gi, min(16 * gp, nt + 128 - 32 * nk)) + SCOST * 2 * min(max(gp - 4, 0), 2 * xw);
    const float rc = __builtin_amdgcn_rcpf((float)(2 * nt + SCOST * 4 * xw)) * (float)max(tend - D0, 0);
    const unsigned tab = ((unsigned)((float)cost * rc) + D0) << 16 | (tail ? nrec : 32768);
    const int kap = __builtin_amdgcn_readfirstlane((int)(rc * (32 * KAP)));
    int freed = 0;
    for (int j = (wave + 2 & 3) + 2; j < nk + 2; j += 4) {
      unsigned ti;
      if (j > 3) {
        const unsigned e = rl(tab, j - 2);
        const int due = e >> 16;
        const u32x4 kr = srd(kc + (size_t)(unsigned)rl(pgk, j & 63) * 256, 32768), vr = srd(vc + (size_t)(unsigned)rl(pgv, j - 2) * 256, e & 0xffff);
        while (freed + 7 < j) {
          freed = __builtin_amdgcn_readfirstlane(lds_ld(fullw + 32));
          if (freed + 7 < j) __builtin_amdgcn_s_sleep(1);
        }
        int late;
        do {
          ti = NOW();
          late = ti - t0 - due;
        } while (late < __builtin_amdgcn_readfirstlane(lds_ld(base + WORDS + 40)) >> 16);
        ti += late * BETA >> 4;
        if (j < nk) DMA4(KPOL, ring + (j & 7) * 8192, kr, go);
        DMA4(VPOL, ring + (j & 7) * 8192 + 4096, vr, go);
      }
      WAITVM0();
      lds_put(fullw + 4 * (j & 7), j + 1);
      if (j < 4) lds_put(fullw + 4 * j - 8, j - 1);
      else if (j >= FB0) {
        const int ex = min(max((int)(NOW() - ti) - LREF - ((rl(pgv, j - 2) & 3) == 1 ? LSL : 0), -LNEG), LCAP) * kap;
        if (lane == 0) {
          __hip_atomic_fetch_add(LDSP(int, base + WORDS + 40), ex, __ATOMIC_RELAXED, __HIP_MEMORY_SCOPE_WORKGROUP);
          __hip_atomic_fetch_max(LDSP(int, base + WORDS + 40), 0, __ATOMIC_RELAXED, __HIP_MEMORY_SCOPE_WORKGROUP);
        }
      }
    }
    return;
  }

  char* const slot = ws + (unsigned)(gives ? b * 8 + h : pb * 8 + ph) * SLOT;
  unsigned tag = 0;
  if (w == 2) {
    if (!nq) return;
    tag = __hip_atomic_load((gu32*)(slot + EPOCH_OFF), __ATOMIC_RELAXED, __HIP_MEMORY_SCOPE_AGENT) + TAGK;
    if (gives) {
      while ((int)(NOW() - t0) < FETCH_AT) __builtin_amdgcn_s_sleep(32);
#pragma nounroll
      for (int spin = 0; spin < (1 << 22); ++spin) {
        if (__builtin_amdgcn_readfirstlane(__hip_atomic_load((gu32*)(slot + FLAG_OFF), __ATOMIC_RELAXED, __HIP_MEMORY_SCOPE_AGENT) == tag)) break;
        __builtin_amdgcn_s_sleep(8);
      }
      asm volatile("" ::: "memory");
      const __amdgpu_buffer_rsrc_t pr = rsrc(slot, 4096 + 64);
      u32x4 y[4];
#pragma unroll
      for (int r = 0; r < 4; ++r) y[r] = __builtin_amdgcn_raw_buffer_load_b128(pr, lane * 16 + r * 1024, 0, 16);
      const unsigned lse = __builtin_amdgcn_raw_buffer_load_b32(pr, 4096 + 4 * c, 0, 16);
#pragma unroll
      for (int k = 0; k < 8; ++k) {
        const unsigned lo = y[k >> 1][2 * (k & 1)], hi = y[k >> 1][2 * (k & 1) + 1];
        *LDSP(u32x4, base + DUMP + DSZ + 1024 * k + 16 * lane) = u32x4{lo << 16, lo & 0xffff0000u, hi << 16, hi & 0xffff0000u};
      }
      *LDSP(unsigned, base + DUMP + DSZ + 8192 + 4 * lane) = lse;
      asm volatile("s_waitcnt lgkmcnt(0)" ::: "memory");
      lds_put(base + WORDS + 128 + 36, 2);
      while (__builtin_amdgcn_readfirstlane(lds_ld(base + WORDS + 36)) < 2) __builtin_amdgcn_s_sleep(8);
      if (lane == 0) __hip_atomic_store((gu32*)(slot + EPOCH_OFF), tag, __ATOMIC_RELAXED, __HIP_MEMORY_SCOPE_AGENT);
      return;
    }
  }
  __builtin_amdgcn_s_setprio(3);
  if (w == 2) {
#pragma unroll
    for (int u = 0; u < 4; ++u) qf[u] = *(const u32x4*)(q + (pb * 8 + ph) * 4096 + c * 256 + g * 16 + 64 * u);
  }
  if ((unimask >> (w == 2 ? pb : b)) & 1) {
#pragma unroll
    for (int u = 0; u < 4; ++u) qf[u] = u32x4{0, 0, 0, 0};
  }
  const float CS = 0.088388347648318440f * 1.4426950408889634f;
  const unsigned ka = base + c * 256 + ((g ^ c) << 4);
  const unsigned va = base + 4096 + (lane >> 2) * 256 + ((((lane & 3) << 2) ^ (lane >> 2)) << 4);
  f32x4 acc[8];
#pragma unroll
  for (int k = 0; k < 8; ++k) acc[k] = f32x4{0.f, 0.f, 0.f, 0.f};
  float m = -1e30f, l = 0.f, alpha, pr[8];
  u32x4 pf = {0, 0, 0, 0};
  asm volatile("" : "+v"(qf[0]), "+v"(qf[1]), "+v"(qf[2]), "+v"(qf[3]), "+v"(tag));
  auto landed = [&](unsigned fw, int i) __attribute__((always_inline)) {
    for (;;) {
      const uint64_t f = __hip_atomic_load(LDSP(uint64_t, fw + 4 * (i & 7)), __ATOMIC_RELAXED, __HIP_MEMORY_SCOPE_WORKGROUP);
      if (__builtin_amdgcn_readfirstlane((unsigned)f + (unsigned)(f >> 32) + lds_ld(fw + 32)) == 3 * i + 3) break;
    }
    asm volatile("" ::: "memory");
  };
  auto vblock = [&](unsigned o, int k) __attribute__((always_inline)) {
    const unsigned a = ((o + va) ^ ((k >> 1) << 4)) + 8 * (k & 1);
    const u32x2 lo = __builtin_bit_cast(u32x2, __builtin_amdgcn_ds_read_tr16_b64_v4i16(LDSP(s16x4, a)));
    const u32x2 hi = __builtin_bit_cast(u32x2, __builtin_amdgcn_ds_read_tr16_b64_v4i16(LDSP(s16x4, a + 8192)));
    return u32x4{lo[0], lo[1], hi[0], hi[1]};
  };
#define PV(k) acc[k] = __builtin_amdgcn_mfma_f32_16x16x32_bf16(__builtin_bit_cast(bf16x8, vt[k]), __builtin_bit_cast(bf16x8, pf), acc[k], 0, 0, 0)
#define PIN() __builtin_amdgcn_sched_barrier(0)
  int i = w == 2 ? 4 : 0, iv = 2, steps = w == 2 ? xw : own;
  const int alt = w == 2 ? 64 : 0, cut = qb > 1 ? 2 : 0;
  unsigned fullv = fullw, ov = 4 * 8192;
  for (;;) {
    landed(fullw, i);
    const unsigned o = (i & 7) * 8192 + (fullw & 64) * 1024;
    if (w != 2) { ov = i ? o : o + 2 * 8192; iv = i; }
    u32x4 kf[2][4], vt[8];
#pragma unroll
    for (int u = 0; u < 4; ++u) {
      const unsigned a = (o + ka) ^ (u << 6);
      kf[0][u] = *LDSP(const u32x4, a);
      kf[1][u] = *LDSP(const u32x4, a + 8192);
    }
#pragma unroll
    for (int k = 0; k < 8; ++k) vt[k] = vblock(ov, k);
    PIN();
    f32x4 sc[2] = {{0.f, 0.f, 0.f, 0.f}, {0.f, 0.f, 0.f, 0.f}};
#pragma unroll
    for (int u = 0; u < 4; ++u)
#pragma unroll
      for (int t = 0; t < 2; ++t)
        sc[t] = __builtin_amdgcn_mfma_f32_16x16x32_bf16(__builtin_bit_cast(bf16x8, kf[t][u]), __builtin_bit_cast(bf16x8, qf[u]), sc[t], 0, 0, 0);
    PIN();
    PV(0);
    if (i == cut) {
#pragma unroll
      for (int r = 0; r < 8; ++r) sc[r >> 2][r & 3] = 16 * (r >> 2) + 4 * g + (r & 3) < rest ? sc[r >> 2][r & 3] : -__builtin_inff();
    }
    const float mx = xg_max(vmax(vmax(vmax(sc[0][0], sc[0][1]), vmax(sc[0][2], sc[0][3])), vmax(vmax(sc[1][0], sc[1][1]), vmax(sc[1][2], sc[1][3]))));
    const float mn = vmax(m, mx * CS);
    alpha = __builtin_amdgcn_exp2f(m - mn);
    m = mn;
#pragma unroll
    for (int r = 0; r < 8; ++r) pr[r] = __builtin_amdgcn_exp2f(__builtin_fmaf(sc[r >> 2][r & 3], CS, -mn));
#pragma unroll
    for (int k = 1; k < 8; ++k) PV(k);
#pragma unroll
    for (int k = 1; k < 8; ++k) {
      __builtin_amdgcn_sched_group_barrier(0x008, 1, 0);
      __builtin_amdgcn_sched_group_barrier(0x002, 2, 0);
    }
    asm volatile("" :: "v"(vt[7]) : "memory");
    lds_put(fullv + 32, iv + 2);
    fullv = fullw; ov = o; iv = i;
    fullw ^= alt;
    i += fullw & alt ? 0 : i == 2 ? 2 + xw : 2;
    l = l * alpha + (((pr[0] + pr[1]) + (pr[2] + pr[3])) + ((pr[4] + pr[5]) + (pr[6] + pr[7])));
    if (!--steps) break;
    pf = u32x4{cvtpk(pr[0], pr[1]), cvtpk(pr[2], pr[3]), cvtpk(pr[4], pr[5]), cvtpk(pr[6], pr[7])};
#pragma unroll
    for (int k = 0; k < 8; ++k) acc[k] *= alpha;
  }
  const bool more = w == 1 || (nq && gives);
  const unsigned src = base + DUMP + (w ? 0 : DSZ), ready = base + WORDS + 36 + (w ? 0 : 128);
  float lt = xg_sum(l), e = -__builtin_inff();
  if (more) {
    while (!__builtin_amdgcn_readfirstlane(lds_ld(ready))) {}
    asm volatile("" ::: "memory");
    e = *LDSP(const float, src + 8192 + 4 * lane);
  }
  const float mn = vmax(m, e), mine = __builtin_amdgcn_exp2f(m - mn), wt = __builtin_amdgcn_exp2f(e - mn);
  lt = lt * mine + wt;
  const float inv = __builtin_amdgcn_rcpf(lt), lse = mn + __builtin_amdgcn_logf(lt), f = mine * inv;
  if (!w) {
    *LDSP(float, base + DUMP + 8192 + 4 * lane) = lse;
    asm volatile("s_waitcnt lgkmcnt(0)" ::: "memory");
    lds_put(base + WORDS + 36, 1);
  }
  pf = u32x4{cvtpk(pr[0] * f, pr[1] * f), cvtpk(pr[2] * f, pr[3] * f), cvtpk(pr[4] * f, pr[5] * f), cvtpk(pr[6] * f, pr[7] * f)};
#pragma unroll
  for (int k = 0; k < 8; ++k) acc[k] *= alpha * f;
  asm volatile("" : "+v"(pf), "+v"(acc[0]), "+v"(acc[1]), "+v"(acc[2]), "+v"(acc[3]), "+v"(acc[4]), "+v"(acc[5]), "+v"(acc[6]), "+v"(acc[7]));
  if (more) {
    while (__builtin_amdgcn_readfirstlane(lds_ld(ready)) < 2) {}
    asm volatile("" ::: "memory");
#pragma unroll
    for (int k = 0; k < 8; ++k) acc[k] += *LDSP(const f32x4, src + 1024 * k + 16 * lane) * (wt * inv);
    asm volatile("" : "+v"(acc[0]), "+v"(acc[1]), "+v"(acc[2]), "+v"(acc[3]), "+v"(acc[4]), "+v"(acc[5]), "+v"(acc[6]), "+v"(acc[7]));
  }
  if (w != 2) { iv = i; ov = (i & 7) * 8192 + (fullw & 64) * 1024; }
  landed(fullv, iv);
  {
    u32x4 vt[8];
#pragma unroll
    for (int k = 0; k < 8; ++k) vt[k] = vblock(ov, k);
    PIN();
#pragma unroll
    for (int k = 0; k < 8; ++k) PV(k);
  }
  if (!w) {
#pragma unroll
    for (int k = 0; k < 8; ++k) *LDSP(f32x4, base + DUMP + 1024 * k + 16 * lane) = acc[k];
    asm volatile("s_waitcnt lgkmcnt(0)" ::: "memory");
    lds_put(base + WORDS + 36, 2);
  } else {
    u32x4 y[4];
#pragma unroll
    for (int r = 0; r < 4; ++r)
      y[r] = u32x4{cvtpk(acc[2 * r][0], acc[2 * r][1]), cvtpk(acc[2 * r][2], acc[2 * r][3]),
                   cvtpk(acc[2 * r + 1][0], acc[2 * r + 1][1]), cvtpk(acc[2 * r + 1][2], acc[2 * r + 1][3])};
    if (w == 1) {
      const __amdgpu_buffer_rsrc_t orr = rsrc(out + (b * 8 + h) * 4096, 4096);
#pragma unroll
      for (int r = 0; r < 4; ++r) __builtin_amdgcn_raw_buffer_store_b128(y[r], orr, c * 256 + g * 64 + r * 16, 0, OPOL);
    } else {
      lds_put(fullv + 32, iv + 2);
      const __amdgpu_buffer_rsrc_t orr = rsrc(slot, 4096 + 64);
#pragma unroll
      for (int r = 0; r < 4; ++r) __builtin_amdgcn_raw_buffer_store_b128(y[r], orr, lane * 16 + r * 1024, 0, 16);
      __builtin_amdgcn_raw_buffer_store_b32(__float_as_uint(lse), orr, lane * 4 + 4096, 0, 16);
      WAITVM0();
      if (lane == 0) __hip_atomic_store((gu32*)(slot + FLAG_OFF), tag, __ATOMIC_RELAXED, __HIP_MEMORY_SCOPE_AGENT);
      return;
    }
  }
}

extern "C" void kernel_launch(void* const* d_in, const int* in_sizes, int n_in, void* d_out, int out_size,
                              void* d_ws, size_t ws_size, hipStream_t stream) {
  pa_kernel<<<256, 704, 0, stream>>>((const char*)d_in[0], (const char*)d_in[1], (const char*)d_in[2],
                                     (const int*)d_in[3], (const int*)d_in[4], (char*)d_out, (char*)d_ws);
}
```

```cpp
#define TAU16 455
#include <hip/hip_runtime.h>
#include <hip/hip_bf16.h>
#include <stdint.h>

#define CAPS 68
#define D0 10
#define FB0 4
#define QRND 1
#define LMAX 84
#define EDEL 60
#define SCOST 8
#define FETCH_AT (tend - 300 - 32 * rg)
#define TSUB 38
#define KAP 300
#define LREF 88
#define LSL 0
#define LNEG 40
#define LCAP 100
#define BETA 6
#define OPOL 2
#define CPFS "0x2060"
#define KPOL " nt"
#define VPOL " nt"

typedef __attribute__((ext_vector_type(8))) __bf16 bf16x8;
typedef __attribute__((ext_vector_type(4))) short s16x4;
typedef __attribute__((ext_vector_type(4))) float f32x4;
typedef __attribute__((ext_vector_type(4))) unsigned u32x4;
typedef __attribute__((ext_vector_type(2))) unsigned u32x2;
typedef __attribute__((address_space(1))) unsigned gu32;
#define LDSP(T, a) ((__attribute__((address_space(3))) T*)(uintptr_t)(a))

constexpr int NBPS = 128, SKV = NBPS * 16, NS = 8;
constexpr unsigned DUMP = 2 * NS * 8192, DSZ = 8192 + 512, WORDS = DUMP + 2 * DSZ;
constexpr unsigned CNT = WORDS + 192, TAB = CNT + 256;
constexpr unsigned SLOT = 8192, FLAG_OFF = 4096 + 64, EPOCH_OFF = 4096 + 68;
constexpr unsigned TAGK = 0x9E3779B9u;

__device__ __forceinline__ unsigned cvtpk(float lo, float hi) {
  typedef __attribute__((ext_vector_type(2))) __bf16 bf16x2;
  bf16x2 r = {(__bf16)lo, (__bf16)hi};
  return __builtin_bit_cast(unsigned, r);
}
__device__ __forceinline__ float vmax(float a, float b) { return __builtin_elementwise_maximum(a, b); }
__device__ __forceinline__ float xg_max(float x) {
  auto s = __builtin_amdgcn_permlane16_swap(__float_as_uint(x), __float_as_uint(x), false, false);
  x = vmax(__uint_as_float(s[0]), __uint_as_float(s[1]));
  auto t = __builtin_amdgcn_permlane32_swap(__float_as_uint(x), __float_as_uint(x), false, false);
  return vmax(__uint_as_float(t[0]), __uint_as_float(t[1]));
}
__device__ __forceinline__ float xg_sum(float x) {
  auto s = __builtin_amdgcn_permlane16_swap(__float_as_uint(x), __float_as_uint(x), false, false);
  x = __uint_as_float(s[0]) + __uint_as_float(s[1]);
  auto t = __builtin_amdgcn_permlane32_swap(__float_as_uint(x), __float_as_uint(x), false, false);
  return __uint_as_float(t[0]) + __uint_as_float(t[1]);
}
__device__ __forceinline__ int scan32(int v) {
  v += __builtin_amdgcn_update_dpp(0, v, 0x111, 0xf, 0xf, false);
  v += __builtin_amdgcn_update_dpp(0, v, 0x112, 0xf, 0xf, false);
  v += __builtin_amdgcn_update_dpp(0, v, 0x114, 0xf, 0xf, false);
  v += __builtin_amdgcn_update_dpp(0, v, 0x118, 0xf, 0xf, false);
  v += __builtin_amdgcn_update_dpp(0, v, 0x142, 0xa, 0xf, false);
  return v;
}
__device__ __forceinline__ __amdgpu_buffer_rsrc_t rsrc(const void* p, unsigned n) {
  return __builtin_amdgcn_make_buffer_rsrc((void*)p, 0, n, 0x00020000);
}
__device__ __forceinline__ u32x4 srd(const void* p, unsigned n) {
  const uint64_t a = (uint64_t)p;
  return u32x4{(unsigned)a, (unsigned)(a >> 32) & 0xffffu, n, 0x00020000u};
}
__device__ __forceinline__ int rl(int v, int l) { return __builtin_amdgcn_readlane(v, l); }
__device__ __forceinline__ unsigned bal32(bool c) { return (unsigned)__builtin_amdgcn_ballot_w64(c); }
__device__ __forceinline__ int lds_ld(unsigned a) {
  return __hip_atomic_load(LDSP(int, a), __ATOMIC_RELAXED, __HIP_MEMORY_SCOPE_WORKGROUP);
}
__device__ __forceinline__ void lds_put(unsigned a, int v) {
  __hip_atomic_store(LDSP(int, a), v, __ATOMIC_RELAXED, __HIP_MEMORY_SCOPE_WORKGROUP);
}

#define DMA4(POL, dst, rs, v)                                                                         \
  do {                                                                                                \
    unsigned keep_;                                                                                   \
    asm volatile("s_mov_b32 %0, m0\n\ts_mov_b32 m0, %1\n\ts_nop 4\n\t"                                \
                 "buffer_load_dwordx4 %3, %2, 0 offen" POL " lds\n\t"                                 \
                 "buffer_load_dwordx4 %4, %2, 0 offen offset:1024" POL " lds\n\t"                     \
                 "buffer_load_dwordx4 %5, %2, 0 offen offset:2048" POL " lds\n\t"                     \
                 "buffer_load_dwordx4 %6, %2, 0 offen offset:3072" POL " lds\n\t"                     \
                 "s_mov_b32 m0, %0"                                                                   \
                 : "=&s"(keep_) : "s"(dst), "s"(rs), "v"(v[0]), "v"(v[1]), "v"(v[2]), "v"(v[3]) : "memory"); \
  } while (0)
#define WAITVM0() asm volatile("s_waitcnt vmcnt(0)" ::: "memory")
#define NOW() ((unsigned)__builtin_amdgcn_s_memrealtime())

__global__ void __launch_bounds__(704)
pa_kernel(const char* __restrict__ q, const char* __restrict__ kc, const char* __restrict__ vc,
          const int* __restrict__ bt, const int* __restrict__ lens, char* __restrict__ out, char* __restrict__ ws) {
  __shared__ __attribute__((aligned(16384))) char lds[TAB + 512];
  int cpf;
  {
    uint64_t pc;
    asm volatile("s_getpc_b64 %1\n\tv_lshlrev_b32 %0, 4, %2\n\tv_min_u32 %0, " CPFS ", %0\n\tglobal_load_dword %0, %0, %1" : "=&v"(cpf), "=&s"(pc) : "v"(threadIdx.x));
  }
  const uint64_t t64 = __builtin_amdgcn_s_memrealtime();
  const int lane = threadIdx.x & 63;
  const int wave = __builtin_amdgcn_readfirstlane(threadIdx.x >> 6);
  const int b = blockIdx.x >> 3, h = (blockIdx.x - b) & 7;
  const int g = lane >> 4, c = lane & 15;
  const unsigned base = (unsigned)(uintptr_t)LDSP(char, lds);
  const int w = wave < 8 ? wave >> 2 : wave - 8;
  if (threadIdx.x - 640 < 56) *LDSP(u32x2, base + WORDS + 8 * lane) = u32x2{0, 0};

  const int pq = (wave >> 1 & 2) + (lane & 1);
  const int ly = lane < 32 ? lane : (lane ^ 16) & 31;
  unsigned go[4];
#pragma unroll
  for (int u = 0; u < 4; ++u) go[u] = (4 * u + g) * 2048 + ((c ^ (4 * u + g)) << 4) - 1024 * u;
  const unsigned ring = base + (w & 1) * 8 * 8192;
  unsigned first;
  int len, leny;
  u32x2 pg;
  asm volatile("s_waitcnt lgkmcnt(0)\n\ts_load_dword %3, %8, %9\n\tglobal_load_dword %0, %4, %7\n\tglobal_load_dword %1, %5, %7\n\tglobal_load_dwordx2 %2, %6, %8"
               : "=&v"(len), "=&v"(leny), "=&v"(pg), "=&s"(first)
               : "v"(4 * (lane & 31)), "v"(4 * ly), "v"(4 * b * NBPS + 8 * lane), "s"(lens), "s"(bt), "s"(4 * (b * NBPS + (wave >> 1 & 2) + (wave & 1))),
                 "s"(q), "s"(kc), "s"(vc), "s"(out), "s"(ws), "s"(t64), "v"(go[0]), "v"(go[1]), "v"(go[2]), "v"(go[3]), "s"(ring), "s"(h) : "memory");
  const unsigned t0 = t64;
  u32x4 qf[4];
  if ((wave & 14) == 8) {
#pragma unroll
    for (int u = 0; u < 4; ++u) qf[u] = *(const u32x4*)(q + (b * 8 + h) * 4096 + c * 256 + g * 16 + 64 * u);
  }
  const bool early = (wave & 10) == 2;
  asm volatile("s_barrier\n\ts_waitcnt lgkmcnt(0)" : "+s"(first) :: "memory");
  if (early) {
    const size_t po = (size_t)(first * 128 + h) * 256;
    const u32x4 kr = srd(kc + po, 32768), vr = srd(vc + po, 32768);
    if (w) while ((int)(NOW() - t0) < EDEL) {}
    DMA4(KPOL, ring + (wave & 1) * 8192, kr, go);
    DMA4(VPOL, ring + (wave & 3) * 8192 + 4096, vr, go);
  }
  asm volatile("s_waitcnt vmcnt(8)\n\ts_cmp_eq_u32 %4, 2\n\ts_cbranch_scc1 1f\n\ts_waitcnt vmcnt(0)\n1:"
               : "+v"(len), "+v"(leny), "+v"(pg), "+v"(cpf) : "s"(wave & 10) : "scc", "memory");
  const unsigned unimask = bal32(len <= 0);
  if (len <= 0 || len > SKV) len = SKV;
  const int lenb = rl(len, b), qb = (lenb + 63) >> 6, rest = lenb + 64 - 64 * qb - 32 * w;
  const int pgr = (wave & 1 ? pg[1] : pg[0]) * 128 + h, last = rl(pgr, 2 * qb - 2 + (w & 1));
  const unsigned nrec = __builtin_amdgcn_readfirstlane(min(max(rest - 16 * (wave & 1), 0), 16) * 2048);
  if (early && qb > 1) {
    const u32x4 kr = srd(kc + (size_t)(unsigned)last * 256, nrec);
    DMA4(KPOL, ring + (wave & 3) * 8192, kr, go);
  }

  if (leny <= 0 || leny > SKV) leny = SKV;
  const int key = ((len + 15) >> 4) * 32 + (lane & 31), keyy = ((leny + 15) >> 4) * 32 + ly;
  unsigned less = (unsigned)(keyy - key) >> 31;
#define ROR(n) less = __builtin_amdgcn_alignbit(less, __builtin_amdgcn_update_dpp(0, keyy, 0x120 + n, 0xf, 0xf, true) - key, 31);
  ROR(1) ROR(2) ROR(3) ROR(4) ROR(5) ROR(6) ROR(7) ROR(8) ROR(9) ROR(10) ROR(11) ROR(12) ROR(13) ROR(14) ROR(15)
  int rank = __builtin_popcount(less);
  {
    auto t = __builtin_amdgcn_permlane32_swap(rank, rank, false, false);
    rank = t[0] + t[1];
  }
  const int tot = rl(scan32(key >> 5), 31), rb = rl(rank, b);
  const bool gives = (h & 3) == 1;
  const int rg = gives ? 31 - rb : 2 * rb + (h >> 1 & 1);
  const int ph = gives ? h - 1 + 2 * (rg & 1) : (h & 4) | 1;
  const int pb = __builtin_ctz(bal32(rank == (gives ? rg >> 1 : 31 - rg)) | 0x80000000u);
  const int npb = (lenb + 15) >> 4, npp = (rl(len, pb) + 15) >> 4;
  const int npg = gives ? npb : npp, nph = gives ? npp : npb;
  int nq = min(min((npg - ((tot * CAPS) >> 11) + QRND) >> 2, ((npg + 3) >> 2) - 2), min(((((tot * LMAX) >> 11) - nph) * 43) >> 8, (128 - nph) >> 2));
  if (nq < 0 || rg > 31 || nph < 11 || (h & 3) == 3) nq = 0;
  nq = __builtin_amdgcn_readfirstlane(nq);
  const int xw = gives ? 0 : 2 * nq, own = gives ? qb - nq : qb;
  const int tend = ((tot - TSUB) * TAU16) >> 9;
  unsigned fullw = base + WORDS + (w & 1) * 64;

  if (wave < 8) {
    int pgk = rl(pgr, 2 + w), pgv = last;
    if (xw) {
      const int fp = pb * NBPS + 4 * (((npp + 3) >> 2) - 1 - nq) + pq + (2 * lane & 124);
      if ((unsigned)(lane - 4) < (unsigned)xw) pgk = bt[fp - 8] * 128 + ph;
      if ((unsigned)(lane - 2) < (unsigned)xw) pgv = bt[fp - 4] * 128 + ph;
    }
    const bool tail = (lane & ~1) == xw + 2;
    const int nk = 2 * own + xw, nt = lenb + 64 * (own - qb) + 32 * xw, gp = (2 * lane & 124) + pq, gi = 16 * gp + nt - 32 * nk;
    const int cost = min(gi + 64, nt) + max(gi, min(16 * gp, nt + 128 - 32 * nk)) + SCOST * 2 * min(max(gp - 4, 0), 2 * xw);
    const float rc = __builtin_amdgcn_rcpf((float)(2 * nt + SCOST * 4 * xw)) * (float)max(tend - D0, 0);
    const unsigned tab = ((unsigned)((float)cost * rc) + D0) << 16 | (tail ? nrec : 32768);
    const int kap = __builtin_amdgcn_readfirstlane((int)(rc * (32 * KAP)));
    int freed = 0;
    for (int j = (wave + 2 & 3) + 2; j < nk + 2; j += 4) {
      unsigned ti;
      if (j > 3) {
        const unsigned e = rl(tab, j - 2);
        const int due = e >> 16;
        const u32x4 kr = srd(kc + (size_t)(unsigned)rl(pgk, j & 63) * 256, 32768), vr = srd(vc + (size_t)(unsigned)rl(pgv, j - 2) * 256, e & 0xffff);
        while (freed + 7 < j) {
          freed = __builtin_amdgcn_readfirstlane(lds_ld(fullw + 32));
          if (freed + 7 < j) __builtin_amdgcn_s_sleep(1);
        }
        int late;
        do {
          ti = NOW();
          late = ti - t0 - due;
        } while (late < __builtin_amdgcn_readfirstlane(lds_ld(base + WORDS + 40)) >> 16);
        ti += late * BETA >> 4;
        if (j < nk) DMA4(KPOL, ring + (j & 7) * 8192, kr, go);
        DMA4(VPOL, ring + (j & 7) * 8192 + 4096, vr, go);
      }
      WAITVM0();
      lds_put(fullw + 4 * (j & 7), j + 1);
      if (j < 4) lds_put(fullw + 4 * j - 8, j - 1);
      else if (j >= FB0) {
        const int ex = min(max((int)(NOW() - ti) - LREF - ((rl(pgv, j - 2) & 3) == 1 ? LSL : 0), -LNEG), LCAP) * kap;
        if (lane == 0) {
          __hip_atomic_fetch_add(LDSP(int, base + WORDS + 40), ex, __ATOMIC_RELAXED, __HIP_MEMORY_SCOPE_WORKGROUP);
          __hip_atomic_fetch_max(LDSP(int, base + WORDS + 40), 0, __ATOMIC_RELAXED, __HIP_MEMORY_SCOPE_WORKGROUP);
        }
      }
      if (j < 6) {
        while (!__builtin_amdgcn_readfirstlane(lds_ld(base + WORDS + 44))) {}
        asm volatile("" ::: "memory");
        if (lane >= xw + 4) pgk = pgv = *LDSP(const int, base + TAB + 4 * (gp - 4 - 2 * xw));
      }
    }
    return;
  }

  char* const slot = ws + (unsigned)(gives ? b * 8 + h : pb * 8 + ph) * SLOT;
  unsigned tag = 0;
  if (w == 2) {
    {
      const bool mid = lane >= 4 && lane < 2 * own - 2;
      const unsigned k0 = min(pg[0] >> 6, 63u), k1 = min(pg[1] >> 6, 63u);
      int p0 = 2 * lane, p1 = p0 + 1;
      if (mid) {
        p0 = __hip_atomic_fetch_add(LDSP(int, base + CNT + 4 * k0), 1, __ATOMIC_RELAXED, __HIP_MEMORY_SCOPE_WORKGROUP);
        p1 = __hip_atomic_fetch_add(LDSP(int, base + CNT + 4 * k1), 1, __ATOMIC_RELAXED, __HIP_MEMORY_SCOPE_WORKGROUP);
      }
      const int n = lds_ld(base + CNT + 4 * lane);
      int s = scan32(n);
      s += __builtin_amdgcn_update_dpp(0, s, 0x143, 0xc, 0xf, false) + 8 - n;
      const int s0 = __builtin_amdgcn_ds_bpermute(4 * k0, s), s1 = __builtin_amdgcn_ds_bpermute(4 * k1, s);
      *LDSP(int, base + TAB + 4 * (mid ? p0 + s0 : p0)) = pg[0] * 128 + h;
      *LDSP(int, base + TAB + 4 * (mid ? p1 + s1 : p1)) = pg[1] * 128 + h;
      asm volatile("s_waitcnt lgkmcnt(0)" ::: "memory");
      lds_put(base + WORDS + 44, 1);
    }
    if (!nq) return;
    tag = __hip_atomic_load((gu32*)(slot + EPOCH_OFF), __ATOMIC_RELAXED, __HIP_MEMORY_SCOPE_AGENT) + TAGK;
    if (gives) {
      while ((int)(NOW() - t0) < FETCH_AT) __builtin_amdgcn_s_sleep(32);
#pragma nounroll
      for (int spin = 0; spin < (1 << 22); ++spin) {
        if (__builtin_amdgcn_readfirstlane(__hip_atomic_load((gu32*)(slot + FLAG_OFF), __ATOMIC_RELAXED, __HIP_MEMORY_SCOPE_AGENT) == tag)) break;
        __builtin_amdgcn_s_sleep(8);
      }
      asm volatile("" ::: "memory");
      const __amdgpu_buffer_rsrc_t pr = rsrc(slot, 4096 + 64);
      u32x4 y[4];
#pragma unroll
      for (int r = 0; r < 4; ++r) y[r] = __builtin_amdgcn_raw_buffer_load_b128(pr, lane * 16 + r * 1024, 0, 16);
      const unsigned lse = __builtin_amdgcn_raw_buffer_load_b32(pr, 4096 + 4 * c, 0, 16);
#pragma unroll
      for (int k = 0; k < 8; ++k) {
        const unsigned lo = y[k >> 1][2 * (k & 1)], hi = y[k >> 1][2 * (k & 1) + 1];
        *LDSP(u32x4, base + DUMP + DSZ + 1024 * k + 16 * lane) = u32x4{lo << 16, lo & 0xffff0000u, hi << 16, hi & 0xffff0000u};
      }
      *LDSP(unsigned, base + DUMP + DSZ + 8192 + 4 * lane) = lse;
      asm volatile("s_waitcnt lgkmcnt(0)" ::: "memory");
      lds_put(base + WORDS + 128 + 36, 2);
      while (__builtin_amdgcn_readfirstlane(lds_ld(base + WORDS + 36)) < 2) __builtin_amdgcn_s_sleep(8);
      if (lane == 0) __hip_atomic_store((gu32*)(slot + EPOCH_OFF), tag, __ATOMIC_RELAXED, __HIP_MEMORY_SCOPE_AGENT);
      return;
    }
  }
  __builtin_amdgcn_s_setprio(3);
  if (w == 2) {
#pragma unroll
    for (int u = 0; u < 4; ++u) qf[u] = *(const u32x4*)(q + (pb * 8 + ph) * 4096 + c * 256 + g * 16 + 64 * u);
  }
  if ((unimask >> (w == 2 ? pb : b)) & 1) {
#pragma unroll
    for (int u = 0; u < 4; ++u) qf[u] = u32x4{0, 0, 0, 0};
  }
  const float CS = 0.088388347648318440f * 1.4426950408889634f;
  const unsigned ka = base + c * 256 + ((g ^ c) << 4);
  const unsigned va = base + 4096 + (lane >> 2) * 256 + ((((lane & 3) << 2) ^ (lane >> 2)) << 4);
  f32x4 acc[8];
#pragma unroll
  for (int k = 0; k < 8; ++k) acc[k] = f32x4{0.f, 0.f, 0.f, 0.f};
  float m = -1e30f, l = 0.f, alpha, pr[8];
  u32x4 pf = {0, 0, 0, 0};
  asm volatile("" : "+v"(qf[0]), "+v"(qf[1]), "+v"(qf[2]), "+v"(qf[3]), "+v"(tag));
  auto landed = [&](unsigned fw, int i) __attribute__((always_inline)) {
    for (;;) {
      const uint64_t f = __hip_atomic_load(LDSP(uint64_t, fw + 4 * (i & 7)), __ATOMIC_RELAXED, __HIP_MEMORY_SCOPE_WORKGROUP);
      if (__builtin_amdgcn_readfirstlane((unsigned)f + (unsigned)(f >> 32) + lds_ld(fw + 32)) == 3 * i + 3) break;
    }
    asm volatile("" ::: "memory");
  };
  auto vblock = [&](unsigned o, int k) __attribute__((always_inline)) {
    const unsigned a = ((o + va) ^ ((k >> 1) << 4)) + 8 * (k & 1);
    const u32x2 lo = __builtin_bit_cast(u32x2, __builtin_amdgcn_ds_read_tr16_b64_v4i16(LDSP(s16x4, a)));
    const u32x2 hi = __builtin_bit_cast(u32x2, __builtin_amdgcn_ds_read_tr16_b64_v4i16(LDSP(s16x4, a + 8192)));
    return u32x4{lo[0], lo[1], hi[0], hi[1]};
  };
#define PV(k) acc[k] = __builtin_amdgcn_mfma_f32_16x16x32_bf16(__builtin_bit_cast(bf16x8, vt[k]), __builtin_bit_cast(bf16x8, pf), acc[k], 0, 0, 0)
#define PIN() __builtin_amdgcn_sched_barrier(0)
  int i = w == 2 ? 4 : 0, iv = 2, steps = w == 2 ? xw : own;
  const int alt = w == 2 ? 64 : 0, cut = qb > 1 ? 2 : 0;
  unsigned fullv = fullw, ov = 4 * 8192;
  for (;;) {
    landed(fullw, i);
    const unsigned o = (i & 7) * 8192 + (fullw & 64) * 1024;
    if (w != 2) { ov = i ? o : o + 2 * 8192; iv = i; }
    u32x4 kf[2][4], vt[8];
#pragma unroll
    for (int u = 0; u < 4; ++u) {
      const unsigned a = (o + ka) ^ (u << 6);
      kf[0][u] = *LDSP(const u32x4, a);
      kf[1][u] = *LDSP(const u32x4, a + 8192);
    }
#pragma unroll
    for (int k = 0; k < 8; ++k) vt[k] = vblock(ov, k);
    PIN();
    f32x4 sc[2] = {{0.f, 0.f, 0.f, 0.f}, {0.f, 0.f, 0.f, 0.f}};
#pragma unroll
    for (int u = 0; u < 4; ++u)
#pragma unroll
      for (int t = 0; t < 2; ++t)
        sc[t] = __builtin_amdgcn_mfma_f32_16x16x32_bf16(__builtin_bit_cast(bf16x8, kf[t][u]), __builtin_bit_cast(bf16x8, qf[u]), sc[t], 0, 0, 0);
    PIN();
    PV(0);
    if (i == cut) {
#pragma unroll
      for (int r = 0; r < 8; ++r) sc[r >> 2][r & 3] = 16 * (r >> 2) + 4 * g + (r & 3) < rest ? sc[r >> 2][r & 3] : -__builtin_inff();
    }
    const float mx = xg_max(vmax(vmax(vmax(sc[0][0], sc[0][1]), vmax(sc[0][2], sc[0][3])), vmax(vmax(sc[1][0], sc[1][1]), vmax(sc[1][2], sc[1][3]))));
    const float mn = vmax(m, mx * CS);
    alpha = __builtin_amdgcn_exp2f(m - mn);
    m = mn;
#pragma unroll
    for (int r = 0; r < 8; ++r) pr[r] = __builtin_amdgcn_exp2f(__builtin_fmaf(sc[r >> 2][r & 3], CS, -mn));
#pragma unroll
    for (int k = 1; k < 8; ++k) PV(k);
#pragma unroll
    for (int k = 1; k < 8; ++k) {
      __builtin_amdgcn_sched_group_barrier(0x008, 1, 0);
      __builtin_amdgcn_sched_group_barrier(0x002, 2, 0);
    }
    asm volatile("" :: "v"(vt[7]) : "memory");
    lds_put(fullv + 32, iv + 2);
    fullv = fullw; ov = o; iv = i;
    fullw ^= alt;
    i += fullw & alt ? 0 : i == 2 ? 2 + xw : 2;
    l = l * alpha + (((pr[0] + pr[1]) + (pr[2] + pr[3])) + ((pr[4] + pr[5]) + (pr[6] + pr[7])));
    if (!--steps) break;
    pf = u32x4{cvtpk(pr[0], pr[1]), cvtpk(pr[2], pr[3]), cvtpk(pr[4], pr[5]), cvtpk(pr[6], pr[7])};
#pragma unroll
    for (int k = 0; k < 8; ++k) acc[k] *= alpha;
  }
  const bool more = w == 1 || (nq && gives);
  const unsigned src = base + DUMP + (w ? 0 : DSZ), ready = base + WORDS + 36 + (w ? 0 : 128);
  float lt = xg_sum(l), e = -__builtin_inff();
  if (more) {
    while (!__builtin_amdgcn_readfirstlane(lds_ld(ready))) {}
    asm volatile("" ::: "memory");
    e = *LDSP(const float, src + 8192 + 4 * lane);
  }
  const float mn = vmax(m, e), mine = __builtin_amdgcn_exp2f(m - mn), wt = __builtin_amdgcn_exp2f(e - mn);
  lt = lt * mine + wt;
  const float inv = __builtin_amdgcn_rcpf(lt), lse = mn + __builtin_amdgcn_logf(lt), f = mine * inv;
  if (!w) {
    *LDSP(float, base + DUMP + 8192 + 4 * lane) = lse;
    asm volatile("s_waitcnt lgkmcnt(0)" ::: "memory");
    lds_put(base + WORDS + 36, 1);
  }
  pf = u32x4{cvtpk(pr[0] * f, pr[1] * f), cvtpk(pr[2] * f, pr[3] * f), cvtpk(pr[4] * f, pr[5] * f), cvtpk(pr[6] * f, pr[7] * f)};
#pragma unroll
  for (int k = 0; k < 8; ++k) acc[k] *= alpha * f;
  asm volatile("" : "+v"(pf), "+v"(acc[0]), "+v"(acc[1]), "+v"(acc[2]), "+v"(acc[3]), "+v"(acc[4]), "+v"(acc[5]), "+v"(acc[6]), "+v"(acc[7]));
  if (more) {
    while (__builtin_amdgcn_readfirstlane(lds_ld(ready)) < 2) {}
    asm volatile("" ::: "memory");
#pragma unroll
    for (int k = 0; k < 8; ++k) acc[k] += *LDSP(const f32x4, src + 1024 * k + 16 * lane) * (wt * inv);
    asm volatile("" : "+v"(acc[0]), "+v"(acc[1]), "+v"(acc[2]), "+v"(acc[3]), "+v"(acc[4]), "+v"(acc[5]), "+v"(acc[6]), "+v"(acc[7]));
  }
  if (w != 2) { iv = i; ov = (i & 7) * 8192 + (fullw & 64) * 1024; }
  landed(fullv, iv);
  {
    u32x4 vt[8];
#pragma unroll
    for (int k = 0; k < 8; ++k) vt[k] = vblock(ov, k);
    PIN();
#pragma unroll
    for (int k = 0; k < 8; ++k) PV(k);
  }
  if (!w) {
#pragma unroll
    for (int k = 0; k < 8; ++k) *LDSP(f32x4, base + DUMP + 1024 * k + 16 * lane) = acc[k];
    asm volatile("s_waitcnt lgkmcnt(0)" ::: "memory");
    lds_put(base + WORDS + 36, 2);
  } else {
    u32x4 y[4];
#pragma unroll
    for (int r = 0; r < 4; ++r)
      y[r] = u32x4{cvtpk(acc[2 * r][0], acc[2 * r][1]), cvtpk(acc[2 * r][2], acc[2 * r][3]),
                   cvtpk(acc[2 * r + 1][0], acc[2 * r + 1][1]), cvtpk(acc[2 * r + 1][2], acc[2 * r + 1][3])};
    if (w == 1) {
      const __amdgpu_buffer_rsrc_t orr = rsrc(out + (b * 8 + h) * 4096, 4096);
#pragma unroll
      for (int r = 0; r < 4; ++r) __builtin_amdgcn_raw_buffer_store_b128(y[r], orr, c * 256 + g * 64 + r * 16, 0, OPOL);
    } else {
      lds_put(fullv + 32, iv + 2);
      const __amdgpu_buffer_rsrc_t orr = rsrc(slot, 4096 + 64);
#pragma unroll
      for (int r = 0; r < 4; ++r) __builtin_amdgcn_raw_buffer_store_b128(y[r], orr, lane * 16 + r * 1024, 0, 16);
      __builtin_amdgcn_raw_buffer_store_b32(__float_as_uint(lse), orr, lane * 4 + 4096, 0, 16);
      WAITVM0();
      if (lane == 0) __hip_atomic_store((gu32*)(slot + FLAG_OFF), tag, __ATOMIC_RELAXED, __HIP_MEMORY_SCOPE_AGENT);
      return;
    }
  }
}

extern "C" void kernel_launch(void* const* d_in, const int* in_sizes, int n_in, void* d_out, int out_size,
                              void* d_ws, size_t ws_size, hipStream_t stream) {
  pa_kernel<<<256, 704, 0, stream>>>((const char*)d_in[0], (const char*)d_in[1], (const char*)d_in[2],
                                     (const int*)d_in[3], (const int*)d_in[4], (char*)d_out, (char*)d_ws);
}
```

```cpp
#define TAU16 458
#include <hip/hip_runtime.h>
#include <hip/hip_bf16.h>
#include <stdint.h>

#define CAPS 68
#define D0 10
#define FB0 6
#define QRND 1
#define LMAX 84
#define EDEL 90
#define SCOST 8
#define FETCH_AT (tend / 2 - 250 - 16 * rg)
#define TSUB 38
#define KAP 300
#define LREF 88
#define LNEG 40
#define LCAP 100
#define BETA 6
#define OPOL 2
#define CPFS "0x1d80"
#define KPOL " nt"
#define VPOL " nt"

typedef __attribute__((ext_vector_type(8))) __bf16 bf16x8;
typedef __attribute__((ext_vector_type(4))) short s16x4;
typedef __attribute__((ext_vector_type(4))) float f32x4;
typedef __attribute__((ext_vector_type(4))) unsigned u32x4;
typedef __attribute__((ext_vector_type(2))) unsigned u32x2;
typedef __attribute__((address_space(1))) unsigned gu32;
#define LDSP(T, a) ((__attribute__((address_space(3))) T*)(uintptr_t)(a))

constexpr int NBPS = 128, SKV = NBPS * 16;
constexpr unsigned PB = 8 * 8192, MB = PB + 64 * 1024, FD = MB + 64 * 64, XL = FD + 4096 + 256;
constexpr unsigned WORDS = XL + 4 * 256, CNT = WORDS + 128, TAB = CNT + 256;
constexpr unsigned SLOT = 8192, FLAG_OFF = 4096 + 64, EPOCH_OFF = 4096 + 68;
constexpr unsigned TAGK = 0x9E3779B9u;
typedef __attribute__((ext_vector_type(8))) _Float16 f16x8;

__device__ __forceinline__ unsigned cvtpk(float lo, float hi) {
  typedef __attribute__((ext_vector_type(2))) __bf16 bf16x2;
  bf16x2 r = {(__bf16)lo, (__bf16)hi};
  return __builtin_bit_cast(unsigned, r);
}
__device__ __forceinline__ float vmax(float a, float b) { return __builtin_elementwise_maximum(a, b); }
__device__ __forceinline__ float xg_max(float x) {
  auto s = __builtin_amdgcn_permlane16_swap(__float_as_uint(x), __float_as_uint(x), false, false);
  x = vmax(__uint_as_float(s[0]), __uint_as_float(s[1]));
  auto t = __builtin_amdgcn_permlane32_swap(__float_as_uint(x), __float_as_uint(x), false, false);
  return vmax(__uint_as_float(t[0]), __uint_as_float(t[1]));
}
__device__ __forceinline__ float xg_sum(float x) {
  auto s = __builtin_amdgcn_permlane16_swap(__float_as_uint(x), __float_as_uint(x), false, false);
  x = __uint_as_float(s[0]) + __uint_as_float(s[1]);
  auto t = __builtin_amdgcn_permlane32_swap(__float_as_uint(x), __float_as_uint(x), false, false);
  return __uint_as_float(t[0]) + __uint_as_float(t[1]);
}
__device__ __forceinline__ int scan32(int v) {
  v += __builtin_amdgcn_update_dpp(0, v, 0x111, 0xf, 0xf, false);
  v += __builtin_amdgcn_update_dpp(0, v, 0x112, 0xf, 0xf, false);
  v += __builtin_amdgcn_update_dpp(0, v, 0x114, 0xf, 0xf, false);
  v += __builtin_amdgcn_update_dpp(0, v, 0x118, 0xf, 0xf, false);
  v += __builtin_amdgcn_update_dpp(0, v, 0x142, 0xa, 0xf, false);
  return v;
}
__device__ __forceinline__ __amdgpu_buffer_rsrc_t rsrc(const void* p, unsigned n) {
  return __builtin_amdgcn_make_buffer_rsrc((void*)p, 0, n, 0x00020000);
}
__device__ __forceinline__ u32x4 srd(const void* p, unsigned n) {
  const uint64_t a = (uint64_t)p;
  return u32x4{(unsigned)a, (unsigned)(a >> 32) & 0xffffu, n, 0x00020000u};
}
__device__ __forceinline__ int rl(int v, int l) { return __builtin_amdgcn_readlane(v, l); }
__device__ __forceinline__ unsigned bal32(bool c) { return (unsigned)__builtin_amdgcn_ballot_w64(c); }
__device__ __forceinline__ int lds_ld(unsigned a) {
  return __hip_atomic_load(LDSP(int, a), __ATOMIC_RELAXED, __HIP_MEMORY_SCOPE_WORKGROUP);
}
__device__ __forceinline__ void lds_put(unsigned a, int v) {
  __hip_atomic_store(LDSP(int, a), v, __ATOMIC_RELAXED, __HIP_MEMORY_SCOPE_WORKGROUP);
}

#define DMA4(POL, dst, rs, v)                                                                         \
  do {                                                                                                \
    unsigned keep_;                                                                                   \
    asm volatile("s_mov_b32 %0, m0\n\ts_mov_b32 m0, %1\n\ts_nop 4\n\t"                                \
                 "buffer_load_dwordx4 %3, %2, 0 offen" POL " lds\n\t"                                 \
                 "buffer_load_dwordx4 %4, %2, 0 offen offset:1024" POL " lds\n\t"                     \
                 "buffer_load_dwordx4 %5, %2, 0 offen offset:2048" POL " lds\n\t"                     \
                 "buffer_load_dwordx4 %6, %2, 0 offen offset:3072" POL " lds\n\t"                     \
                 "s_mov_b32 m0, %0"                                                                   \
                 : "=&s"(keep_) : "s"(dst), "s"(rs), "v"(v[0]), "v"(v[1]), "v"(v[2]), "v"(v[3]) : "memory"); \
  } while (0)
#define WAITVM0() asm volatile("s_waitcnt vmcnt(0)" ::: "memory")
#define NOW() ((unsigned)__builtin_amdgcn_s_memrealtime())

__global__ void __launch_bounds__(768)
pa_kernel(const char* __restrict__ q, const char* __restrict__ kc, const char* __restrict__ vc,
          const int* __restrict__ bt, const int* __restrict__ lens, char* __restrict__ out, char* __restrict__ ws) {
  __shared__ __attribute__((aligned(16384))) char lds[TAB + 512];
  int cpf;
  {
    uint64_t pc;
    asm volatile("s_getpc_b64 %1\n\tv_lshlrev_b32 %0, 4, %2\n\tv_min_u32 %0, " CPFS ", %0\n\tglobal_load_dword %0, %0, %1" : "=&v"(cpf), "=&s"(pc) : "v"(threadIdx.x));
  }
  const uint64_t t64 = __builtin_amdgcn_s_memrealtime();
  const int lane = threadIdx.x & 63;
  const int wave = __builtin_amdgcn_readfirstlane(threadIdx.x >> 6);
  const int b = blockIdx.x >> 3, h = (blockIdx.x - b) & 7;
  const int g = lane >> 4, c = lane & 15;
  const unsigned base = (unsigned)(uintptr_t)LDSP(char, lds);
  if (threadIdx.x - 704 < 48) *LDSP(u32x2, base + WORDS + 8 * lane) = u32x2{0, 0};

  const int ly = lane < 32 ? lane : (lane ^ 16) & 31;
  unsigned go[4];
#pragma unroll
  for (int u = 0; u < 4; ++u) go[u] = (4 * u + g) * 2048 + ((c ^ (4 * u + g)) << 4) - 1024 * u;
  const unsigned ring = base + wave * 8192;
  u32x2 first;
  int len, leny;
  u32x2 pg;
  asm volatile("s_waitcnt lgkmcnt(0)\n\ts_load_dwordx2 %3, %8, %9\n\tglobal_load_dword %0, %4, %7\n\tglobal_load_dword %1, %5, %7\n\tglobal_load_dwordx2 %2, %6, %8"
               : "=&v"(len), "=&v"(leny), "=&v"(pg), "=&s"(first)
               : "v"(4 * (lane & 31)), "v"(4 * ly), "v"(4 * b * NBPS + 8 * lane), "s"(lens), "s"(bt), "s"(4 * b * NBPS + 8 * wave),
                 "s"(q), "s"(kc), "s"(vc), "s"(out), "s"(ws), "s"(t64), "v"(go[0]), "v"(go[1]), "v"(go[2]), "v"(go[3]), "s"(ring), "s"(h) : "memory");
  const unsigned t0 = t64;
  u32x4 qf[4];
  if ((wave & 14) == 8)
    asm volatile("global_load_dwordx4 %0, %4, %5\n\tglobal_load_dwordx4 %1, %4, %5 offset:64\n\tglobal_load_dwordx4 %2, %4, %5 offset:128\n\tglobal_load_dwordx4 %3, %4, %5 offset:192"
                 : "=&v"(qf[0]), "=&v"(qf[1]), "=&v"(qf[2]), "=&v"(qf[3]) : "v"((b * 8 + h) * 4096 + c * 256 + g * 16), "s"(q) : "memory");
  asm volatile("s_barrier\n\ts_waitcnt lgkmcnt(0)" : "+s"(first) :: "memory");
  if (wave < 4) {
    const u32x4 r0 = srd(kc + (size_t)(first[0] * 128 + h) * 256, 32768), r1 = srd(kc + (size_t)(first[1] * 128 + h) * 256, 32768);
    if (wave > 1) while ((int)(NOW() - t0) < EDEL) {}
    DMA4(KPOL, ring, r0, go);
    DMA4(KPOL, ring + 4096, r1, go);
  }
  asm volatile("s_waitcnt vmcnt(8)\n\ts_cmp_lt_u32 %8, 4\n\ts_cbranch_scc1 1f\n\ts_waitcnt vmcnt(0)\n1:"
               : "+v"(len), "+v"(leny), "+v"(pg), "+v"(cpf), "+v"(qf[0]), "+v"(qf[1]), "+v"(qf[2]), "+v"(qf[3]) : "s"(wave) : "scc", "memory");
  const unsigned unimask = bal32(len <= 0);
  if (len <= 0 || len > SKV) len = SKV;
  const int lenb = rl(len, b), qb = (lenb + 63) >> 6, pre = qb > 2 ? 6 : 4;
#pragma unroll
  for (int u = 0; u < 2; ++u) pg[u] = (pg[u] * 128 + h) | min(max(lenb - 32 * lane - 16 * u, 0), 16) << 27;
  if (wave >= 4 && wave < pre) {
    const unsigned p0 = rl(pg[0], 2 * qb - 6 + wave), p1 = rl(pg[1], 2 * qb - 6 + wave);
    const u32x4 r0 = srd(kc + (size_t)(p0 & 0x7ffffff) * 256, p0 >> 27 << 11), r1 = srd(kc + (size_t)(p1 & 0x7ffffff) * 256, p1 >> 27 << 11);
    DMA4(KPOL, ring, r0, go);
    DMA4(KPOL, ring + 4096, r1, go);
  }

  if (leny <= 0 || leny > SKV) leny = SKV;
  const int key = ((len + 15) >> 4) * 32 + (lane & 31), keyy = ((leny + 15) >> 4) * 32 + ly;
  unsigned less = (unsigned)(keyy - key) >> 31;
#define ROR(n) less = __builtin_amdgcn_alignbit(less, __builtin_amdgcn_update_dpp(0, keyy, 0x120 + n, 0xf, 0xf, true) - key, 31);
  ROR(1) ROR(2) ROR(3) ROR(4) ROR(5) ROR(6) ROR(7) ROR(8) ROR(9) ROR(10) ROR(11) ROR(12) ROR(13) ROR(14) ROR(15)
  int rank = __builtin_popcount(less);
  {
    auto t = __builtin_amdgcn_permlane32_swap(rank, rank, false, false);
    rank = t[0] + t[1];
  }
  const int tot = rl(scan32(key >> 5), 31), rb = rl(rank, b);
  const bool gives = (h & 3) == 1;
  const int rg = gives ? 31 - rb : 2 * rb + (h >> 1 & 1);
  const int ph = gives ? h - 1 + 2 * (rg & 1) : (h & 4) | 1;
  const int pb = __builtin_ctz(bal32(rank == (gives ? rg >> 1 : 31 - rg)) | 0x80000000u);
  const int npb = (lenb + 15) >> 4, npp = (rl(len, pb) + 15) >> 4;
  const int npg = gives ? npb : npp, nph = gives ? npp : npb;
  int nq = min(min((npg - ((tot * CAPS) >> 11) + QRND) >> 2, ((npg + 3) >> 2) - 3), min(((((tot * LMAX) >> 11) - nph) * 43) >> 8, min((128 - nph) >> 2, 16)));
  if (nq < 0 || rg > 31 || nph < 11 || (h & 3) == 3) nq = 0;
  nq = __builtin_amdgcn_readfirstlane(nq);
  const int xw = gives ? 0 : 2 * nq, nb = 2 * __builtin_amdgcn_readfirstlane(max(gives ? qb - nq : qb, 2));
  const int tend = ((tot - TSUB) * TAU16) >> 9;

  if (wave < 8) {
    const int ev = wave + 8 * (lane >> 1), f = ev - 6, eo = f < 0 ? ev : max(ev - 2 * xw, 6);
    const bool foreign = (unsigned)f < (unsigned)(2 * xw), isv = foreign ? f >= xw : eo >= nb;
    const int i = isv && !foreign ? eo - nb : eo, rest = qb > 2 ? lenb + 64 - 64 * qb : 64, nt = 32 * nb - 64 + rest;
    const int cost = max(32 * i - 64 + rest, min(32 * i, 128 + rest)) + (isv && !foreign ? nt : 0) + (32 + 2 * SCOST) * min(max(f, 0), 2 * xw);
    const float rc = __builtin_amdgcn_rcpf((float)(2 * nt + (64 + 4 * SCOST) * xw)) * (float)max(tend - D0, 0);
    const unsigned tab = ((unsigned)((float)cost * rc) + D0) << 1 | isv;
    const unsigned ta = base + TAB + ((8 * (foreign ? nb + f - (isv ? xw : 0) : i) + 4 * (lane & 1)) & 511);
    const int kap = __builtin_amdgcn_readfirstlane((int)(rc * (32 * KAP)));
    int freed = 0;
    asm volatile("" :: "v"(tab), "v"(ta), "s"(kap));
    while (!__builtin_amdgcn_readfirstlane(lds_ld(base + WORDS + 52))) {}
    asm volatile("" ::: "memory");
    const unsigned pt = *LDSP(const unsigned, ta);
    for (int e = wave; e < 2 * (nb + xw); e += 8) {
      unsigned ti;
      if (e >= pre) {
        const unsigned d = rl(tab, e >> 2), p0 = rl(pt, e >> 2 & 62), p1 = rl(pt, e >> 2 | 1);
        const int due = d >> 1;
        const char* const from = d & 1 ? vc : kc;
        const u32x4 r0 = srd(from + (size_t)(p0 & 0x7ffffff) * 256, p0 >> 27 << 11), r1 = srd(from + (size_t)(p1 & 0x7ffffff) * 256, p1 >> 27 << 11);
        asm volatile("" :: "s"(r0), "s"(r1));
        while (freed + 7 < e) {
          const uint64_t x = __hip_atomic_load(LDSP(uint64_t, base + WORDS + 32), __ATOMIC_RELAXED, __HIP_MEMORY_SCOPE_WORKGROUP);
          const uint64_t y = __hip_atomic_load(LDSP(uint64_t, base + WORDS + 40), __ATOMIC_RELAXED, __HIP_MEMORY_SCOPE_WORKGROUP);
          freed = __builtin_amdgcn_readfirstlane(min(min((int)x, (int)(x >> 32)), min((int)y, (int)(y >> 32))));
          if (freed + 7 < e) __builtin_amdgcn_s_sleep(1);
        }
        int late;
        do {
          ti = NOW();
          late = ti - t0 - due;
        } while (late < __builtin_amdgcn_readfirstlane(lds_ld(base + WORDS + 48)) >> 16);
        ti += late * BETA >> 4;
        DMA4(KPOL, ring, r0, go);
        DMA4(KPOL, ring + 4096, r1, go);
      }
      WAITVM0();
      lds_put(base + WORDS + 4 * wave, e + 1);
      if (e >= FB0) {
        const int ex = min(max((int)(NOW() - ti) - LREF, -LNEG), LCAP) * kap;
        if (lane == 0) {
          __hip_atomic_fetch_add(LDSP(int, base + WORDS + 48), ex, __ATOMIC_RELAXED, __HIP_MEMORY_SCOPE_WORKGROUP);
          __hip_atomic_fetch_max(LDSP(int, base + WORDS + 48), 0, __ATOMIC_RELAXED, __HIP_MEMORY_SCOPE_WORKGROUP);
        }
      }
    }
    return;
  }

  const int x = wave & 1;
  const bool other = wave >= 10;
  const unsigned used = base + WORDS + 4 * wave;
  char* const slot = ws + (unsigned)(gives ? b * 8 + h : pb * 8 + ph) * SLOT;
  unsigned tag = 0;
  lds_put(used, !other ? x : xw ? 6 + x : 1 << 20);
  if (other) {
    if (x) {
      int fp;
      if (lane < 2 * xw) fp = bt[pb * NBPS + 4 * (((npp + 3) >> 2) - 1 - nq) + lane];
      const bool mid = lane >= 4 && lane < nb - 2;
      const unsigned k0 = min(pg[0] >> 13 & 0x3fff, 63u), k1 = min(pg[1] >> 13 & 0x3fff, 63u);
      int p0 = lane < 4 ? 2 * lane : 2 * lane + 12 - 4 * qb, p1 = p0 + 1;
      if (mid) {
        p0 = __hip_atomic_fetch_add(LDSP(int, base + CNT + 4 * k0), 1, __ATOMIC_RELAXED, __HIP_MEMORY_SCOPE_WORKGROUP);
        p1 = __hip_atomic_fetch_add(LDSP(int, base + CNT + 4 * k1), 1, __ATOMIC_RELAXED, __HIP_MEMORY_SCOPE_WORKGROUP);
      }
      const int n = lds_ld(base + CNT + 4 * lane);
      int s = scan32(n);
      s += __builtin_amdgcn_update_dpp(0, s, 0x143, 0xc, 0xf, false) + 12 - n;
      const int s0 = __builtin_amdgcn_ds_bpermute(4 * k0, s), s1 = __builtin_amdgcn_ds_bpermute(4 * k1, s);
      if (lane < 4 || mid || lane >> 1 == qb - 1) {
        *LDSP(unsigned, base + TAB + 4 * (mid ? p0 + s0 : p0)) = pg[0];
        *LDSP(unsigned, base + TAB + 4 * (mid ? p1 + s1 : p1)) = pg[1];
      }
      if (lane < 2 * xw) *LDSP(unsigned, base + TAB + 8 * nb + 4 * lane) = (fp * 128 + ph) | 16u << 27;
      asm volatile("s_waitcnt lgkmcnt(0)" ::: "memory");
      lds_put(base + WORDS + 52, 1);
    }
    if (!nq || (gives && x)) return;
    tag = __hip_atomic_load((gu32*)(slot + EPOCH_OFF), __ATOMIC_RELAXED, __HIP_MEMORY_SCOPE_AGENT) + TAGK;
    if (gives) {
      while ((int)(NOW() - t0) < FETCH_AT) __builtin_amdgcn_s_sleep(32);
#pragma nounroll
      for (int spin = 0; spin < (1 << 22); ++spin) {
        if (__builtin_amdgcn_readfirstlane(__hip_atomic_load((gu32*)(slot + FLAG_OFF), __ATOMIC_RELAXED, __HIP_MEMORY_SCOPE_AGENT) == tag)) break;
        __builtin_amdgcn_s_sleep(8);
      }
      asm volatile("" ::: "memory");
      const __amdgpu_buffer_rsrc_t pr = rsrc(slot, 4096 + 64);
      u32x4 y[4];
#pragma unroll
      for (int r = 0; r < 4; ++r) y[r] = __builtin_amdgcn_raw_buffer_load_b128(pr, lane * 16 + r * 1024, 0, 16);
      const unsigned lse = __builtin_amdgcn_raw_buffer_load_b32(pr, 4096 + 4 * c, 0, 16);
#pragma unroll
      for (int r = 0; r < 4; ++r) *LDSP(u32x4, base + FD + 1024 * r + 16 * lane) = y[r];
      *LDSP(unsigned, base + FD + 4096 + 4 * lane) = lse;
      asm volatile("s_waitcnt lgkmcnt(0)" ::: "memory");
      lds_put(base + WORDS + 56, 1);
      if (lane == 0) __hip_atomic_store((gu32*)(slot + EPOCH_OFF), tag, __ATOMIC_RELAXED, __HIP_MEMORY_SCOPE_AGENT);
      return;
    }
  }
  __builtin_amdgcn_s_setprio(3);
  if (other) {
#pragma unroll
    for (int u = 0; u < 4; ++u) qf[u] = *(const u32x4*)(q + (pb * 8 + ph) * 4096 + c * 256 + g * 16 + 64 * u);
  }
  if ((unimask >> (other ? pb : b)) & 1) {
#pragma unroll
    for (int u = 0; u < 4; ++u) qf[u] = u32x4{0, 0, 0, 0};
  }
  const float CS = 0.088388347648318440f * 1.4426950408889634f;
  const unsigned ka = base + c * 256 + ((g ^ c) << 4);
  const unsigned va = base + (lane >> 2) * 256 + ((((lane & 3) << 2) ^ (lane >> 2) ^ (2 * x)) << 4);
  float m = -1e30f, l = 0.f;
  asm volatile("" : "+v"(qf[0]), "+v"(qf[1]), "+v"(qf[2]), "+v"(qf[3]), "+v"(tag));
  auto landed = [&](int e) __attribute__((always_inline)) {
    while (__builtin_amdgcn_readfirstlane(lds_ld(base + WORDS + 4 * (e & 7))) != e + 1) {}
    asm volatile("" ::: "memory");
  };
#define PIN() __builtin_amdgcn_sched_barrier(0)
  const int i0 = other ? nb : 0, i1 = other ? nb + xw : nb, ev = other ? 6 + xw : nb + 2 * xw;
  int e = (other ? 6 : 0) + x;
  for (int i = i0 + x; i < i1; i += 2) {
    landed(e);
    u32x4 kf[2][4];
#pragma unroll
    for (int u = 0; u < 4; ++u) {
      const unsigned a = ((e & 7) * 8192 + ka) ^ (u << 6);
      kf[0][u] = *LDSP(const u32x4, a);
      kf[1][u] = *LDSP(const u32x4, a + 4096);
    }
    e = min(e + (i >> 1 == 2 ? 2 + 2 * xw : 2), ev);
    asm volatile("" ::: "memory");
    lds_put(used, e);
    PIN();
    f32x4 sc[2] = {{0.f, 0.f, 0.f, 0.f}, {0.f, 0.f, 0.f, 0.f}};
#pragma unroll
    for (int u = 0; u < 4; ++u)
#pragma unroll
      for (int t = 0; t < 2; ++t)
        sc[t] = __builtin_amdgcn_mfma_f32_16x16x32_bf16(__builtin_bit_cast(bf16x8, kf[t][u]), __builtin_bit_cast(bf16x8, qf[u]), sc[t], 0, 0, 0);
    if (i < 6) {
      const int rest = (i < 4 ? lenb : lenb + 192 - 64 * qb) - 32 * i;
#pragma unroll
      for (int r = 0; r < 8; ++r) sc[r >> 2][r & 3] = 16 * (r >> 2) + 4 * g + (r & 3) < rest ? sc[r >> 2][r & 3] : -__builtin_inff();
    }
    const float mx = xg_max(vmax(vmax(vmax(sc[0][0], sc[0][1]), vmax(sc[0][2], sc[0][3])), vmax(vmax(sc[1][0], sc[1][1]), vmax(sc[1][2], sc[1][3]))));
    const float mn = vmax(m, mx);
    float pr[8];
#pragma unroll
    for (int r = 0; r < 8; ++r) pr[r] = __builtin_amdgcn_exp2f(__builtin_fmaf(sc[r >> 2][r & 3] - mn, CS, 15.f));
    l = l * __builtin_amdgcn_exp2f((m - mn) * CS) + (((pr[0] + pr[1]) + (pr[2] + pr[3])) + ((pr[4] + pr[5]) + (pr[6] + pr[7])));
    m = mn;
    f16x8 pw;
#pragma unroll
    for (int r = 0; r < 8; ++r) pw[r] = (_Float16)pr[r];
    *LDSP(f16x8, base + PB + 1024 * i + 16 * lane) = pw;
    *LDSP(float, base + MB + 64 * i + 4 * c) = __builtin_fmaf(mn, CS, -15.f);
  }
  float lse = __builtin_fmaf(m, CS, -15.f) + __builtin_amdgcn_logf(xg_sum(l)), lf = -__builtin_inff();
  *LDSP(float, base + XL - 2048 + 256 * wave + 4 * lane) = lse;
  asm volatile("s_waitcnt lgkmcnt(0)" ::: "memory");
  lds_put(used + 32, 1);
  u32x4 y[2] = {{0, 0, 0, 0}, {0, 0, 0, 0}};
  if (nq && gives) {
    while (!__builtin_amdgcn_readfirstlane(lds_ld(base + WORDS + 56))) {}
    asm volatile("" ::: "memory");
    y[0] = *LDSP(const u32x4, base + FD + 2048 * x + 16 * lane);
    y[1] = *LDSP(const u32x4, base + FD + 2048 * x + 1024 + 16 * lane);
    lf = *LDSP(const float, base + FD + 4096 + 4 * lane);
  }
  while (!__builtin_amdgcn_readfirstlane(lds_ld((used ^ 4) + 32))) {}
  asm volatile("" ::: "memory");
  unsigned pa = base + PB + 1024 * i0 + 16 * lane, ma = base + MB + 64 * i0 + 4 * c;
  f16x8 pw = *LDSP(const f16x8, pa);
  float mi = *LDSP(const float, ma);
  f32x4 acc[4];
  {
    const float lo = *LDSP(const float, base + XL - 2048 + 256 * (wave ^ 1) + 4 * lane), mn = vmax(vmax(lse, lo), lf);
    lse = mn + __builtin_amdgcn_logf((__builtin_amdgcn_exp2f(lse - mn) + __builtin_amdgcn_exp2f(lo - mn)) + __builtin_amdgcn_exp2f(lf - mn));
    const float wt = __builtin_amdgcn_exp2f(lf - lse);
#pragma unroll
    for (int k = 0; k < 4; ++k) {
      const unsigned lo = y[k >> 1][2 * (k & 1)], hi = y[k >> 1][2 * (k & 1) + 1];
      acc[k] = __builtin_bit_cast(f32x4, u32x4{lo << 16, lo & 0xffff0000u, hi << 16, hi & 0xffff0000u}) * wt;
    }
  }
  for (const int e1 = e + i1 - i0;;) {
    const float s = __builtin_amdgcn_exp2f(mi - lse);
    u32x4 pf;
#pragma unroll
    for (int r = 0; r < 4; ++r) pf[r] = cvtpk((float)pw[2 * r] * s, (float)pw[2 * r + 1] * s);
    asm volatile("" : "+v"(pf));
    landed(e);
    u32x4 vt[4];
#pragma unroll
    for (int k = 0; k < 4; ++k) {
      const unsigned a = (((e & 7) * 8192 + va) ^ ((k >> 1) << 4)) + 8 * (k & 1);
      const u32x2 lo = __builtin_bit_cast(u32x2, __builtin_amdgcn_ds_read_tr16_b64_v4i16(LDSP(s16x4, a)));
      const u32x2 hi = __builtin_bit_cast(u32x2, __builtin_amdgcn_ds_read_tr16_b64_v4i16(LDSP(s16x4, a + 4096)));
      vt[k] = u32x4{lo[0], lo[1], hi[0], hi[1]};
    }
    ++e;
    asm volatile("" ::: "memory");
    lds_put(used, e == e1 ? 1 << 20 : e);
    pa += 1024; ma += 64;
    pw = *LDSP(const f16x8, pa);
    mi = *LDSP(const float, ma);
    PIN();
#pragma unroll
    for (int k = 0; k < 4; ++k) acc[k] = __builtin_amdgcn_mfma_f32_16x16x32_bf16(__builtin_bit_cast(bf16x8, vt[k]), __builtin_bit_cast(bf16x8, pf), acc[k], 0, 0, 0);
    if (e == e1) break;
  }
#pragma unroll
  for (int r = 0; r < 2; ++r)
    y[r] = u32x4{cvtpk(acc[2 * r][0], acc[2 * r][1]), cvtpk(acc[2 * r][2], acc[2 * r][3]),
                 cvtpk(acc[2 * r + 1][0], acc[2 * r + 1][1]), cvtpk(acc[2 * r + 1][2], acc[2 * r + 1][3])};
  if (!other) {
    const __amdgpu_buffer_rsrc_t orr = rsrc(out + (b * 8 + h) * 4096, 4096);
#pragma unroll
    for (int r = 0; r < 2; ++r) __builtin_amdgcn_raw_buffer_store_b128(y[r], orr, c * 256 + g * 64 + x * 32 + r * 16, 0, OPOL);
  } else {
    const __amdgpu_buffer_rsrc_t orr = rsrc(slot, 4096 + 64);
#pragma unroll
    for (int r = 0; r < 2; ++r) __builtin_amdgcn_raw_buffer_store_b128(y[r], orr, lane * 16 + x * 2048 + r * 1024, 0, 16);
    if (!x) __builtin_amdgcn_raw_buffer_store_b32(__float_as_uint(lse), orr, lane * 4 + 4096, 0, 16);
    WAITVM0();
    if (lane == 0 && __hip_atomic_fetch_add(LDSP(int, base + WORDS + 60), 1, __ATOMIC_RELAXED, __HIP_MEMORY_SCOPE_WORKGROUP))
      __hip_atomic_store((gu32*)(slot + FLAG_OFF), tag, __ATOMIC_RELAXED, __HIP_MEMORY_SCOPE_AGENT);
    return;
  }
}

extern "C" void kernel_launch(void* const* d_in, const int* in_sizes, int n_in, void* d_out, int out_size,
                              void* d_ws, size_t ws_size, hipStream_t stream) {
  pa_kernel<<<256, 768, 0, stream>>>((const char*)d_in[0], (const char*)d_in[1], (const char*)d_in[2],
                                     (const int*)d_in[3], (const int*)d_in[4], (char*)d_out, (char*)d_ws);
}
```

```cpp
#include <hip/hip_runtime.h>
#include <hip/hip_bf16.h>
#include <stdint.h>

#define CAPS 65
#define D0 10
#define QRND 1
#define LMAX 84
#define EDEL 90
#define SCOST 8
#define FETCH_AT (tend - 500 - 16 * rg)
#define TAU16 452
#define TSUB 38
#define KAP 300
#define LREF 88
#define LNEG 40
#define LCAP 100
#define BETA 6
#define BETA2 BETA
#define LREF2 110
#define MG 5
#define OPOL 2
#define CPFS "0x1f80"
#define KPOL " nt"
#define VPOL " nt"

typedef __attribute__((ext_vector_type(8))) __bf16 bf16x8;
typedef __attribute__((ext_vector_type(4))) short s16x4;
typedef __attribute__((ext_vector_type(4))) float f32x4;
typedef __attribute__((ext_vector_type(4))) unsigned u32x4;
typedef __attribute__((ext_vector_type(2))) unsigned u32x2;
typedef __attribute__((address_space(1))) unsigned gu32;
#define LDSP(T, a) ((__attribute__((address_space(3))) T*)(uintptr_t)(a))

constexpr int NBPS = 128, SKV = NBPS * 16;
constexpr unsigned PB = 8 * 8192, MB = PB + 64 * 1024, FD = MB + 64 * 64, XL = FD + 4096 + 256;
constexpr unsigned WORDS = XL + 4 * 512, CNT = WORDS + 128, TAB = CNT + 256;
constexpr unsigned EARLY = PB + 16 * 1024;
constexpr unsigned SLOT = 8192, FLAG_OFF = 4096 + 64, EPOCH_OFF = 4096 + 68;
constexpr unsigned TAGK = 0x9E3779B9u;
typedef __attribute__((ext_vector_type(2))) unsigned short u16x2;
typedef __attribute__((ext_vector_type(2))) float f32x2;
typedef __attribute__((ext_vector_type(2))) __bf16 bf16x2;

__device__ __forceinline__ unsigned cvtpk(float lo, float hi) {
  bf16x2 r = {(__bf16)lo, (__bf16)hi};
  return __builtin_bit_cast(unsigned, r);
}
__device__ __forceinline__ float vmax(float a, float b) { return __builtin_elementwise_maximum(a, b); }
__device__ __forceinline__ float xg_max(float x) {
  auto s = __builtin_amdgcn_permlane16_swap(__float_as_uint(x), __float_as_uint(x), false, false);
  x = vmax(__uint_as_float(s[0]), __uint_as_float(s[1]));
  auto t = __builtin_amdgcn_permlane32_swap(__float_as_uint(x), __float_as_uint(x), false, false);
  return vmax(__uint_as_float(t[0]), __uint_as_float(t[1]));
}
__device__ __forceinline__ float xg_sum(float x) {
  auto s = __builtin_amdgcn_permlane16_swap(__float_as_uint(x), __float_as_uint(x), false, false);
  x = __uint_as_float(s[0]) + __uint_as_float(s[1]);
  auto t = __builtin_amdgcn_permlane32_swap(__float_as_uint(x), __float_as_uint(x), false, false);
  return __uint_as_float(t[0]) + __uint_as_float(t[1]);
}
__device__ __forceinline__ int scan32(int v) {
  v += __builtin_amdgcn_update_dpp(0, v, 0x111, 0xf, 0xf, false);
  v += __builtin_amdgcn_update_dpp(0, v, 0x112, 0xf, 0xf, false);
  v += __builtin_amdgcn_update_dpp(0, v, 0x114, 0xf, 0xf, false);
  v += __builtin_amdgcn_update_dpp(0, v, 0x118, 0xf, 0xf, false);
  v += __builtin_amdgcn_update_dpp(0, v, 0x142, 0xa, 0xf, false);
  return v;
}
__device__ __forceinline__ __amdgpu_buffer_rsrc_t rsrc(const void* p, unsigned n) {
  return __builtin_amdgcn_make_buffer_rsrc((void*)p, 0, n, 0x00020000);
}
__device__ __forceinline__ u32x4 srd(const void* p, unsigned n) {
  const uint64_t a = (uint64_t)p;
  return u32x4{(unsigned)a, (unsigned)(a >> 32) & 0xffffu, n, 0x00020000u};
}
__device__ __forceinline__ int rl(int v, int l) { return __builtin_amdgcn_readlane(v, l); }
__device__ __forceinline__ unsigned bal32(bool c) { return (unsigned)__builtin_amdgcn_ballot_w64(c); }
__device__ __forceinline__ int lds_ld(unsigned a) {
  return __hip_atomic_load(LDSP(int, a), __ATOMIC_RELAXED, __HIP_MEMORY_SCOPE_WORKGROUP);
}
__device__ __forceinline__ void lds_put(unsigned a, int v) {
  __hip_atomic_store(LDSP(int, a), v, __ATOMIC_RELAXED, __HIP_MEMORY_SCOPE_WORKGROUP);
}

#define DMA4(POL, dst, rs, v)                                                                         \
  do {                                                                                                \
    unsigned keep_;                                                                                   \
    asm volatile("s_mov_b32 %0, m0\n\ts_mov_b32 m0, %1\n\ts_nop 4\n\t"                                \
                 "buffer_load_dwordx4 %3, %2, 0 offen" POL " lds\n\t"                                 \
                 "buffer_load_dwordx4 %4, %2, 0 offen offset:1024" POL " lds\n\t"                     \
                 "buffer_load_dwordx4 %5, %2, 0 offen offset:2048" POL " lds\n\t"                     \
                 "buffer_load_dwordx4 %6, %2, 0 offen offset:3072" POL " lds\n\t"                     \
                 "s_mov_b32 m0, %0"                                                                   \
                 : "=&s"(keep_) : "s"(dst), "s"(rs), "v"(v[0]), "v"(v[1]), "v"(v[2]), "v"(v[3]) : "memory"); \
  } while (0)
#define WAITVM0() asm volatile("s_waitcnt vmcnt(0)" ::: "memory")
#define NOW() ((unsigned)__builtin_amdgcn_s_memrealtime())

__global__ void __launch_bounds__(768)
pa_kernel(const char* __restrict__ q, const char* __restrict__ kc, const char* __restrict__ vc,
          const int* __restrict__ bt, const int* __restrict__ lens, char* __restrict__ out, char* __restrict__ ws) {
  __shared__ __attribute__((aligned(16384))) char lds[TAB + 512];
  int cpf;
  {
    uint64_t pc;
    asm volatile("s_getpc_b64 %1\n\tv_lshlrev_b32 %0, 4, %2\n\tv_min_u32 %0, " CPFS ", %0\n\tglobal_load_dword %0, %0, %1" : "=&v"(cpf), "=&s"(pc) : "v"(threadIdx.x));
  }
  const uint64_t t64 = __builtin_amdgcn_s_memrealtime();
  const int lane = threadIdx.x & 63;
  const int wave = __builtin_amdgcn_readfirstlane(threadIdx.x >> 6);
  const int b = blockIdx.x >> 3, h = (blockIdx.x - b) & 7;
  const int g = lane >> 4, c = lane & 15;
  const unsigned base = (unsigned)(uintptr_t)LDSP(char, lds);
  if (threadIdx.x - 704 < 48) *LDSP(u32x2, base + WORDS + 8 * lane) = u32x2{0, 0};

  const int ly = lane < 32 ? lane : (lane ^ 16) & 31;
  unsigned go[4];
#pragma unroll
  for (int u = 0; u < 4; ++u) go[u] = (4 * u + g) * 2048 + ((c ^ (4 * u + g)) << 4) - 1024 * u;
  const unsigned ring = base + wave * 8192;
  u32x2 first;
  int len, leny;
  u32x2 pg;
  asm volatile("s_waitcnt lgkmcnt(0)\n\ts_load_dwordx2 %3, %8, %9\n\tglobal_load_dword %0, %4, %7\n\tglobal_load_dword %1, %5, %7\n\tglobal_load_dwordx2 %2, %6, %8"
               : "=&v"(len), "=&v"(leny), "=&v"(pg), "=&s"(first)
               : "v"(4 * (lane & 31)), "v"(4 * ly), "v"(4 * b * NBPS + 8 * lane), "s"(lens), "s"(bt), "s"(4 * b * NBPS + 8 * wave),
                 "s"(q), "s"(kc), "s"(vc), "s"(out), "s"(ws), "s"(t64), "v"(go[0]), "v"(go[1]), "v"(go[2]), "v"(go[3]), "s"(ring), "s"(h) : "memory");
  const unsigned t0 = t64;
  u32x4 qf[4];
  if ((wave & 14) == 8)
    asm volatile("global_load_dwordx4 %0, %4, %5\n\tglobal_load_dwordx4 %1, %4, %5 offset:64\n\tglobal_load_dwordx4 %2, %4, %5 offset:128\n\tglobal_load_dwordx4 %3, %4, %5 offset:192\n\ts_setprio 3"
                 : "=&v"(qf[0]), "=&v"(qf[1]), "=&v"(qf[2]), "=&v"(qf[3]) : "v"((b * 8 + h) * 4096 + c * 256 + g * 16), "s"(q) : "memory");
  asm volatile("s_barrier\n\ts_waitcnt lgkmcnt(0)" : "+s"(first) :: "memory");
  if (wave < 4) {
    const u32x4 r0 = srd(kc + (size_t)(first[0] * 128 + h) * 256, 32768), r1 = srd(kc + (size_t)(first[1] * 128 + h) * 256, 32768);
    if (wave > 1) while ((int)(NOW() - t0) < EDEL) {}
    DMA4(KPOL, ring + EARLY, r0, go);
    DMA4(KPOL, ring + EARLY + 4096, r1, go);
  }
  asm volatile("s_waitcnt vmcnt(8)\n\ts_cmp_lt_u32 %8, 4\n\ts_cbranch_scc1 1f\n\ts_waitcnt vmcnt(0)\n1:"
               : "+v"(len), "+v"(leny), "+v"(pg), "+v"(cpf), "+v"(qf[0]), "+v"(qf[1]), "+v"(qf[2]), "+v"(qf[3]) : "s"(wave) : "scc", "memory");
  const unsigned unimask = bal32(len <= 0);
  if (len <= 0 || len > SKV) len = SKV;
  const int lenb = rl(len, b), qb = (lenb + 63) >> 6, pre = qb > 2 ? 6 : 4;
#pragma unroll
  for (int u = 0; u < 2; ++u) pg[u] = (pg[u] * 128 + h) | min(max(lenb - 32 * lane - 16 * u, 0), 16) << 27;
  if (wave >= 4 && wave < pre) {
    const unsigned p0 = rl(pg[0], 2 * qb - 6 + wave), p1 = rl(pg[1], 2 * qb - 6 + wave);
    const u32x4 r0 = srd(kc + (size_t)(p0 & 0x7ffffff) * 256, p0 >> 27 << 11), r1 = srd(kc + (size_t)(p1 & 0x7ffffff) * 256, p1 >> 27 << 11);
    DMA4(KPOL, ring + EARLY, r0, go);
    DMA4(KPOL, ring + EARLY + 4096, r1, go);
  }

  const bool gives = (h & 3) == 1;
  if (wave == 11) {
    __builtin_amdgcn_s_setprio(3);
    const unsigned k0 = min(pg[0] >> 13 & 0x3fff, 63u), k1 = min(pg[1] >> 13 & 0x3fff, 63u);
    const bool last = lane >> 1 == qb - 1;
    int p0 = 2 * lane + (lane < 4 ? 0 : last ? 12 - 4 * qb : 4), p1 = p0 + 1, lim = 2 * qb;
    if (gives) while (!(lim = __builtin_amdgcn_readfirstlane(lds_ld(base + WORDS + 80)))) __builtin_amdgcn_s_sleep(1);
    const bool mid = lane >= 6 && lane < lim - 2;
    if (mid) {
      p0 = __hip_atomic_fetch_add(LDSP(int, base + CNT + 4 * k0), 1, __ATOMIC_RELAXED, __HIP_MEMORY_SCOPE_WORKGROUP);
      p1 = __hip_atomic_fetch_add(LDSP(int, base + CNT + 4 * k1), 1, __ATOMIC_RELAXED, __HIP_MEMORY_SCOPE_WORKGROUP);
    }
    const int n = lds_ld(base + CNT + 4 * lane);
    int s = scan32(n);
    s += __builtin_amdgcn_update_dpp(0, s, 0x143, 0xc, 0xf, false) + 16 - n;
    const int s0 = __builtin_amdgcn_ds_bpermute(4 * k0, s), s1 = __builtin_amdgcn_ds_bpermute(4 * k1, s);
    if (lane < 4 || lane < lim - 2 || last) {
      *LDSP(unsigned, base + TAB + 4 * (mid ? p0 + s0 : p0)) = pg[0];
      *LDSP(unsigned, base + TAB + 4 * (mid ? p1 + s1 : p1)) = pg[1];
    }
    asm volatile("s_waitcnt lgkmcnt(0)" ::: "memory");
    lds_put(base + WORDS + 92, 1);
  }

  if (leny <= 0 || leny > SKV) leny = SKV;
  const int key = ((len + 15) >> 4) * 32 + (lane & 31), keyy = ((leny + 15) >> 4) * 32 + ly;
  unsigned less = (unsigned)(keyy - key) >> 31;
#define ROR(n) less = __builtin_amdgcn_alignbit(less, __builtin_amdgcn_update_dpp(0, keyy, 0x120 + n, 0xf, 0xf, true) - key, 31);
  ROR(1) ROR(2) ROR(3) ROR(4) ROR(5) ROR(6) ROR(7) ROR(8) ROR(9) ROR(10) ROR(11) ROR(12) ROR(13) ROR(14) ROR(15)
  int rank = __builtin_popcount(less);
  {
    auto t = __builtin_amdgcn_permlane32_swap(rank, rank, false, false);
    rank = t[0] + t[1];
  }
  const int tot = rl(scan32(key >> 5), 31), rb = rl(rank, b);
  const int rg = gives ? 31 - rb : 2 * rb + (h >> 1 & 1);
  const int ph = gives ? h - 1 + 2 * (rg & 1) : (h & 4) | 1;
  const int pb = __builtin_ctz(bal32(rank == (gives ? rg >> 1 : 31 - rg)) | 0x80000000u);
  const int npb = (lenb + 15) >> 4, npp = (rl(len, pb) + 15) >> 4;
  const int npg = gives ? npb : npp, nph = gives ? npp : npb;
  int nq = min(min((npg - ((tot * CAPS) >> 11) + QRND) >> 2, ((npg + 3) >> 2) - 3), min(((((tot * LMAX) >> 11) - nph) * 43) >> 8, min((128 - nph) >> 2, 16)));
  if (nq < 0 || rg > 31 || nph < 13 || (h & 3) == 3) nq = 0;
  nq = __builtin_amdgcn_readfirstlane(nq);
  const int xw = gives ? 0 : 2 * nq, nb = 2 * __builtin_amdgcn_readfirstlane(max(gives ? qb - nq : qb, 2));
  const int tend = ((tot - TSUB) * TAU16) >> 9;

  if (wave < 8) {
    lds_put(base + WORDS + 80, nb);
    const int ev = wave + 8 * (lane >> 1), f = ev - 8, eo = f < 0 ? ev : max(ev - 2 * xw, 8);
    const bool foreign = (unsigned)f < (unsigned)(2 * xw), isv = foreign ? f >= xw : eo >= nb;
    const int i = isv && !foreign ? eo - nb : eo, rest = qb > 2 ? lenb + 64 - 64 * qb : 64, nt = 32 * nb - 64 + rest;
    const int cost = max(32 * i - 64 + rest, min(32 * i, 128 + rest)) + (isv && !foreign ? nt : 0) + (32 + 2 * SCOST) * min(max(f, 0), 2 * xw);
    const float rc = __builtin_amdgcn_rcpf((float)(2 * nt + (64 + 4 * SCOST) * xw)) * (float)max(tend - (D0), 0);
    const unsigned tab = ((unsigned)((float)cost * rc) + (D0)) << 1 | isv;
    const unsigned ta = base + TAB + ((8 * (foreign ? nb + f - (isv ? xw : 0) : i) + 4 * (lane & 1)) & 511);
    const int kap = __builtin_amdgcn_readfirstlane((int)(rc * (32 * KAP)));
    int freed = pre;
    asm volatile("" :: "v"(tab), "v"(ta), "s"(kap));
    const int l0 = (wave - (wave < nb ? 2 : nb)) & 63;
    const unsigned f0 = rl(pg[0], l0), f1 = rl(pg[1], l0);
    unsigned pt = lane & 1 ? f1 : f0;
    for (int e = wave;;) {
      unsigned ti;
      if (e >= pre) {
        const unsigned d = rl(tab, e >> 2), p0 = rl(pt, e >> 2 & 62), p1 = rl(pt, e >> 2 | 1);
        const int due = (int)d >> 1;
        const char* const from = d & 1 ? vc : kc;
        const u32x4 r0 = srd(from + (size_t)(p0 & 0x7ffffff) * 256, p0 >> 27 << 11), r1 = srd(from + (size_t)(p1 & 0x7ffffff) * 256, p1 >> 27 << 11);
        asm volatile("" :: "s"(r0), "s"(r1));
        while (freed + 7 < e) {
          const uint64_t x = __hip_atomic_load(LDSP(uint64_t, base + WORDS + 32), __ATOMIC_RELAXED, __HIP_MEMORY_SCOPE_WORKGROUP);
          const uint64_t y = __hip_atomic_load(LDSP(uint64_t, base + WORDS + 40), __ATOMIC_RELAXED, __HIP_MEMORY_SCOPE_WORKGROUP);
          freed = __builtin_amdgcn_readfirstlane(min(min((int)x, (int)(x >> 32)), min((int)y, (int)(y >> 32))));
          if (freed + 7 < e) __builtin_amdgcn_s_sleep(1);
        }
        int late;
        do {
          ti = NOW();
          late = ti - t0 - due;
        } while (late < __builtin_amdgcn_readfirstlane(lds_ld(base + WORDS + 48)) >> 16);
        ti += (late * (gives ? BETA2 : BETA) >> 4) + (gives ? LREF2 : LREF);
        DMA4(KPOL, ring, r0, go);
        DMA4(KPOL, ring + 4096, r1, go);
      }
      WAITVM0();
      lds_put(base + WORDS + 4 * wave, e + 1);
      if (e >= pre) {
        const int ex = min(max((int)(NOW() - ti), -LNEG), LCAP) * kap;
        if (lane == 0) {
          __hip_atomic_fetch_add(LDSP(int, base + WORDS + 48), ex, __ATOMIC_RELAXED, __HIP_MEMORY_SCOPE_WORKGROUP);
          __hip_atomic_fetch_max(LDSP(int, base + WORDS + 48), 0, __ATOMIC_RELAXED, __HIP_MEMORY_SCOPE_WORKGROUP);
        }
      }
      e += 8;
      if (e >= 2 * (nb + xw)) break;
      if (e < 16) {
        for (;;) {
          const uint64_t x = __hip_atomic_load(LDSP(uint64_t, base + WORDS + 88), __ATOMIC_RELAXED, __HIP_MEMORY_SCOPE_WORKGROUP);
          asm volatile("" ::: "memory");
          pt = *LDSP(const unsigned, ta);
          asm volatile("" : "+v"(pt) :: "memory");
          if (__builtin_amdgcn_readfirstlane(((unsigned)x | !xw) & (unsigned)(x >> 32))) break;
          __builtin_amdgcn_s_sleep(1);
        }
      }
    }
    return;
  }

  const int x = wave & 1;
  const bool other = wave >= 10;
  const unsigned used = base + WORDS + 4 * wave;
  char* const slot = ws + (unsigned)(gives ? b * 8 + h : pb * 8 + ph) * SLOT;
  unsigned tag = 0;
  lds_put(used, !other ? x : xw ? 8 + x : 1 << 20);
  if (other) {
    if (!nq || (gives && x)) return;
    if (!x && !gives) {
      if (lane < 2 * xw) *LDSP(unsigned, base + TAB + 8 * nb + 4 * lane) = (bt[pb * NBPS + 4 * (((npp + 3) >> 2) - 1 - nq) + lane] * 128 + ph) | 16u << 27;
      asm volatile("s_waitcnt lgkmcnt(0)" ::: "memory");
      lds_put(base + WORDS + 88, 1);
    }
    tag = __hip_atomic_load((gu32*)(slot + EPOCH_OFF), __ATOMIC_RELAXED, __HIP_MEMORY_SCOPE_AGENT) + TAGK;
    if (gives) {
      while ((int)(NOW() - t0) < FETCH_AT) __builtin_amdgcn_s_sleep(32);
#pragma nounroll
      for (int spin = 0; spin < (1 << 22); ++spin) {
        if (__builtin_amdgcn_readfirstlane(__hip_atomic_load((gu32*)(slot + FLAG_OFF), __ATOMIC_RELAXED, __HIP_MEMORY_SCOPE_AGENT) == tag)) break;
        __builtin_amdgcn_s_sleep(8);
      }
      asm volatile("" ::: "memory");
      const __amdgpu_buffer_rsrc_t pr = rsrc(slot, 4096 + 64);
      u32x4 y[4];
#pragma unroll
      for (int r = 0; r < 4; ++r) y[r] = __builtin_amdgcn_raw_buffer_load_b128(pr, lane * 16 + r * 1024, 0, 16);
      const unsigned lse = __builtin_amdgcn_raw_buffer_load_b32(pr, 4096 + 4 * c, 0, 16);
#pragma unroll
      for (int r = 0; r < 4; ++r) *LDSP(u32x4, base + FD + 1024 * r + 16 * lane) = y[r];
      *LDSP(unsigned, base + FD + 4096 + 4 * lane) = lse;
      asm volatile("s_waitcnt lgkmcnt(0)" ::: "memory");
      lds_put(base + WORDS + 56, 1);
      if (lane == 0) __hip_atomic_store((gu32*)(slot + EPOCH_OFF), tag, __ATOMIC_RELAXED, __HIP_MEMORY_SCOPE_AGENT);
      return;
    }
  }
  __builtin_amdgcn_s_setprio(3);
  if (other) {
#pragma unroll
    for (int u = 0; u < 4; ++u) qf[u] = *(const u32x4*)(q + (pb * 8 + ph) * 4096 + c * 256 + g * 16 + 64 * u);
  }
  if ((unimask >> (other ? pb : b)) & 1) {
#pragma unroll
    for (int u = 0; u < 4; ++u) qf[u] = u32x4{0, 0, 0, 0};
  }
  const float CS = 0.088388347648318440f * 1.4426950408889634f;
  const unsigned ka = base + c * 256 + ((g ^ c) << 4);
  const unsigned va = base + (lane >> 2) * 256 + ((((lane & 3) << 2) ^ (lane >> 2) ^ (2 * x)) << 4);
  float m = -1e30f, l = 0.f;
  asm volatile("" : "+v"(qf[0]), "+v"(qf[1]), "+v"(qf[2]), "+v"(qf[3]), "+v"(tag));
  int seen = 0;
  auto look = [&](int e) __attribute__((always_inline)) { seen = lds_ld(base + WORDS + 4 * (e & 7)); };
  auto landed = [&](int e) __attribute__((always_inline)) {
    while (__builtin_amdgcn_readfirstlane(seen) <= e) look(e);
    asm volatile("" ::: "memory");
  };
  if (other) while (__builtin_amdgcn_readfirstlane(min(lds_ld(base + WORDS + 32), lds_ld(base + WORDS + 36))) < 6) __builtin_amdgcn_s_sleep(1);
#define PIN() __builtin_amdgcn_sched_barrier(0)
  const int i0 = other ? nb : 0, i1 = other ? nb + xw : nb, ev = other ? 8 + xw : nb + 2 * xw;
  int e = (other ? 8 : 0) + x;
  for (int i = i0 + x; i < i1; i += 2) {
    landed(e);
    u32x4 kf[2][4];
#pragma unroll
    for (int u = 0; u < 4; ++u) {
      const unsigned a = ((e & 7) * 8192 + (e < 6 ? EARLY : 0) + ka) ^ (u << 6);
      kf[0][u] = *LDSP(const u32x4, a);
      kf[1][u] = *LDSP(const u32x4, a + 4096);
    }
    e = min(e + (i >> 1 == 3 ? 2 + 2 * xw : 2), ev);
    asm volatile("" ::: "memory");
    lds_put(used, e);
    PIN();
    f32x4 sc[2] = {{0.f, 0.f, 0.f, 0.f}, {0.f, 0.f, 0.f, 0.f}};
#pragma unroll
    for (int u = 0; u < 4; ++u)
#pragma unroll
      for (int t = 0; t < 2; ++t)
        sc[t] = __builtin_amdgcn_mfma_f32_16x16x32_bf16(__builtin_bit_cast(bf16x8, kf[t][u]), __builtin_bit_cast(bf16x8, qf[u]), sc[t], 0, 0, 0);
    if (i < 6) {
      const int rest = (i < 4 ? lenb : lenb + 192 - 64 * qb) - 32 * i;
#pragma unroll
      for (int r = 0; r < 8; ++r) sc[r >> 2][r & 3] = 16 * (r >> 2) + 4 * g + (r & 3) < rest ? sc[r >> 2][r & 3] : -__builtin_inff();
    }
    const float mx = xg_max(vmax(vmax(vmax(sc[0][0], sc[0][1]), vmax(sc[0][2], sc[0][3])), vmax(vmax(sc[1][0], sc[1][1]), vmax(sc[1][2], sc[1][3]))));
    const float mn = vmax(m, __builtin_ceilf(mx * CS));
    PIN();
    look(e);
    PIN();
    float pr[8];
#pragma unroll
    for (int r = 0; r < 8; ++r) pr[r] = __builtin_amdgcn_exp2f(__builtin_fmaf(sc[r >> 2][r & 3], CS, -mn));
    l *= __builtin_amdgcn_exp2f(m - mn);
    m = mn;
    u32x4 pw;
#pragma unroll
    for (int r = 0; r < 4; ++r) {
      const unsigned w = cvtpk(pr[2 * r], pr[2 * r + 1]);
      pw[r] = w;
      l = __builtin_amdgcn_fdot2_f32_bf16(__builtin_bit_cast(bf16x2, w), bf16x2{1, 1}, l, false);
    }
    *LDSP(u32x4, base + PB + 1024 * i + 16 * lane) = pw;
    *LDSP(float, base + MB + 64 * i + 4 * c) = mn;
  }
  l = xg_sum(l);
  *LDSP(f32x2, base + XL - 4096 + 512 * wave + 8 * lane) = f32x2{m, l};
  asm volatile("" ::: "memory");
  lds_put(used + 32, 1);
  unsigned pa = base + PB + 1024 * i0 + 16 * lane, ma = base + MB + 64 * i0 + 4 * c;
  f32x4 acc[4] = {{0.f, 0.f, 0.f, 0.f}, {0.f, 0.f, 0.f, 0.f}, {0.f, 0.f, 0.f, 0.f}, {0.f, 0.f, 0.f, 0.f}};
  u32x4 pw;
  float mi;
  {
    f32x2 o;
    int ok;
    do {
      ok = lds_ld((used ^ 4) + 32);
      asm volatile("" ::: "memory");
      o = *LDSP(const f32x2, base + XL - 4096 + 512 * (wave ^ 1) + 8 * lane);
      pw = *LDSP(const u32x4, pa);
      mi = *LDSP(const float, ma);
      asm volatile("" : "+v"(o), "+v"(pw), "+v"(mi) :: "memory");
    } while (!__builtin_amdgcn_readfirstlane(ok));
    const float mn = vmax(m, o[0]);
    l = l * __builtin_amdgcn_exp2f(m - mn) + o[1] * __builtin_amdgcn_exp2f(o[0] - mn);
    m = mn;
  }
  float rl_ = __builtin_amdgcn_rcpf(l);
  const int e1 = e + i1 - i0;
  for (int e2 = nq && gives ? e1 - MG : e1;;) {
    asm volatile("" : "+v"(rl_));
    landed(e);
    u32x4 vt[4];
#pragma unroll
    for (int k = 0; k < 4; ++k) {
      const unsigned a = (((e & 7) * 8192 + va) ^ ((k >> 1) << 4)) + 8 * (k & 1);
      const u32x2 lo = __builtin_bit_cast(u32x2, __builtin_amdgcn_ds_read_tr16_b64_v4i16(LDSP(s16x4, a)));
      const u32x2 hi = __builtin_bit_cast(u32x2, __builtin_amdgcn_ds_read_tr16_b64_v4i16(LDSP(s16x4, a + 4096)));
      vt[k] = u32x4{lo[0], lo[1], hi[0], hi[1]};
    }
    ++e;
    asm volatile("" ::: "memory");
    lds_put(used, e == e1 ? 1 << 20 : e);
    PIN();
    const float dn = (m - mi) * (128.f / 65535.f);
    const u16x2 sh = __builtin_amdgcn_cvt_pknorm_u16(dn, dn);
    u32x4 pf;
#pragma unroll
    for (int r = 0; r < 4; ++r) {
      const unsigned w = pw[r];
      pf[r] = __builtin_bit_cast(unsigned, __builtin_elementwise_sub_sat(__builtin_bit_cast(u16x2, w), sh));
    }
    PIN();
    look(e);
    pa += 1024; ma += 64;
    pw = *LDSP(const u32x4, pa);
    mi = *LDSP(const float, ma);
    PIN();
#pragma unroll
    for (int k = 0; k < 4; ++k) acc[k] = __builtin_amdgcn_mfma_f32_16x16x32_bf16(__builtin_bit_cast(bf16x8, vt[k]), __builtin_bit_cast(bf16x8, pf), acc[k], 0, 0, 0);
    if (e == e2) {
      if (e == e1) break;
      e2 = e1;
      while (!__builtin_amdgcn_readfirstlane(lds_ld(base + WORDS + 56))) {}
      asm volatile("" ::: "memory");
      const u32x4 y0 = *LDSP(const u32x4, base + FD + 2048 * x + 16 * lane), y1 = *LDSP(const u32x4, base + FD + 2048 * x + 1024 + 16 * lane);
      const float lf = *LDSP(const float, base + FD + 4096 + 4 * lane);
      const float mn = vmax(m, __builtin_ceilf(lf)), sa = __builtin_amdgcn_exp2f(m - mn), wt = __builtin_amdgcn_exp2f(lf - mn);
      m = mn;
      rl_ = __builtin_amdgcn_rcpf(l * sa + wt);
#pragma unroll
      for (int k = 0; k < 4; ++k) {
        const unsigned lo = (k >> 1 ? y1 : y0)[2 * (k & 1)], hi = (k >> 1 ? y1 : y0)[2 * (k & 1) + 1];
        acc[k] = acc[k] * sa + __builtin_bit_cast(f32x4, u32x4{lo << 16, lo & 0xffff0000u, hi << 16, hi & 0xffff0000u}) * wt;
      }
    }
  }
#pragma unroll
  for (int k = 0; k < 4; ++k) acc[k] *= rl_;
  u32x4 y[2];
#pragma unroll
  for (int r = 0; r < 2; ++r)
    y[r] = u32x4{cvtpk(acc[2 * r][0], acc[2 * r][1]), cvtpk(acc[2 * r][2], acc[2 * r][3]),
                 cvtpk(acc[2 * r + 1][0], acc[2 * r + 1][1]), cvtpk(acc[2 * r + 1][2], acc[2 * r + 1][3])};
  if (!other) {
    const __amdgpu_buffer_rsrc_t orr = rsrc(out + (b * 8 + h) * 4096, 4096);
#pragma unroll
    for (int r = 0; r < 2; ++r) __builtin_amdgcn_raw_buffer_store_b128(y[r], orr, c * 256 + g * 64 + x * 32 + r * 16, 0, OPOL);
  } else {
    const __amdgpu_buffer_rsrc_t orr = rsrc(slot, 4096 + 64);
#pragma unroll
    for (int r = 0; r < 2; ++r) __builtin_amdgcn_raw_buffer_store_b128(y[r], orr, lane * 16 + x * 2048 + r * 1024, 0, 16);
    if (!x) __builtin_amdgcn_raw_buffer_store_b32(__float_as_uint(m + __builtin_amdgcn_logf(l)), orr, lane * 4 + 4096, 0, 16);
    WAITVM0();
    if (lane == 0 && __hip_atomic_fetch_add(LDSP(int, base + WORDS + 60), 1, __ATOMIC_RELAXED, __HIP_MEMORY_SCOPE_WORKGROUP))
      __hip_atomic_store((gu32*)(slot + FLAG_OFF), tag, __ATOMIC_RELAXED, __HIP_MEMORY_SCOPE_AGENT);
    return;
  }
}

extern "C" void kernel_launch(void* const* d_in, const int* in_sizes, int n_in, void* d_out, int out_size,
                              void* d_ws, size_t ws_size, hipStream_t stream) {
  pa_kernel<<<256, 768, 0, stream>>>((const char*)d_in[0], (const char*)d_in[1], (const char*)d_in[2],
                                     (const int*)d_in[3], (const int*)d_in[4], (char*)d_out, (char*)d_ws);
}
```

```cpp
#include <hip/hip_runtime.h>
#include <hip/hip_bf16.h>
#include <stdint.h>

#define CAPS 65
#define D0 10
#define QRND 1
#define LMAX 84
#define EDEL 90
#define SCOST 8
#define FETCH_AT (tend - 500 - 16 * rg)
#define TAU16 452
#define TSUB 38
#define KAP 300
#define LREF 88
#define LNEG 40
#define LCAP 100
#define BETA 6
#define BETA2 BETA
#define LREF2 110
#define MG 5
#define TOFF 0
#define OPOL 2
#define CPFS "0x2100"
#define KPOL " nt"
#define VPOL " nt"

typedef __attribute__((ext_vector_type(8))) __bf16 bf16x8;
typedef __attribute__((ext_vector_type(4))) short s16x4;
typedef __attribute__((ext_vector_type(4))) float f32x4;
typedef __attribute__((ext_vector_type(4))) unsigned u32x4;
typedef __attribute__((ext_vector_type(2))) unsigned u32x2;
typedef __attribute__((address_space(1))) unsigned gu32;
#define LDSP(T, a) ((__attribute__((address_space(3))) T*)(uintptr_t)(a))

constexpr int NBPS = 128, SKV = NBPS * 16;
constexpr unsigned PB = 8 * 8192, MB = PB + 64 * 1024, FD = MB + 64 * 64, XL = FD + 4096 + 256;
constexpr unsigned WORDS = XL + 4 * 512, CNT = WORDS + 128, TAB = CNT + 256;
constexpr unsigned EARLY = PB + 16 * 1024;
constexpr unsigned SLOT = 8192, FLAG_OFF = 4096 + 64, EPOCH_OFF = 4096 + 68;
constexpr unsigned STARTS = 256 * SLOT;
constexpr unsigned TAGK = 0x9E3779B9u;
typedef __attribute__((ext_vector_type(2))) unsigned short u16x2;
typedef __attribute__((ext_vector_type(2))) float f32x2;
typedef __attribute__((ext_vector_type(2))) __bf16 bf16x2;

__device__ __forceinline__ unsigned cvtpk(float lo, float hi) {
  bf16x2 r = {(__bf16)lo, (__bf16)hi};
  return __builtin_bit_cast(unsigned, r);
}
__device__ __forceinline__ float vmax(float a, float b) { return __builtin_elementwise_maximum(a, b); }
__device__ __forceinline__ float xg_max(float x) {
  auto s = __builtin_amdgcn_permlane16_swap(__float_as_uint(x), __float_as_uint(x), false, false);
  x = vmax(__uint_as_float(s[0]), __uint_as_float(s[1]));
  auto t = __builtin_amdgcn_permlane32_swap(__float_as_uint(x), __float_as_uint(x), false, false);
  return vmax(__uint_as_float(t[0]), __uint_as_float(t[1]));
}
__device__ __forceinline__ float xg_sum(float x) {
  auto s = __builtin_amdgcn_permlane16_swap(__float_as_uint(x), __float_as_uint(x), false, false);
  x = __uint_as_float(s[0]) + __uint_as_float(s[1]);
  auto t = __builtin_amdgcn_permlane32_swap(__float_as_uint(x), __float_as_uint(x), false, false);
  return __uint_as_float(t[0]) + __uint_as_float(t[1]);
}
__device__ __forceinline__ int scan32(int v) {
  v += __builtin_amdgcn_update_dpp(0, v, 0x111, 0xf, 0xf, false);
  v += __builtin_amdgcn_update_dpp(0, v, 0x112, 0xf, 0xf, false);
  v += __builtin_amdgcn_update_dpp(0, v, 0x114, 0xf, 0xf, false);
  v += __builtin_amdgcn_update_dpp(0, v, 0x118, 0xf, 0xf, false);
  v += __builtin_amdgcn_update_dpp(0, v, 0x142, 0xa, 0xf, false);
  return v;
}
__device__ __forceinline__ __amdgpu_buffer_rsrc_t rsrc(const void* p, unsigned n) {
  return __builtin_amdgcn_make_buffer_rsrc((void*)p, 0, n, 0x00020000);
}
__device__ __forceinline__ u32x4 srd(const void* p, unsigned n) {
  const uint64_t a = (uint64_t)p;
  return u32x4{(unsigned)a, (unsigned)(a >> 32) & 0xffffu, n, 0x00020000u};
}
__device__ __forceinline__ int rl(int v, int l) { return __builtin_amdgcn_readlane(v, l); }
__device__ __forceinline__ unsigned bal32(bool c) { return (unsigned)__builtin_amdgcn_ballot_w64(c); }
__device__ __forceinline__ int lds_ld(unsigned a) {
  return __hip_atomic_load(LDSP(int, a), __ATOMIC_RELAXED, __HIP_MEMORY_SCOPE_WORKGROUP);
}
__device__ __forceinline__ void lds_put(unsigned a, int v) {
  __hip_atomic_store(LDSP(int, a), v, __ATOMIC_RELAXED, __HIP_MEMORY_SCOPE_WORKGROUP);
}

#define DMA4(POL, dst, rs, v)                                                                         \
  do {                                                                                                \
    unsigned keep_;                                                                                   \
    asm volatile("s_mov_b32 %0, m0\n\ts_mov_b32 m0, %1\n\ts_nop 4\n\t"                                \
                 "buffer_load_dwordx4 %3, %2, 0 offen" POL " lds\n\t"                                 \
                 "buffer_load_dwordx4 %4, %2, 0 offen offset:1024" POL " lds\n\t"                     \
                 "buffer_load_dwordx4 %5, %2, 0 offen offset:2048" POL " lds\n\t"                     \
                 "buffer_load_dwordx4 %6, %2, 0 offen offset:3072" POL " lds\n\t"                     \
                 "s_mov_b32 m0, %0"                                                                   \
                 : "=&s"(keep_) : "s"(dst), "s"(rs), "v"(v[0]), "v"(v[1]), "v"(v[2]), "v"(v[3]) : "memory"); \
  } while (0)
#define WAITVM0() asm volatile("s_waitcnt vmcnt(0)" ::: "memory")
#define NOW() ((unsigned)__builtin_amdgcn_s_memrealtime())

__global__ void __launch_bounds__(768)
pa_kernel(const char* __restrict__ q, const char* __restrict__ kc, const char* __restrict__ vc,
          const int* __restrict__ bt, const int* __restrict__ lens, char* __restrict__ out, char* __restrict__ ws) {
  __shared__ __attribute__((aligned(16384))) char lds[TAB + 512];
  int cpf;
  {
    uint64_t pc;
    asm volatile("s_getpc_b64 %1\n\tv_lshlrev_b32 %0, 4, %2\n\tv_min_u32 %0, " CPFS ", %0\n\tglobal_load_dword %0, %0, %1" : "=&v"(cpf), "=&s"(pc) : "v"(threadIdx.x));
  }
  const uint64_t t64 = __builtin_amdgcn_s_memrealtime();
  const int lane = threadIdx.x & 63;
  const int wave = __builtin_amdgcn_readfirstlane(threadIdx.x >> 6);
  const int b = blockIdx.x >> 3, h = (blockIdx.x - b) & 7;
  const int g = lane >> 4, c = lane & 15;
  const unsigned base = (unsigned)(uintptr_t)LDSP(char, lds);
  if (threadIdx.x - 704 < 48) *LDSP(u32x2, base + WORDS + 8 * lane) = u32x2{0, 0};

  const int ly = lane < 32 ? lane : (lane ^ 16) & 31;
  unsigned go[4];
#pragma unroll
  for (int u = 0; u < 4; ++u) go[u] = (4 * u + g) * 2048 + ((c ^ (4 * u + g)) << 4) - 1024 * u;
  const unsigned ring = base + wave * 8192;
  u32x2 first;
  int len, leny;
  u32x2 pg;
  asm volatile("s_waitcnt lgkmcnt(0)\n\ts_load_dwordx2 %3, %8, %9\n\tglobal_load_dword %0, %4, %7\n\tglobal_load_dword %1, %5, %7\n\tglobal_load_dwordx2 %2, %6, %8"
               : "=&v"(len), "=&v"(leny), "=&v"(pg), "=&s"(first)
               : "v"(4 * (lane & 31)), "v"(4 * ly), "v"(4 * b * NBPS + 8 * lane), "s"(lens), "s"(bt), "s"(4 * b * NBPS + 8 * wave),
                 "s"(q), "s"(kc), "s"(vc), "s"(out), "s"(ws), "s"(t64), "v"(go[0]), "v"(go[1]), "v"(go[2]), "v"(go[3]), "s"(ring), "s"(h) : "memory");
  const unsigned t0 = t64;
  u32x4 qf[4];
  if ((wave & 14) == 8)
    asm volatile("global_load_dwordx4 %0, %4, %5\n\tglobal_load_dwordx4 %1, %4, %5 offset:64\n\tglobal_load_dwordx4 %2, %4, %5 offset:128\n\tglobal_load_dwordx4 %3, %4, %5 offset:192\n\ts_setprio 3"
                 : "=&v"(qf[0]), "=&v"(qf[1]), "=&v"(qf[2]), "=&v"(qf[3]) : "v"((b * 8 + h) * 4096 + c * 256 + g * 16), "s"(q) : "memory");
  asm volatile("s_barrier\n\ts_waitcnt lgkmcnt(0)" : "+s"(first) :: "memory");
  if (wave < 4) {
    const u32x4 r0 = srd(kc + (size_t)(first[0] * 128 + h) * 256, 32768), r1 = srd(kc + (size_t)(first[1] * 128 + h) * 256, 32768);
    if (wave > 1) while ((int)(NOW() - t0) < EDEL) {}
    DMA4(KPOL, ring + EARLY, r0, go);
    DMA4(KPOL, ring + EARLY + 4096, r1, go);
  }
  asm volatile("s_waitcnt vmcnt(8)\n\ts_cmp_lt_u32 %8, 4\n\ts_cbranch_scc1 1f\n\ts_waitcnt vmcnt(0)\n1:"
               : "+v"(len), "+v"(leny), "+v"(pg), "+v"(cpf), "+v"(qf[0]), "+v"(qf[1]), "+v"(qf[2]), "+v"(qf[3]) : "s"(wave) : "scc", "memory");
  if (threadIdx.x == 640) __hip_atomic_store((gu32*)(ws + STARTS + 256 * b + 4 * h), t0, __ATOMIC_RELAXED, __HIP_MEMORY_SCOPE_AGENT);
  const unsigned unimask = bal32(len <= 0);
  if (len <= 0 || len > SKV) len = SKV;
  const int lenb = rl(len, b), qb = (lenb + 63) >> 6, pre = qb > 2 ? 6 : 4;
#pragma unroll
  for (int u = 0; u < 2; ++u) pg[u] = (pg[u] * 128 + h) | min(max(lenb - 32 * lane - 16 * u, 0), 16) << 27;
  if (wave >= 4 && wave < pre) {
    const unsigned p0 = rl(pg[0], 2 * qb - 6 + wave), p1 = rl(pg[1], 2 * qb - 6 + wave);
    const u32x4 r0 = srd(kc + (size_t)(p0 & 0x7ffffff) * 256, p0 >> 27 << 11), r1 = srd(kc + (size_t)(p1 & 0x7ffffff) * 256, p1 >> 27 << 11);
    DMA4(KPOL, ring + EARLY, r0, go);
    DMA4(KPOL, ring + EARLY + 4096, r1, go);
  }

  const bool gives = (h & 3) == 1;
  if (wave == 11) {
    __builtin_amdgcn_s_setprio(3);
    const unsigned k0 = min(pg[0] >> 13 & 0x3fff, 63u), k1 = min(pg[1] >> 13 & 0x3fff, 63u);
    const bool last = lane >> 1 == qb - 1;
    int p0 = 2 * lane + (lane < 4 ? 0 : last ? 12 - 4 * qb : 4), p1 = p0 + 1, lim = 2 * qb;
    if (gives) while (!(lim = __builtin_amdgcn_readfirstlane(lds_ld(base + WORDS + 80)))) __builtin_amdgcn_s_sleep(1);
    const bool mid = lane >= 6 && lane < lim - 2;
    if (mid) {
      p0 = __hip_atomic_fetch_add(LDSP(int, base + CNT + 4 * k0), 1, __ATOMIC_RELAXED, __HIP_MEMORY_SCOPE_WORKGROUP);
      p1 = __hip_atomic_fetch_add(LDSP(int, base + CNT + 4 * k1), 1, __ATOMIC_RELAXED, __HIP_MEMORY_SCOPE_WORKGROUP);
    }
    const int n = lds_ld(base + CNT + 4 * lane);
    int s = scan32(n);
    s += __builtin_amdgcn_update_dpp(0, s, 0x143, 0xc, 0xf, false) + 16 - n;
    const int s0 = __builtin_amdgcn_ds_bpermute(4 * k0, s), s1 = __builtin_amdgcn_ds_bpermute(4 * k1, s);
    if (lane < 4 || lane < lim - 2 || last) {
      *LDSP(unsigned, base + TAB + 4 * (mid ? p0 + s0 : p0)) = pg[0];
      *LDSP(unsigned, base + TAB + 4 * (mid ? p1 + s1 : p1)) = pg[1];
    }
    asm volatile("s_waitcnt lgkmcnt(0)" ::: "memory");
    lds_put(base + WORDS + 92, 1);
  }

  if (leny <= 0 || leny > SKV) leny = SKV;
  const int key = ((len + 15) >> 4) * 32 + (lane & 31), keyy = ((leny + 15) >> 4) * 32 + ly;
  unsigned less = (unsigned)(keyy - key) >> 31;
#define ROR(n) less = __builtin_amdgcn_alignbit(less, __builtin_amdgcn_update_dpp(0, keyy, 0x120 + n, 0xf, 0xf, true) - key, 31);
  ROR(1) ROR(2) ROR(3) ROR(4) ROR(5) ROR(6) ROR(7) ROR(8) ROR(9) ROR(10) ROR(11) ROR(12) ROR(13) ROR(14) ROR(15)
  int rank = __builtin_popcount(less);
  {
    auto t = __builtin_amdgcn_permlane32_swap(rank, rank, false, false);
    rank = t[0] + t[1];
  }
  const int tot = rl(scan32(key >> 5), 31), rb = rl(rank, b);
  const int rg = gives ? 31 - rb : 2 * rb + (h >> 1 & 1);
  const int ph = gives ? h - 1 + 2 * (rg & 1) : (h & 4) | 1;
  const int pb = __builtin_ctz(bal32(rank == (gives ? rg >> 1 : 31 - rg)) | 0x80000000u);
  const int npb = (lenb + 15) >> 4, npp = (rl(len, pb) + 15) >> 4;
  const int npg = gives ? npb : npp, nph = gives ? npp : npb;
  int nq = min(min((npg - ((tot * CAPS) >> 11) + QRND) >> 2, ((npg + 3) >> 2) - 3), min(((((tot * LMAX) >> 11) - nph) * 43) >> 8, min((128 - nph) >> 2, 16)));
  if (nq < 0 || rg > 31 || nph < 13 || (h & 3) == 3) nq = 0;
  nq = __builtin_amdgcn_readfirstlane(nq);
  const int xw = gives ? 0 : 2 * nq, nb = 2 * __builtin_amdgcn_readfirstlane(max(gives ? qb - nq : qb, 2));
  const int tend = ((tot - TSUB) * TAU16) >> 9;

  if (wave < 8) {
    lds_put(base + WORDS + 80, nb);
    __builtin_amdgcn_sched_barrier(0);
    const int ev = wave + 8 * (lane >> 1), f = ev - 8, eo = f < 0 ? ev : max(ev - 2 * xw, 8);
    const bool foreign = (unsigned)f < (unsigned)(2 * xw), isv = foreign ? f >= xw : eo >= nb;
    const int i = isv && !foreign ? eo - nb : eo, rest = qb > 2 ? lenb + 64 - 64 * qb : 64, nt = 32 * nb - 64 + rest;
    const int cost = max(32 * i - 64 + rest, min(32 * i, 128 + rest)) + (isv && !foreign ? nt : 0) + (32 + 2 * SCOST) * min(max(f, 0), 2 * xw);
    const float rc = __builtin_amdgcn_rcpf((float)(2 * nt + (64 + 4 * SCOST) * xw)) * (float)max(tend - (D0), 0);
    const unsigned tab = ((unsigned)((float)cost * rc) + (D0)) << 1 | isv;
    const unsigned ta = base + TAB + ((8 * (foreign ? nb + f - (isv ? xw : 0) : i) + 4 * (lane & 1)) & 511);
    const int kap = __builtin_amdgcn_readfirstlane((int)(rc * (32 * KAP)));
    int freed = pre;
    bool ask = wave == 7;
    asm volatile("" :: "v"(tab), "v"(ta), "s"(kap));
    const int l0 = (wave - (wave < nb ? 2 : nb)) & 63;
    const unsigned f0 = rl(pg[0], l0), f1 = rl(pg[1], l0);
    unsigned pt = lane & 1 ? f1 : f0;
    for (int e = wave;;) {
      unsigned ti;
      if (e >= pre) {
        const unsigned d = rl(tab, e >> 2), p0 = rl(pt, e >> 2 & 62), p1 = rl(pt, e >> 2 | 1);
        const int due = (int)d >> 1;
        const char* const from = d & 1 ? vc : kc;
        const u32x4 r0 = srd(from + (size_t)(p0 & 0x7ffffff) * 256, p0 >> 27 << 11), r1 = srd(from + (size_t)(p1 & 0x7ffffff) * 256, p1 >> 27 << 11);
        asm volatile("" :: "s"(r0), "s"(r1));
        while (freed + 7 < e) {
          const uint64_t x = __hip_atomic_load(LDSP(uint64_t, base + WORDS + 32), __ATOMIC_RELAXED, __HIP_MEMORY_SCOPE_WORKGROUP);
          const uint64_t y = __hip_atomic_load(LDSP(uint64_t, base + WORDS + 40), __ATOMIC_RELAXED, __HIP_MEMORY_SCOPE_WORKGROUP);
          freed = __builtin_amdgcn_readfirstlane(min(min((int)x, (int)(x >> 32)), min((int)y, (int)(y >> 32))));
          if (freed + 7 < e) __builtin_amdgcn_s_sleep(1);
        }
        int late, hold;
        do {
          ti = NOW();
          u32x4 w = *LDSP(const volatile u32x4, base + WORDS + 96);
          hold = __builtin_amdgcn_readfirstlane(w[0]) >> 16;
          const int part = (due - __builtin_amdgcn_readfirstlane(w[2])) * __builtin_amdgcn_readfirstlane(w[3]) >> 4;
          late = ti - t0 - due - (part * (__builtin_amdgcn_readfirstlane(w[1]) - (int)t0) >> 16);
        } while (late < hold);
        ti += (late * (gives ? BETA2 : BETA) >> 4) + (gives ? LREF2 : LREF);
        DMA4(KPOL, ring, r0, go);
        DMA4(KPOL, ring + 4096, r1, go);
      }
      int st;
      if (ask) asm volatile("global_load_dword %0, %1, %2 sc1" : "=v"(st) : "v"(STARTS + 256 * b + 4 * (lane & 7)), "s"(ws) : "memory");
      asm volatile("s_waitcnt vmcnt(0)" : "+v"(st) :: "memory");
      lds_put(base + WORDS + 4 * wave, e + 1);
      if (e >= pre) {
        const int ex = min(max((int)(NOW() - ti), -LNEG), LCAP) * kap;
        if (lane == 0) {
          __hip_atomic_fetch_add(LDSP(int, base + WORDS + 96), ex, __ATOMIC_RELAXED, __HIP_MEMORY_SCOPE_WORKGROUP);
          __hip_atomic_fetch_max(LDSP(int, base + WORDS + 96), 0, __ATOMIC_RELAXED, __HIP_MEMORY_SCOPE_WORKGROUP);
        }
      }
      if (ask) {
        st -= t0;
        if (!__builtin_amdgcn_ballot_w64((unsigned)st + 1000u > 2000u)) {
          ask = false;
          st += __builtin_amdgcn_update_dpp(0, st, 0x111, 0xf, 0xf, false);
          st += __builtin_amdgcn_update_dpp(0, st, 0x112, 0xf, 0xf, false);
          st += __builtin_amdgcn_update_dpp(0, st, 0x114, 0xf, 0xf, false);
          const int now = NOW() - t0;
          lds_put(base + WORDS + 100, t0 + TOFF + (rl(st, 7) >> 3));
          asm volatile("" ::: "memory");
          if (tend - now > 300) *LDSP(volatile u32x2, base + WORDS + 104) = u32x2{(unsigned)now, (unsigned)(__builtin_amdgcn_rcpf((float)(tend - now)) * 1048576.f)};
        }
      }
      e += 8;
      if (e >= 2 * (nb + xw)) break;
      if (e < 16) {
        for (;;) {
          const uint64_t x = __hip_atomic_load(LDSP(uint64_t, base + WORDS + 88), __ATOMIC_RELAXED, __HIP_MEMORY_SCOPE_WORKGROUP);
          asm volatile("" ::: "memory");
          pt = *LDSP(const unsigned, ta);
          asm volatile("" : "+v"(pt) :: "memory");
          if (__builtin_amdgcn_readfirstlane(((unsigned)x | !xw) & (unsigned)(x >> 32))) break;
          __builtin_amdgcn_s_sleep(1);
        }
      }
    }
    return;
  }

  const int x = wave & 1;
  const bool other = wave >= 10;
  const unsigned used = base + WORDS + 4 * wave;
  char* const slot = ws + (unsigned)(gives ? b * 8 + h : pb * 8 + ph) * SLOT;
  unsigned tag = 0;
  lds_put(used, !other ? x : xw ? 8 + x : 1 << 20);
  if (other) {
    if (!nq || (gives && x)) return;
    if (!x && !gives) {
      if (lane < 2 * xw) *LDSP(unsigned, base + TAB + 8 * nb + 4 * lane) = (bt[pb * NBPS + 4 * (((npp + 3) >> 2) - 1 - nq) + lane] * 128 + ph) | 16u << 27;
      asm volatile("s_waitcnt lgkmcnt(0)" ::: "memory");
      lds_put(base + WORDS + 88, 1);
    }
    tag = __hip_atomic_load((gu32*)(slot + EPOCH_OFF), __ATOMIC_RELAXED, __HIP_MEMORY_SCOPE_AGENT) + TAGK;
    if (gives) {
      while ((int)(NOW() - t0) < FETCH_AT) __builtin_amdgcn_s_sleep(32);
#pragma nounroll
      for (int spin = 0; spin < (1 << 22); ++spin) {
        if (__builtin_amdgcn_readfirstlane(__hip_atomic_load((gu32*)(slot + FLAG_OFF), __ATOMIC_RELAXED, __HIP_MEMORY_SCOPE_AGENT) == tag)) break;
        __builtin_amdgcn_s_sleep(8);
      }
      asm volatile("" ::: "memory");
      const __amdgpu_buffer_rsrc_t pr = rsrc(slot, 4096 + 64);
      u32x4 y[4];
#pragma unroll
      for (int r = 0; r < 4; ++r) y[r] = __builtin_amdgcn_raw_buffer_load_b128(pr, lane * 16 + r * 1024, 0, 16);
      const unsigned lse = __builtin_amdgcn_raw_buffer_load_b32(pr, 4096 + 4 * c, 0, 16);
#pragma unroll
      for (int r = 0; r < 4; ++r) *LDSP(u32x4, base + FD + 1024 * r + 16 * lane) = y[r];
      *LDSP(unsigned, base + FD + 4096 + 4 * lane) = lse;
      asm volatile("s_waitcnt lgkmcnt(0)" ::: "memory");
      lds_put(base + WORDS + 56, 1);
      if (lane == 0) __hip_atomic_store((gu32*)(slot + EPOCH_OFF), tag, __ATOMIC_RELAXED, __HIP_MEMORY_SCOPE_AGENT);
      return;
    }
  }
  __builtin_amdgcn_s_setprio(3);
  if (other) {
#pragma unroll
    for (int u = 0; u < 4; ++u) qf[u] = *(const u32x4*)(q + (pb * 8 + ph) * 4096 + c * 256 + g * 16 + 64 * u);
  }
  if ((unimask >> (other ? pb : b)) & 1) {
#pragma unroll
    for (int u = 0; u < 4; ++u) qf[u] = u32x4{0, 0, 0, 0};
  }
  const float CS = 0.088388347648318440f * 1.4426950408889634f;
  const unsigned ka = base + c * 256 + ((g ^ c) << 4);
  const unsigned va = base + (lane >> 2) * 256 + ((((lane & 3) << 2) ^ (lane >> 2) ^ (2 * x)) << 4);
  float m = -1e30f, l = 0.f;
  asm volatile("" : "+v"(qf[0]), "+v"(qf[1]), "+v"(qf[2]), "+v"(qf[3]), "+v"(tag));
  int seen = 0;
  auto look = [&](int e) __attribute__((always_inline)) { seen = lds_ld(base + WORDS + 4 * (e & 7)); };
  auto landed = [&](int e) __attribute__((always_inline)) {
    while (__builtin_amdgcn_readfirstlane(seen) <= e) look(e);
    asm volatile("" ::: "memory");
  };
  if (other) while (__builtin_amdgcn_readfirstlane(min(lds_ld(base + WORDS + 32), lds_ld(base + WORDS + 36))) < 6) __builtin_amdgcn_s_sleep(1);
#define PIN() __builtin_amdgcn_sched_barrier(0)
  const int i0 = other ? nb : 0, i1 = other ? nb + xw : nb, ev = other ? 8 + xw : nb + 2 * xw;
  int e = (other ? 8 : 0) + x;
  for (int i = i0 + x; i < i1; i += 2) {
    landed(e);
    u32x4 kf[2][4];
#pragma unroll
    for (int u = 0; u < 4; ++u) {
      const unsigned a = ((e & 7) * 8192 + (e < 6 ? EARLY : 0) + ka) ^ (u << 6);
      kf[0][u] = *LDSP(const u32x4, a);
      kf[1][u] = *LDSP(const u32x4, a + 4096);
    }
    e = min(e + (i >> 1 == 3 ? 2 + 2 * xw : 2), ev);
    asm volatile("" ::: "memory");
    lds_put(used, e);
    PIN();
    f32x4 sc[2] = {{0.f, 0.f, 0.f, 0.f}, {0.f, 0.f, 0.f, 0.f}};
#pragma unroll
    for (int u = 0; u < 4; ++u)
#pragma unroll
      for (int t = 0; t < 2; ++t)
        sc[t] = __builtin_amdgcn_mfma_f32_16x16x32_bf16(__builtin_bit_cast(bf16x8, kf[t][u]), __builtin_bit_cast(bf16x8, qf[u]), sc[t], 0, 0, 0);
    if (i < 6) {
      const int rest = (i < 4 ? lenb : lenb + 192 - 64 * qb) - 32 * i;
#pragma unroll
      for (int r = 0; r < 8; ++r) sc[r >> 2][r & 3] = 16 * (r >> 2) + 4 * g + (r & 3) < rest ? sc[r >> 2][r & 3] : -__builtin_inff();
    }
    const float mx = xg_max(vmax(vmax(vmax(sc[0][0], sc[0][1]), vmax(sc[0][2], sc[0][3])), vmax(vmax(sc[1][0], sc[1][1]), vmax(sc[1][2], sc[1][3]))));
    const float mn = vmax(m, __builtin_ceilf(mx * CS));
    PIN();
    look(e);
    PIN();
    float pr[8];
#pragma unroll
    for (int r = 0; r < 8; ++r) pr[r] = __builtin_amdgcn_exp2f(__builtin_fmaf(sc[r >> 2][r & 3], CS, -mn));
    l *= __builtin_amdgcn_exp2f(m - mn);
    m = mn;
    u32x4 pw;
#pragma unroll
    for (int r = 0; r < 4; ++r) {
      const unsigned w = cvtpk(pr[2 * r], pr[2 * r + 1]);
      pw[r] = w;
      l = __builtin_amdgcn_fdot2_f32_bf16(__builtin_bit_cast(bf16x2, w), bf16x2{1, 1}, l, false);
    }
    *LDSP(u32x4, base + PB + 1024 * i + 16 * lane) = pw;
    *LDSP(float, base + MB + 64 * i + 4 * c) = mn;
  }
  l = xg_sum(l);
  *LDSP(f32x2, base + XL - 4096 + 512 * wave + 8 * lane) = f32x2{m, l};
  asm volatile("" ::: "memory");
  lds_put(used + 32, 1);
  unsigned pa = base + PB + 1024 * i0 + 16 * lane, ma = base + MB + 64 * i0 + 4 * c;
  f32x4 acc[4] = {{0.f, 0.f, 0.f, 0.f}, {0.f, 0.f, 0.f, 0.f}, {0.f, 0.f, 0.f, 0.f}, {0.f, 0.f, 0.f, 0.f}};
  u32x4 pw;
  float mi;
  {
    f32x2 o;
    int ok;
    do {
      ok = lds_ld((used ^ 4) + 32);
      asm volatile("" ::: "memory");
      o = *LDSP(const f32x2, base + XL - 4096 + 512 * (wave ^ 1) + 8 * lane);
      pw = *LDSP(const u32x4, pa);
      mi = *LDSP(const float, ma);
      asm volatile("" : "+v"(o), "+v"(pw), "+v"(mi) :: "memory");
    } while (!__builtin_amdgcn_readfirstlane(ok));
    const float mn = vmax(m, o[0]);
    l = l * __builtin_amdgcn_exp2f(m - mn) + o[1] * __builtin_amdgcn_exp2f(o[0] - mn);
    m = mn;
  }
  float rl_ = __builtin_amdgcn_rcpf(l);
  const int e1 = e + i1 - i0;
  for (int e2 = nq && gives ? e1 - MG : e1;;) {
    asm volatile("" : "+v"(rl_));
    landed(e);
    u32x4 vt[4];
#pragma unroll
    for (int k = 0; k < 4; ++k) {
      const unsigned a = (((e & 7) * 8192 + va) ^ ((k >> 1) << 4)) + 8 * (k & 1);
      const u32x2 lo = __builtin_bit_cast(u32x2, __builtin_amdgcn_ds_read_tr16_b64_v4i16(LDSP(s16x4, a)));
      const u32x2 hi = __builtin_bit_cast(u32x2, __builtin_amdgcn_ds_read_tr16_b64_v4i16(LDSP(s16x4, a + 4096)));
      vt[k] = u32x4{lo[0], lo[1], hi[0], hi[1]};
    }
    ++e;
    asm volatile("" ::: "memory");
    lds_put(used, e == e1 ? 1 << 20 : e);
    PIN();
    const float dn = (m - mi) * (128.f / 65535.f);
    const u16x2 sh = __builtin_amdgcn_cvt_pknorm_u16(dn, dn);
    u32x4 pf;
#pragma unroll
    for (int r = 0; r < 4; ++r) {
      const unsigned w = pw[r];
      pf[r] = __builtin_bit_cast(unsigned, __builtin_elementwise_sub_sat(__builtin_bit_cast(u16x2, w), sh));
    }
    PIN();
    look(e);
    pa += 1024; ma += 64;
    pw = *LDSP(const u32x4, pa);
    mi = *LDSP(const float, ma);
    PIN();
#pragma unroll
    for (int k = 0; k < 4; ++k) acc[k] = __builtin_amdgcn_mfma_f32_16x16x32_bf16(__builtin_bit_cast(bf16x8, vt[k]), __builtin_bit_cast(bf16x8, pf), acc[k], 0, 0, 0);
    if (e == e2) {
      if (e == e1) break;
      e2 = e1;
      while (!__builtin_amdgcn_readfirstlane(lds_ld(base + WORDS + 56))) {}
      asm volatile("" ::: "memory");
      const u32x4 y0 = *LDSP(const u32x4, base + FD + 2048 * x + 16 * lane), y1 = *LDSP(const u32x4, base + FD + 2048 * x + 1024 + 16 * lane);
      const float lf = *LDSP(const float, base + FD + 4096 + 4 * lane);
      const float mn = vmax(m, __builtin_ceilf(lf)), sa = __builtin_amdgcn_exp2f(m - mn), wt = __builtin_amdgcn_exp2f(lf - mn);
      m = mn;
      rl_ = __builtin_amdgcn_rcpf(l * sa + wt);
#pragma unroll
      for (int k = 0; k < 4; ++k) {
        const unsigned lo = (k >> 1 ? y1 : y0)[2 * (k & 1)], hi = (k >> 1 ? y1 : y0)[2 * (k & 1) + 1];
        acc[k] = acc[k] * sa + __builtin_bit_cast(f32x4, u32x4{lo << 16, lo & 0xffff0000u, hi << 16, hi & 0xffff0000u}) * wt;
      }
    }
  }
#pragma unroll
  for (int k = 0; k < 4; ++k) acc[k] *= rl_;
  u32x4 y[2];
#pragma unroll
  for (int r = 0; r < 2; ++r)
    y[r] = u32x4{cvtpk(acc[2 * r][0], acc[2 * r][1]), cvtpk(acc[2 * r][2], acc[2 * r][3]),
                 cvtpk(acc[2 * r + 1][0], acc[2 * r + 1][1]), cvtpk(acc[2 * r + 1][2], acc[2 * r + 1][3])};
  if (!other) {
    const __amdgpu_buffer_rsrc_t orr = rsrc(out + (b * 8 + h) * 4096, 4096);
#pragma unroll
    for (int r = 0; r < 2; ++r) __builtin_amdgcn_raw_buffer_store_b128(y[r], orr, c * 256 + g * 64 + x * 32 + r * 16, 0, OPOL);
  } else {
    const __amdgpu_buffer_rsrc_t orr = rsrc(slot, 4096 + 64);
#pragma unroll
    for (int r = 0; r < 2; ++r) __builtin_amdgcn_raw_buffer_store_b128(y[r], orr, lane * 16 + x * 2048 + r * 1024, 0, 16);
    if (!x) __builtin_amdgcn_raw_buffer_store_b32(__float_as_uint(m + __builtin_amdgcn_logf(l)), orr, lane * 4 + 4096, 0, 16);
    WAITVM0();
    if (lane == 0 && __hip_atomic_fetch_add(LDSP(int, base + WORDS + 60), 1, __ATOMIC_RELAXED, __HIP_MEMORY_SCOPE_WORKGROUP))
      __hip_atomic_store((gu32*)(slot + FLAG_OFF), tag, __ATOMIC_RELAXED, __HIP_MEMORY_SCOPE_AGENT);
    return;
  }
}

extern "C" void kernel_launch(void* const* d_in, const int* in_sizes, int n_in, void* d_out, int out_size,
                              void* d_ws, size_t ws_size, hipStream_t stream) {
  pa_kernel<<<256, 768, 0, stream>>>((const char*)d_in[0], (const char*)d_in[1], (const char*)d_in[2],
                                     (const int*)d_in[3], (const int*)d_in[4], (char*)d_out, (char*)d_ws);
}
```

```cpp
#include <hip/hip_runtime.h>
#include <hip/hip_bf16.h>
#include <stdint.h>

#define CAPS 65
#define NOM0 185
#define NOM2 243
#define GAIN 5
#define SOFF 0
#define HW 96
#define HS 16
#define D0 10
#define QRND 1
#define LMAX 84
#define EDEL 90
#define SCOST 8
#define FETCH_AT (tend - 500 - 16 * rg)
#define TAU16 452
#define TSUB 38
#define KAP 300
#define LREF 88
#define LNEG 40
#define LCAP 100
#define BETA 6
#define BETA2 BETA
#define LREF2 110
#define MG 5
#define TOFF 0
#define OPOL 2
#define CPFS "0x2200"
#define KPOL " nt"
#define VPOL " nt"

typedef __attribute__((ext_vector_type(8))) __bf16 bf16x8;
typedef __attribute__((ext_vector_type(4))) short s16x4;
typedef __attribute__((ext_vector_type(4))) float f32x4;
typedef __attribute__((ext_vector_type(4))) unsigned u32x4;
typedef __attribute__((ext_vector_type(2))) unsigned u32x2;
typedef __attribute__((address_space(1))) unsigned gu32;
#define LDSP(T, a) ((__attribute__((address_space(3))) T*)(uintptr_t)(a))

constexpr int NBPS = 128, SKV = NBPS * 16;
constexpr unsigned PB = 8 * 8192, MB = PB + 64 * 1024, FD = MB + 64 * 64, XL = FD + 4096 + 256;
constexpr unsigned WORDS = XL + 4 * 512, CNT = WORDS + 128, TAB = CNT + 256;
constexpr unsigned EARLY = PB + 16 * 1024;
constexpr unsigned SLOT = 8192, FLAG_OFF = 4096 + 64, EPOCH_OFF = 4096 + 68;
constexpr unsigned STARTS = 256 * SLOT;
constexpr unsigned TAGK = 0x9E3779B9u;
typedef __attribute__((ext_vector_type(2))) unsigned short u16x2;
typedef __attribute__((ext_vector_type(2))) float f32x2;
typedef __attribute__((ext_vector_type(2))) __bf16 bf16x2;

__device__ __forceinline__ unsigned cvtpk(float lo, float hi) {
  bf16x2 r = {(__bf16)lo, (__bf16)hi};
  return __builtin_bit_cast(unsigned, r);
}
__device__ __forceinline__ float vmax(float a, float b) { return __builtin_elementwise_maximum(a, b); }
__device__ __forceinline__ float xg_max(float x) {
  auto s = __builtin_amdgcn_permlane16_swap(__float_as_uint(x), __float_as_uint(x), false, false);
  x = vmax(__uint_as_float(s[0]), __uint_as_float(s[1]));
  auto t = __builtin_amdgcn_permlane32_swap(__float_as_uint(x), __float_as_uint(x), false, false);
  return vmax(__uint_as_float(t[0]), __uint_as_float(t[1]));
}
__device__ __forceinline__ float xg_sum(float x) {
  auto s = __builtin_amdgcn_permlane16_swap(__float_as_uint(x), __float_as_uint(x), false, false);
  x = __uint_as_float(s[0]) + __uint_as_float(s[1]);
  auto t = __builtin_amdgcn_permlane32_swap(__float_as_uint(x), __float_as_uint(x), false, false);
  return __uint_as_float(t[0]) + __uint_as_float(t[1]);
}
__device__ __forceinline__ int scan32(int v) {
  v += __builtin_amdgcn_update_dpp(0, v, 0x111, 0xf, 0xf, false);
  v += __builtin_amdgcn_update_dpp(0, v, 0x112, 0xf, 0xf, false);
  v += __builtin_amdgcn_update_dpp(0, v, 0x114, 0xf, 0xf, false);
  v += __builtin_amdgcn_update_dpp(0, v, 0x118, 0xf, 0xf, false);
  v += __builtin_amdgcn_update_dpp(0, v, 0x142, 0xa, 0xf, false);
  return v;
}
__device__ __forceinline__ __amdgpu_buffer_rsrc_t rsrc(const void* p, unsigned n) {
  return __builtin_amdgcn_make_buffer_rsrc((void*)p, 0, n, 0x00020000);
}
__device__ __forceinline__ u32x4 srd(const void* p, unsigned n) {
  const uint64_t a = (uint64_t)p;
  return u32x4{(unsigned)a, (unsigned)(a >> 32) & 0xffffu, n, 0x00020000u};
}
__device__ __forceinline__ int rl(int v, int l) { return __builtin_amdgcn_readlane(v, l); }
__device__ __forceinline__ unsigned bal32(bool c) { return (unsigned)__builtin_amdgcn_ballot_w64(c); }
__device__ __forceinline__ int lds_ld(unsigned a) {
  return __hip_atomic_load(LDSP(int, a), __ATOMIC_RELAXED, __HIP_MEMORY_SCOPE_WORKGROUP);
}
__device__ __forceinline__ void lds_put(unsigned a, int v) {
  __hip_atomic_store(LDSP(int, a), v, __ATOMIC_RELAXED, __HIP_MEMORY_SCOPE_WORKGROUP);
}

#define DMA4(POL, dst, rs, v)                                                                         \
  do {                                                                                                \
    unsigned keep_;                                                                                   \
    asm volatile("s_mov_b32 %0, m0\n\ts_mov_b32 m0, %1\n\ts_nop 4\n\t"                                \
                 "buffer_load_dwordx4 %3, %2, 0 offen" POL " lds\n\t"                                 \
                 "buffer_load_dwordx4 %4, %2, 0 offen offset:1024" POL " lds\n\t"                     \
                 "buffer_load_dwordx4 %5, %2, 0 offen offset:2048" POL " lds\n\t"                     \
                 "buffer_load_dwordx4 %6, %2, 0 offen offset:3072" POL " lds\n\t"                     \
                 "s_mov_b32 m0, %0"                                                                   \
                 : "=&s"(keep_) : "s"(dst), "s"(rs), "v"(v[0]), "v"(v[1]), "v"(v[2]), "v"(v[3]) : "memory"); \
  } while (0)
#define WAITVM0() asm volatile("s_waitcnt vmcnt(0)" ::: "memory")
#define NOW() ((unsigned)__builtin_amdgcn_s_memrealtime())

__global__ void __launch_bounds__(768)
pa_kernel(const char* __restrict__ q, const char* __restrict__ kc, const char* __restrict__ vc,
          const int* __restrict__ bt, const int* __restrict__ lens, char* __restrict__ out, char* __restrict__ ws) {
  __shared__ __attribute__((aligned(16384))) char lds[TAB + 512];
  int cpf;
  {
    uint64_t pc;
    asm volatile("s_getpc_b64 %1\n\tv_lshlrev_b32 %0, 4, %2\n\tv_min_u32 %0, " CPFS ", %0\n\tglobal_load_dword %0, %0, %1" : "=&v"(cpf), "=&s"(pc) : "v"(threadIdx.x));
  }
  const uint64_t t64 = __builtin_amdgcn_s_memrealtime();
  const int lane = threadIdx.x & 63;
  const int wave = __builtin_amdgcn_readfirstlane(threadIdx.x >> 6);
  const int b = blockIdx.x >> 3, h = (blockIdx.x - b) & 7;
  const int g = lane >> 4, c = lane & 15;
  const unsigned base = (unsigned)(uintptr_t)LDSP(char, lds);
  if (threadIdx.x - 704 < 48) *LDSP(u32x2, base + WORDS + 8 * lane) = u32x2{0, 0};

  const int ly = lane < 32 ? lane : (lane ^ 16) & 31;
  unsigned go[4];
#pragma unroll
  for (int u = 0; u < 4; ++u) go[u] = (4 * u + g) * 2048 + ((c ^ (4 * u + g)) << 4) - 1024 * u;
  const unsigned ring = base + wave * 8192;
  u32x2 first;
  int len, leny;
  u32x2 pg;
  asm volatile("s_waitcnt lgkmcnt(0)\n\ts_load_dwordx2 %3, %8, %9\n\tglobal_load_dword %0, %4, %7\n\tglobal_load_dword %1, %5, %7\n\tglobal_load_dwordx2 %2, %6, %8"
               : "=&v"(len), "=&v"(leny), "=&v"(pg), "=&s"(first)
               : "v"(4 * (lane & 31)), "v"(4 * ly), "v"(4 * b * NBPS + 8 * lane), "s"(lens), "s"(bt), "s"(4 * b * NBPS + 8 * wave),
                 "s"(q), "s"(kc), "s"(vc), "s"(out), "s"(ws), "s"(t64), "v"(go[0]), "v"(go[1]), "v"(go[2]), "v"(go[3]), "s"(ring), "s"(h) : "memory");
  const unsigned t0 = t64;
  u32x4 qf[4];
  if ((wave & 14) == 8)
    asm volatile("global_load_dwordx4 %0, %4, %5\n\tglobal_load_dwordx4 %1, %4, %5 offset:64\n\tglobal_load_dwordx4 %2, %4, %5 offset:128\n\tglobal_load_dwordx4 %3, %4, %5 offset:192\n\ts_setprio 3"
                 : "=&v"(qf[0]), "=&v"(qf[1]), "=&v"(qf[2]), "=&v"(qf[3]) : "v"((b * 8 + h) * 4096 + c * 256 + g * 16), "s"(q) : "memory");
  asm volatile("s_barrier\n\ts_waitcnt lgkmcnt(0)" : "+s"(first) :: "memory");
  if (wave < 4) {
    const u32x4 r0 = srd(kc + (size_t)(first[0] * 128 + h) * 256, 32768), r1 = srd(kc + (size_t)(first[1] * 128 + h) * 256, 32768);
    if (wave > 1) while ((int)(NOW() - t0) < EDEL) {}
    DMA4(KPOL, ring + EARLY, r0, go);
    DMA4(KPOL, ring + EARLY + 4096, r1, go);
  }
  asm volatile("s_waitcnt vmcnt(8)\n\ts_cmp_lt_u32 %8, 4\n\ts_cbranch_scc1 1f\n\ts_waitcnt vmcnt(0)\n1:"
               : "+v"(len), "+v"(leny), "+v"(pg), "+v"(cpf), "+v"(qf[0]), "+v"(qf[1]), "+v"(qf[2]), "+v"(qf[3]) : "s"(wave) : "scc", "memory");
  const unsigned unimask = bal32(len <= 0);
  if (len <= 0 || len > SKV) len = SKV;
  const int lenb = rl(len, b), qb = (lenb + 63) >> 6, pre = qb > 2 ? 6 : 4;
#pragma unroll
  for (int u = 0; u < 2; ++u) pg[u] = (pg[u] * 128 + h) | min(max(lenb - 32 * lane - 16 * u, 0), 16) << 27;
  if (wave >= 4 && wave < pre) {
    const unsigned p0 = rl(pg[0], 2 * qb - 6 + wave), p1 = rl(pg[1], 2 * qb - 6 + wave);
    const u32x4 r0 = srd(kc + (size_t)(p0 & 0x7ffffff) * 256, p0 >> 27 << 11), r1 = srd(kc + (size_t)(p1 & 0x7ffffff) * 256, p1 >> 27 << 11);
    DMA4(KPOL, ring + EARLY, r0, go);
    DMA4(KPOL, ring + EARLY + 4096, r1, go);
  }

  const bool gives = (h & 3) == 1;
  if (wave == 11) {
    __builtin_amdgcn_s_setprio(3);
    const unsigned k0 = min(pg[0] >> 13 & 0x3fff, 63u), k1 = min(pg[1] >> 13 & 0x3fff, 63u);
    const bool last = lane >> 1 == qb - 1;
    int p0 = 2 * lane + (lane < 4 ? 0 : last ? 12 - 4 * qb : 4), p1 = p0 + 1, lim = 2 * qb;
    if (gives) while (!(lim = __builtin_amdgcn_readfirstlane(lds_ld(base + WORDS + 80)))) __builtin_amdgcn_s_sleep(1);
    const bool mid = lane >= 6 && lane < lim - 2;
    if (mid) {
      p0 = __hip_atomic_fetch_add(LDSP(int, base + CNT + 4 * k0), 1, __ATOMIC_RELAXED, __HIP_MEMORY_SCOPE_WORKGROUP);
      p1 = __hip_atomic_fetch_add(LDSP(int, base + CNT + 4 * k1), 1, __ATOMIC_RELAXED, __HIP_MEMORY_SCOPE_WORKGROUP);
    }
    const int n = lds_ld(base + CNT + 4 * lane);
    int s = scan32(n);
    s += __builtin_amdgcn_update_dpp(0, s, 0x143, 0xc, 0xf, false) + 16 - n;
    const int s0 = __builtin_amdgcn_ds_bpermute(4 * k0, s), s1 = __builtin_amdgcn_ds_bpermute(4 * k1, s);
    if (lane < 4 || lane < lim - 2 || last) {
      *LDSP(unsigned, base + TAB + 4 * (mid ? p0 + s0 : p0)) = pg[0];
      *LDSP(unsigned, base + TAB + 4 * (mid ? p1 + s1 : p1)) = pg[1];
    }
    asm volatile("s_waitcnt lgkmcnt(0)" ::: "memory");
    lds_put(base + WORDS + 92, 1);
  }

  if (leny <= 0 || leny > SKV) leny = SKV;
  const int key = ((len + 15) >> 4) * 32 + (lane & 31), keyy = ((leny + 15) >> 4) * 32 + ly;
  unsigned less = (unsigned)(keyy - key) >> 31;
#define ROR(n) less = __builtin_amdgcn_alignbit(less, __builtin_amdgcn_update_dpp(0, keyy, 0x120 + n, 0xf, 0xf, true) - key, 31);
  ROR(1) ROR(2) ROR(3) ROR(4) ROR(5) ROR(6) ROR(7) ROR(8) ROR(9) ROR(10) ROR(11) ROR(12) ROR(13) ROR(14) ROR(15)
  int rank = __builtin_popcount(less);
  {
    auto t = __builtin_amdgcn_permlane32_swap(rank, rank, false, false);
    rank = t[0] + t[1];
  }
  const int tot = rl(scan32(key >> 5), 31), rb = rl(rank, b);
  const int rg = gives ? 31 - rb : 2 * rb + (h >> 1 & 1);
  const int ph = gives ? h - 1 + 2 * (rg & 1) : (h & 4) | 1;
  const int pb = __builtin_ctz(bal32(rank == (gives ? rg >> 1 : 31 - rg)) | 0x80000000u);
  const int npb = (lenb + 15) >> 4, npp = (rl(len, pb) + 15) >> 4;
  const int npg = gives ? npb : npp, nph = gives ? npp : npb;
  int nq = min(min((npg - ((tot * CAPS) >> 11) + QRND) >> 2, ((npg + 3) >> 2) - 3), min(((((tot * LMAX) >> 11) - nph) * 43) >> 8, min((128 - nph) >> 2, 16)));
  if (nq < 0 || rg > 31 || nph < 13 || (h & 3) == 3) nq = 0;
  nq = __builtin_amdgcn_readfirstlane(nq);
  const int xw = gives ? 0 : 2 * nq, nb = 2 * __builtin_amdgcn_readfirstlane(max(gives ? qb - nq : qb, 2));
  const int tend = ((tot - TSUB) * TAU16) >> 9;

  if (wave < 8) {
    lds_put(base + WORDS + 80, nb);
    __builtin_amdgcn_sched_barrier(0);
    const int ev = wave + 8 * (lane >> 1), f = ev - 8, eo = f < 0 ? ev : max(ev - 2 * xw, 8);
    const bool foreign = (unsigned)f < (unsigned)(2 * xw), isv = foreign ? f >= xw : eo >= nb;
    const int i = isv && !foreign ? eo - nb : eo, rest = qb > 2 ? lenb + 64 - 64 * qb : 64, nt = 32 * nb - 64 + rest;
    const int cost = max(32 * i - 64 + rest, min(32 * i, 128 + rest)) + (isv && !foreign ? nt : 0) + (32 + 2 * SCOST) * min(max(f, 0), 2 * xw);
    const float rc = __builtin_amdgcn_rcpf((float)(2 * nt + (64 + 4 * SCOST) * xw)) * (float)max(tend - (D0), 0);
    const unsigned tab = ((unsigned)((float)cost * rc) + (D0)) << 1 | isv;
    const unsigned ta = base + TAB + ((8 * (foreign ? nb + f - (isv ? xw : 0) : i) + 4 * (lane & 1)) & 511);
    const int kap = __builtin_amdgcn_readfirstlane((int)(rc * (32 * KAP)));
    int freed = pre;
    bool ask = false;
    int own = 0;
    asm volatile("" :: "v"(tab), "v"(ta), "s"(kap));
    const int l0 = (wave - (wave < nb ? 2 : nb)) & 63;
    const unsigned f0 = rl(pg[0], l0), f1 = rl(pg[1], l0);
    unsigned pt = lane & 1 ? f1 : f0;
    for (int e = wave;;) {
      unsigned ti;
      if (e >= pre) {
        const unsigned d = rl(tab, e >> 2), p0 = rl(pt, e >> 2 & 62), p1 = rl(pt, e >> 2 | 1);
        const int due = (int)d >> 1;
        const char* const from = d & 1 ? vc : kc;
        const u32x4 r0 = srd(from + (size_t)(p0 & 0x7ffffff) * 256, p0 >> 27 << 11), r1 = srd(from + (size_t)(p1 & 0x7ffffff) * 256, p1 >> 27 << 11);
        asm volatile("" :: "s"(r0), "s"(r1));
        while (freed + 7 < e) {
          const uint64_t x = __hip_atomic_load(LDSP(uint64_t, base + WORDS + 32), __ATOMIC_RELAXED, __HIP_MEMORY_SCOPE_WORKGROUP);
          const uint64_t y = __hip_atomic_load(LDSP(uint64_t, base + WORDS + 40), __ATOMIC_RELAXED, __HIP_MEMORY_SCOPE_WORKGROUP);
          freed = __builtin_amdgcn_readfirstlane(min(min((int)x, (int)(x >> 32)), min((int)y, (int)(y >> 32))));
          if (freed + 7 < e) __builtin_amdgcn_s_sleep(1);
        }
        int late, hold;
        do {
          ti = NOW();
          u32x4 w = *LDSP(const volatile u32x4, base + WORDS + 96);
          hold = __builtin_amdgcn_readfirstlane(w[0]) >> 16;
          const unsigned fs = __builtin_amdgcn_readfirstlane(w[3]);
          const int part = (due - (int)(fs & 0xffff)) * (int)(fs >> 16) >> 4;
          late = ti - t0 - due - __builtin_amdgcn_readfirstlane(w[1]) - (part * (__builtin_amdgcn_readfirstlane(w[2]) - (int)t0) >> 16);
        } while (late < hold);
        ti += (late * (gives ? BETA2 : BETA) >> 4) + (gives ? LREF2 : LREF);
        DMA4(KPOL, ring, r0, go);
        DMA4(KPOL, ring + 4096, r1, go);
      }
      int st;
      if (ask) asm volatile("global_load_dword %0, %1, %2 sc1" : "=v"(st) : "v"(STARTS + 256 * b + 4 * (lane & 7)), "s"(ws) : "memory");
      asm volatile("s_waitcnt vmcnt(0)" : "+v"(st) :: "memory");
      lds_put(base + WORDS + 4 * wave, e + 1);
      if (e >= pre) {
        const int ex = min(max((int)(NOW() - ti), -LNEG), LCAP) * kap;
        if (lane == 0) {
          __hip_atomic_fetch_add(LDSP(int, base + WORDS + 96), ex, __ATOMIC_RELAXED, __HIP_MEMORY_SCOPE_WORKGROUP);
          __hip_atomic_fetch_max(LDSP(int, base + WORDS + 96), 0, __ATOMIC_RELAXED, __HIP_MEMORY_SCOPE_WORKGROUP);
        }
      } else if (e < 4) {
        const int ex = min(max((int)(NOW() - t0) - (e < 2 ? NOM0 : NOM2), 0), 400) * GAIN >> 4;
        if (lane == 0) __hip_atomic_fetch_add(LDSP(int, base + WORDS + 100), ex, __ATOMIC_RELAXED, __HIP_MEMORY_SCOPE_WORKGROUP);
      }
      if (ask) {
        st -= t0;
        if (!__builtin_amdgcn_ballot_w64((unsigned)st + 1000u > 2000u)) {
          ask = false;
          st += __builtin_amdgcn_update_dpp(0, st, 0x111, 0xf, 0xf, false);
          st += __builtin_amdgcn_update_dpp(0, st, 0x112, 0xf, 0xf, false);
          st += __builtin_amdgcn_update_dpp(0, st, 0x114, 0xf, 0xf, false);
          const int now = NOW() - t0 - own;
          if (tend - now > 300) *LDSP(volatile u32x2, base + WORDS + 104) = u32x2{t0 + TOFF - own + (rl(st, 7) >> 3), (unsigned)now | (unsigned)(__builtin_amdgcn_rcpf((float)(tend - now)) * 1048576.f) << 16};
        }
      }
      if (e == 7) {
        own = __builtin_amdgcn_readfirstlane(lds_ld(base + WORDS + 100));
        if (lane == 0) __hip_atomic_store((gu32*)(ws + STARTS + 256 * b + 4 * h), t0 + own + SOFF, __ATOMIC_RELAXED, __HIP_MEMORY_SCOPE_AGENT);
        ask = true;
      }
      e += 8;
      if (e >= 2 * (nb + xw)) break;
      if (e < 16) {
        for (;;) {
          const uint64_t x = __hip_atomic_load(LDSP(uint64_t, base + WORDS + 88), __ATOMIC_RELAXED, __HIP_MEMORY_SCOPE_WORKGROUP);
          asm volatile("" ::: "memory");
          pt = *LDSP(const unsigned, ta);
          asm volatile("" : "+v"(pt) :: "memory");
          if (__builtin_amdgcn_readfirstlane(((unsigned)x | !xw) & (unsigned)(x >> 32))) break;
          __builtin_amdgcn_s_sleep(1);
        }
      }
    }
    return;
  }

  const int x = wave & 1;
  const bool other = wave >= 10;
  const unsigned used = base + WORDS + 4 * wave;
  char* const slot = ws + (unsigned)(gives ? b * 8 + h : pb * 8 + ph) * SLOT;
  unsigned tag = 0;
  lds_put(used, !other ? x : xw ? 8 + x : 1 << 20);
  if (other) {
    if (!nq || (gives && x)) return;
    if (!x && !gives) {
      if (lane < 2 * xw) *LDSP(unsigned, base + TAB + 8 * nb + 4 * lane) = (bt[pb * NBPS + 4 * (((npp + 3) >> 2) - 1 - nq) + lane] * 128 + ph) | 16u << 27;
      asm volatile("s_waitcnt lgkmcnt(0)" ::: "memory");
      lds_put(base + WORDS + 88, 1);
    }
    tag = __hip_atomic_load((gu32*)(slot + EPOCH_OFF), __ATOMIC_RELAXED, __HIP_MEMORY_SCOPE_AGENT) + TAGK;
    if (gives) {
      while ((int)(NOW() - t0) < FETCH_AT) __builtin_amdgcn_s_sleep(32);
#pragma nounroll
      for (int spin = 0; spin < (1 << 22); ++spin) {
        if (__builtin_amdgcn_readfirstlane(__hip_atomic_load((gu32*)(slot + FLAG_OFF), __ATOMIC_RELAXED, __HIP_MEMORY_SCOPE_AGENT) == tag)) break;
        __builtin_amdgcn_s_sleep(8);
      }
      asm volatile("" ::: "memory");
      const __amdgpu_buffer_rsrc_t pr = rsrc(slot, 4096 + 64);
      u32x4 y[4];
#pragma unroll
      for (int r = 0; r < 4; ++r) y[r] = __builtin_amdgcn_raw_buffer_load_b128(pr, lane * 16 + r * 1024, 0, 16);
      const unsigned lse = __builtin_amdgcn_raw_buffer_load_b32(pr, 4096 + 4 * c, 0, 16);
#pragma unroll
      for (int r = 0; r < 4; ++r) *LDSP(u32x4, base + FD + 1024 * r + 16 * lane) = y[r];
      *LDSP(unsigned, base + FD + 4096 + 4 * lane) = lse;
      asm volatile("s_waitcnt lgkmcnt(0)" ::: "memory");
      lds_put(base + WORDS + 56, 1);
      if (lane == 0) __hip_atomic_store((gu32*)(slot + EPOCH_OFF), tag, __ATOMIC_RELAXED, __HIP_MEMORY_SCOPE_AGENT);
      return;
    }
  }
  __builtin_amdgcn_s_setprio(3);
  if (other) {
#pragma unroll
    for (int u = 0; u < 4; ++u) qf[u] = *(const u32x4*)(q + (pb * 8 + ph) * 4096 + c * 256 + g * 16 + 64 * u);
  }
  if ((unimask >> (other ? pb : b)) & 1) {
#pragma unroll
    for (int u = 0; u < 4; ++u) qf[u] = u32x4{0, 0, 0, 0};
  }
  const float CS = 0.088388347648318440f * 1.4426950408889634f;
  const unsigned ka = base + c * 256 + ((g ^ c) << 4);
  const unsigned va = base + (lane >> 2) * 256 + ((((lane & 3) << 2) ^ (lane >> 2) ^ (2 * x)) << 4);
  float m = -1e30f, l = 0.f;
  asm volatile("" : "+v"(qf[0]), "+v"(qf[1]), "+v"(qf[2]), "+v"(qf[3]), "+v"(tag));
  int seen = 0;
  auto look = [&](int e) __attribute__((always_inline)) { seen = lds_ld(base + WORDS + 4 * (e & 7)); };
  auto landed = [&](int e) __attribute__((always_inline)) {
    while (__builtin_amdgcn_readfirstlane(seen) <= e) look(e);
    asm volatile("" ::: "memory");
  };
  if (other) while (__builtin_amdgcn_readfirstlane(min(lds_ld(base + WORDS + 32), lds_ld(base + WORDS + 36))) < 6) __builtin_amdgcn_s_sleep(1);
#define PIN() __builtin_amdgcn_sched_barrier(0)
  const int i0 = other ? nb : 0, i1 = other ? nb + xw : nb, ev = other ? 8 + xw : nb + 2 * xw;
  int e = (other ? 8 : 0) + x;
  for (int i = i0 + x; i < i1; i += 2) {
    landed(e);
    u32x4 kf[2][4];
#pragma unroll
    for (int u = 0; u < 4; ++u) {
      const unsigned a = ((e & 7) * 8192 + (e < 6 ? EARLY : 0) + ka) ^ (u << 6);
      kf[0][u] = *LDSP(const u32x4, a);
      kf[1][u] = *LDSP(const u32x4, a + 4096);
    }
    e = min(e + (i >> 1 == 3 ? 2 + 2 * xw : 2), ev);
    asm volatile("" ::: "memory");
    lds_put(used, e);
    PIN();
    f32x4 sc[2] = {{0.f, 0.f, 0.f, 0.f}, {0.f, 0.f, 0.f, 0.f}};
#pragma unroll
    for (int u = 0; u < 4; ++u)
#pragma unroll
      for (int t = 0; t < 2; ++t)
        sc[t] = __builtin_amdgcn_mfma_f32_16x16x32_bf16(__builtin_bit_cast(bf16x8, kf[t][u]), __builtin_bit_cast(bf16x8, qf[u]), sc[t], 0, 0, 0);
    if (i < 6) {
      const int rest = (i < 4 ? lenb : lenb + 192 - 64 * qb) - 32 * i;
#pragma unroll
      for (int r = 0; r < 8; ++r) sc[r >> 2][r & 3] = 16 * (r >> 2) + 4 * g + (r & 3) < rest ? sc[r >> 2][r & 3] : -__builtin_inff();
    }
    const float mx = xg_max(vmax(vmax(vmax(sc[0][0], sc[0][1]), vmax(sc[0][2], sc[0][3])), vmax(vmax(sc[1][0], sc[1][1]), vmax(sc[1][2], sc[1][3]))));
    const float mn = vmax(m, __builtin_ceilf(mx * CS));
    PIN();
    look(e);
    PIN();
    float pr[8];
#pragma unroll
    for (int r = 0; r < 8; ++r) pr[r] = __builtin_amdgcn_exp2f(__builtin_fmaf(sc[r >> 2][r & 3], CS, -mn));
    l *= __builtin_amdgcn_exp2f(m - mn);
    m = mn;
    u32x4 pw;
#pragma unroll
    for (int r = 0; r < 4; ++r) {
      const unsigned w = cvtpk(pr[2 * r], pr[2 * r + 1]);
      pw[r] = w;
      l = __builtin_amdgcn_fdot2_f32_bf16(__builtin_bit_cast(bf16x2, w), bf16x2{1, 1}, l, false);
    }
    *LDSP(u32x4, base + PB + 1024 * i + 16 * lane) = pw;
    *LDSP(float, base + MB + 64 * i + 4 * c) = mn;
  }
  l = xg_sum(l);
  *LDSP(f32x2, base + XL - 4096 + 512 * wave + 8 * lane) = f32x2{m, l};
  asm volatile("" ::: "memory");
  lds_put(used + 32, 1);
  unsigned pa = base + PB + 1024 * i0 + 16 * lane, ma = base + MB + 64 * i0 + 4 * c;
  f32x4 acc[4] = {{0.f, 0.f, 0.f, 0.f}, {0.f, 0.f, 0.f, 0.f}, {0.f, 0.f, 0.f, 0.f}, {0.f, 0.f, 0.f, 0.f}};
  u32x4 pw;
  float mi;
  {
    f32x2 o;
    int ok;
    do {
      ok = lds_ld((used ^ 4) + 32);
      asm volatile("" ::: "memory");
      o = *LDSP(const f32x2, base + XL - 4096 + 512 * (wave ^ 1) + 8 * lane);
      pw = *LDSP(const u32x4, pa);
      mi = *LDSP(const float, ma);
      asm volatile("" : "+v"(o), "+v"(pw), "+v"(mi) :: "memory");
    } while (!__builtin_amdgcn_readfirstlane(ok));
    const float mn = vmax(m, o[0]);
    l = l * __builtin_amdgcn_exp2f(m - mn) + o[1] * __builtin_amdgcn_exp2f(o[0] - mn);
    m = mn;
  }
  float rl_ = __builtin_amdgcn_rcpf(l);
  const int e1 = e + i1 - i0;
  for (int e2 = nq && gives ? e1 - MG : e1;;) {
    asm volatile("" : "+v"(rl_));
    landed(e);
    u32x4 vt[4];
#pragma unroll
    for (int k = 0; k < 4; ++k) {
      const unsigned a = (((e & 7) * 8192 + va) ^ ((k >> 1) << 4)) + 8 * (k & 1);
      const u32x2 lo = __builtin_bit_cast(u32x2, __builtin_amdgcn_ds_read_tr16_b64_v4i16(LDSP(s16x4, a)));
      const u32x2 hi = __builtin_bit_cast(u32x2, __builtin_amdgcn_ds_read_tr16_b64_v4i16(LDSP(s16x4, a + 4096)));
      vt[k] = u32x4{lo[0], lo[1], hi[0], hi[1]};
    }
    ++e;
    asm volatile("" ::: "memory");
    lds_put(used, e == e1 ? 1 << 20 : e);
    PIN();
    const float dn = (m - mi) * (128.f / 65535.f);
    const u16x2 sh = __builtin_amdgcn_cvt_pknorm_u16(dn, dn);
    u32x4 pf;
#pragma unroll
    for (int r = 0; r < 4; ++r) {
      const unsigned w = pw[r];
      pf[r] = __builtin_bit_cast(unsigned, __builtin_elementwise_sub_sat(__builtin_bit_cast(u16x2, w), sh));
    }
    PIN();
    look(e);
    pa += 1024; ma += 64;
    pw = *LDSP(const u32x4, pa);
    mi = *LDSP(const float, ma);
    PIN();
#pragma unroll
    for (int k = 0; k < 4; ++k) acc[k] = __builtin_amdgcn_mfma_f32_16x16x32_bf16(__builtin_bit_cast(bf16x8, vt[k]), __builtin_bit_cast(bf16x8, pf), acc[k], 0, 0, 0);
    if (e == e2) {
      if (e == e1) break;
      e2 = e1;
      while (!__builtin_amdgcn_readfirstlane(lds_ld(base + WORDS + 56))) {}
      asm volatile("" ::: "memory");
      const u32x4 y0 = *LDSP(const u32x4, base + FD + 2048 * x + 16 * lane), y1 = *LDSP(const u32x4, base + FD + 2048 * x + 1024 + 16 * lane);
      const float lf = *LDSP(const float, base + FD + 4096 + 4 * lane);
      const float mn = vmax(m, __builtin_ceilf(lf)), sa = __builtin_amdgcn_exp2f(m - mn), wt = __builtin_amdgcn_exp2f(lf - mn);
      m = mn;
      rl_ = __builtin_amdgcn_rcpf(l * sa + wt);
#pragma unroll
      for (int k = 0; k < 4; ++k) {
        const unsigned lo = (k >> 1 ? y1 : y0)[2 * (k & 1)], hi = (k >> 1 ? y1 : y0)[2 * (k & 1) + 1];
        acc[k] = acc[k] * sa + __builtin_bit_cast(f32x4, u32x4{lo << 16, lo & 0xffff0000u, hi << 16, hi & 0xffff0000u}) * wt;
      }
    }
  }
#pragma unroll
  for (int k = 0; k < 4; ++k) acc[k] *= rl_;
  u32x4 y[2];
#pragma unroll
  for (int r = 0; r < 2; ++r)
    y[r] = u32x4{cvtpk(acc[2 * r][0], acc[2 * r][1]), cvtpk(acc[2 * r][2], acc[2 * r][3]),
                 cvtpk(acc[2 * r + 1][0], acc[2 * r + 1][1]), cvtpk(acc[2 * r + 1][2], acc[2 * r + 1][3])};
  if (!other) {
    const __amdgpu_buffer_rsrc_t orr = rsrc(out + (b * 8 + h) * 4096, 4096);
#pragma unroll
    for (int r = 0; r < 2; ++r) __builtin_amdgcn_raw_buffer_store_b128(y[r], orr, c * 256 + g * 64 + x * 32 + r * 16, 0, OPOL);
  } else {
    const __amdgpu_buffer_rsrc_t orr = rsrc(slot, 4096 + 64);
#pragma unroll
    for (int r = 0; r < 2; ++r) __builtin_amdgcn_raw_buffer_store_b128(y[r], orr, lane * 16 + x * 2048 + r * 1024, 0, 16);
    if (!x) __builtin_amdgcn_raw_buffer_store_b32(__float_as_uint(m + __builtin_amdgcn_logf(l)), orr, lane * 4 + 4096, 0, 16);
    WAITVM0();
    if (lane == 0 && __hip_atomic_fetch_add(LDSP(int, base + WORDS + 60), 1, __ATOMIC_RELAXED, __HIP_MEMORY_SCOPE_WORKGROUP))
      __hip_atomic_store((gu32*)(slot + FLAG_OFF), tag, __ATOMIC_RELAXED, __HIP_MEMORY_SCOPE_AGENT);
    return;
  }
}

extern "C" void kernel_launch(void* const* d_in, const int* in_sizes, int n_in, void* d_out, int out_size,
                              void* d_ws, size_t ws_size, hipStream_t stream) {
  pa_kernel<<<256, 768, 0, stream>>>((const char*)d_in[0], (const char*)d_in[1], (const char*)d_in[2],
                                     (const int*)d_in[3], (const int*)d_in[4], (char*)d_out, (char*)d_ws);
}
```

```cpp
#define TS
#define STAT 0
#include <hip/hip_runtime.h>
#include <hip/hip_bf16.h>
#include <stdint.h>

#define AHEAD 2
#define NS 6
#define KPOL " nt"
#define VPOL ""
#define DELTA 4
#define TAU16 872
#define POFS 0

typedef __attribute__((ext_vector_type(8))) __bf16 bf16x8;
typedef __attribute__((ext_vector_type(4))) short s16x4;
typedef __attribute__((ext_vector_type(4))) float f32x4;
typedef __attribute__((ext_vector_type(4))) unsigned u32x4;
typedef __attribute__((ext_vector_type(2))) unsigned u32x2;
typedef __attribute__((address_space(1))) unsigned gu32;
#define LDSP(T, a) ((__attribute__((address_space(3))) T*)(uintptr_t)(a))

constexpr int NB = 32, NBPS = 128, SKV = NBPS * 16;
constexpr unsigned SLOT = 16384, EPOCH_OFF = 64 * 8 * SLOT;
constexpr int RS = 9216, PAIR_LDS = NS * RS + 256;

__device__ __forceinline__ unsigned cvtpk(float lo, float hi) {
  typedef __attribute__((ext_vector_type(2))) __bf16 bf16x2;
  bf16x2 r = {(__bf16)lo, (__bf16)hi};
  return __builtin_bit_cast(unsigned, r);
}
__device__ __forceinline__ float vmax(float a, float b) { return __builtin_elementwise_maximum(a, b); }
__device__ __forceinline__ float xg_max(float x) {
  auto s = __builtin_amdgcn_permlane16_swap(__float_as_uint(x), __float_as_uint(x), false, false);
  x = vmax(__uint_as_float(s[0]), __uint_as_float(s[1]));
  auto t = __builtin_amdgcn_permlane32_swap(__float_as_uint(x), __float_as_uint(x), false, false);
  return vmax(__uint_as_float(t[0]), __uint_as_float(t[1]));
}
__device__ __forceinline__ float xg_sum(float x) {
  auto s = __builtin_amdgcn_permlane16_swap(__float_as_uint(x), __float_as_uint(x), false, false);
  x = __uint_as_float(s[0]) + __uint_as_float(s[1]);
  auto t = __builtin_amdgcn_permlane32_swap(__float_as_uint(x), __float_as_uint(x), false, false);
  return __uint_as_float(t[0]) + __uint_as_float(t[1]);
}
__device__ __forceinline__ __amdgpu_buffer_rsrc_t rsrc(const void* p, unsigned n) {
  return __builtin_amdgcn_make_buffer_rsrc((void*)p, 0, n, 0x00020000);
}
__device__ __forceinline__ u32x4 srd(const void* p, unsigned n) {
  const uint64_t a = (uint64_t)p;
  return u32x4{(unsigned)a, (unsigned)(a >> 32) & 0xffffu, n, 0x00020000u};
}
__device__ __forceinline__ int rl(int v, int l) { return __builtin_amdgcn_readlane(v, l); }
__device__ __forceinline__ int lds_get(unsigned a) {
  return __builtin_amdgcn_readfirstlane(__hip_atomic_load(LDSP(int, a), __ATOMIC_RELAXED, __HIP_MEMORY_SCOPE_WORKGROUP));
}
__device__ __forceinline__ void lds_put(unsigned a, int v) {
  __hip_atomic_store(LDSP(int, a), v, __ATOMIC_RELAXED, __HIP_MEMORY_SCOPE_WORKGROUP);
}

#define DMA4(POL, dst, rs, so, v0, v1, v2, v3)                                                        \
  do {                                                                                                \
    unsigned keep_;                                                                                   \
    asm volatile("s_mov_b32 %0, m0\n\ts_mov_b32 m0, %1\n\ts_nop 4\n\t"                                \
                 "buffer_load_dwordx4 %4, %2, %3 offen" POL " lds\n\t"                                \
                 "buffer_load_dwordx4 %5, %2, %3 offen offset:1024" POL " lds\n\t"                    \
                 "buffer_load_dwordx4 %6, %2, %3 offen offset:2048" POL " lds\n\t"                    \
                 "buffer_load_dwordx4 %7, %2, %3 offen offset:3072" POL " lds\n\t"                    \
                 "s_mov_b32 m0, %0"                                                                   \
                 : "=&s"(keep_) : "s"(dst), "s"(rs), "s"(so), "v"(v0), "v"(v1), "v"(v2), "v"(v3) : "memory"); \
  } while (0)
#define DMA1(POL, dst, rs, so, v0)                                                                    \
  do {                                                                                                \
    unsigned keep_;                                                                                   \
    asm volatile("s_mov_b32 %0, m0\n\ts_mov_b32 m0, %1\n\ts_nop 4\n\t"                                \
                 "buffer_load_dwordx4 %4, %2, %3 offen" POL " lds\n\t"                                \
                 "s_mov_b32 m0, %0"                                                                   \
                 : "=&s"(keep_) : "s"(dst), "s"(rs), "s"(so), "v"(v0) : "memory");                    \
  } while (0)
#define WAITVM(n) asm volatile("s_waitcnt vmcnt(%0)" :: "n"(n) : "memory")
#define NOW() ((unsigned)__builtin_amdgcn_s_memrealtime())

__global__ void __launch_bounds__(512)
pa_kernel(const char* __restrict__ q_, const char* __restrict__ kc_, const char* __restrict__ vc_,
          const int* __restrict__ bt_, const int* __restrict__ lens_, char* __restrict__ out_, char* __restrict__ ws_) {
  __shared__ __attribute__((aligned(256))) char lds[2 * PAIR_LDS];
  typedef __attribute__((address_space(4))) const uint64_t* kap_t;
  kap_t kargs = (kap_t)__builtin_amdgcn_kernarg_segment_ptr();
  uint64_t t0l; asm volatile("s_memrealtime %0\n\ts_waitcnt lgkmcnt(0)" : "=s"(t0l), "+s"(kargs) :: "memory");
  const unsigned t0 = (unsigned)t0l;
  typedef __attribute__((address_space(1))) char* gp_t;
  const char* q = (const char*)(gp_t)kargs[0]; const char* kc = (const char*)(gp_t)kargs[1]; const char* vc = (const char*)(gp_t)kargs[2];
  const int* bt = (const int*)(gp_t)kargs[3]; const int* lens = (const int*)(gp_t)kargs[4]; char* out = (char*)(gp_t)kargs[5]; char* ws = (char*)(gp_t)kargs[6];
  const int lane = threadIdx.x & 63;
  const int wave = __builtin_amdgcn_readfirstlane(threadIdx.x >> 6);
  const int p = blockIdx.x >> 2;
  const int h = 2 * ((blockIdx.x - (p >> 1)) & 3) + (wave & 1);
  const int g = lane >> 4, c = lane & 15;
  const bool loader = wave < 2;

  int len = lens[lane & 31];
  gu32* const ep = (gu32*)(ws + EPOCH_OFF) + h * 64 + ((p + lane) & 63);
  unsigned tagv;
  if (loader) tagv = bt[(wave * 64 + lane) * 32];
  else tagv = __hip_atomic_load(ep, __ATOMIC_RELAXED, __HIP_MEMORY_SCOPE_AGENT);
  const unsigned ring = (unsigned)(uintptr_t)LDSP(char, lds) + (wave & 1) * PAIR_LDS;
  const unsigned fullw = ring + NS * RS, freew = fullw + 128;
  lds_put(loader ? fullw : freew, 0);
  asm volatile("s_waitcnt lgkmcnt(0)\n\ts_barrier" ::: "memory");

  const bool uni = len <= 0;
  if (uni || len > SKV) len = SKV;
  const int np = (len + 15) >> 4;
  const int dl = np >= 16 ? DELTA : 0;
  const int vl = np - dl;
  int incl = vl;
  incl += __builtin_amdgcn_update_dpp(0, incl, 0x111, 0xf, 0xf, false);
  incl += __builtin_amdgcn_update_dpp(0, incl, 0x112, 0xf, 0xf, false);
  incl += __builtin_amdgcn_update_dpp(0, incl, 0x114, 0xf, 0xf, false);
  incl += __builtin_amdgcn_update_dpp(0, incl, 0x118, 0xf, 0xf, false);
  incl += __builtin_amdgcn_update_dpp(0, incl, 0x142, 0xa, 0xf, false);
  const int V = rl(incl, 31), VV = max(V, 64);
  const int lo = (VV * p) >> 6, hi = min(V, (VV * (p + 1)) >> 6);
  if (lo >= hi) return;
  const int b0 = __builtin_popcount((unsigned)__builtin_amdgcn_ballot_w64(incl <= lo));
  const int b1 = __builtin_popcount((unsigned)__builtin_amdgcn_ballot_w64(incl < hi));
  const int ex = incl - vl;
  const int r0v = lo > ex ? lo - ex + dl : 0, cntv = min(hi, incl) - ex + dl - r0v;
  const int nv = rl(r0v, b1) == 0 ? __builtin_popcountll(__builtin_amdgcn_ballot_w64(((VV * lane) >> 6) < rl(incl, b1))) - 1 - p : 0;
  const unsigned loff = lane * 16;
  auto load_partial = [&](unsigned dst, int j) __attribute__((always_inline)) {
    const u32x4 pr = srd(ws + (unsigned)((p + j) * 8 + h) * SLOT, SLOT);
    DMA4(" sc1", dst, pr, 0, loff, loff, loff, loff);
    DMA4(" sc1", dst + 4096, pr, 4096, loff, loff, loff, loff);
    DMA1(" sc1", dst + 8192, pr, 8192, loff);
  };
  unsigned* const tsp = (unsigned*)(ws + (64u << 20)) + (blockIdx.x * 2 + (wave & 1)) * 16;

  if (loader) {
    int n = 0;
    unsigned tab[2] = {0u, 0u};
    int idx[2] = {0, 0};
    for (int s = b0; s <= b1; ++s) {
      const int r0 = rl(r0v, s), cnt = rl(cntv, s), ls = rl(len, s);
#pragma unroll
      for (int u = 0; u < 2; ++u) {
        const int k = lane + 64 * u - n, pg = r0 + k;
        idx[u] = k >= 0 ? s * NBPS + pg : idx[u];
        tab[u] = k >= 0 ? (unsigned)(min(16, ls - 16 * pg) - 1) << 17 : tab[u];
      }
      n += cnt;
    }
    tab[0] |= (unsigned)bt[min(idx[0], NB * NBPS - 1)];
    tab[1] |= (unsigned)bt[min(idx[1], NB * NBPS - 1)];
    unsigned go[4];
#pragma unroll
    for (int r = 0; r < 4; ++r) go[r] = (4 * r + g) * 2048 + ((c ^ (4 * r + g)) << 4) - 1024 * r;
    asm volatile("" : "+v"(tab[0]), "+v"(tab[1]) : "v"(tagv));
    const int ntot = n + nv;
    unsigned dst = ring;
    int freed = 0;
    if (lane == 0) { tsp[14] = NOW(); tsp[4] = n; }
    for (int j = 0; j < ntot; ++j) {
      while (freed + NS <= j) { freed = lds_get(freew); if (freed + NS <= j) __builtin_amdgcn_s_sleep(1); }
      if (TAU16) {
        const int due = ((j * TAU16) >> 4) + POFS;
        while ((int)(NOW() - t0) < due) __builtin_amdgcn_s_sleep(1);
      }
      if (j < n) {
        const unsigned e = rl(j < 64 ? tab[0] : tab[1], j & 63);
        const size_t po = (size_t)(e & 0x1ffffu) * 32768 + h * 256;
        const unsigned nrec = ((e >> 17) & 15) * 2048 + 256;
        const u32x4 kr = srd(kc + po, nrec), vr = srd(vc + po, nrec);
        DMA4(KPOL, dst, kr, 0, go[0], go[1], go[2], go[3]);
        DMA4(VPOL, dst + 4096, vr, 0, go[0], go[1], go[2], go[3]);
      } else {
        load_partial(dst, j - n + 1);
      }
      dst = dst + RS == ring + NS * RS ? ring : dst + RS;
      if (j >= AHEAD) { WAITVM(8 * AHEAD); lds_put(fullw, j - AHEAD + 1); }
    }
    if (lane == 0) tsp[15] = NOW();
    if (AHEAD > 3) { WAITVM(24); lds_put(fullw, ntot - 3); }
    if (AHEAD > 2) { WAITVM(16); lds_put(fullw, ntot - 2); }
    if (AHEAD > 1) { WAITVM(8); lds_put(fullw, ntot - 1); }
    WAITVM(0); lds_put(fullw, ntot);
    return;
  }

  const unsigned unimask = (unsigned)__builtin_amdgcn_ballot_w64(uni);
  const unsigned qoff = c * 256 + g * 16;
  const __amdgpu_buffer_rsrc_t qr = rsrc(q + h * 4096, NB * 32768);
  u32x4 qn[4];
#pragma unroll
  for (int s = 0; s < 4; ++s) qn[s] = __builtin_amdgcn_raw_buffer_load_b128(qr, qoff + 64 * s, b0 * 32768, 0);
  tagv += 0x9E3779B9u;
  const float CS = 0.088388347648318440f * 1.4426950408889634f;
  const unsigned ka = c * 256 + ((g ^ c) << 4);
  const unsigned va = 4096 + (lane >> 2) * 256 + ((((lane & 3) << 2) ^ (lane >> 2)) << 4);
  unsigned cur = ring;
  int i = 0, full = 0;
  unsigned ts_first = 0, ts_real = 0, ts_spins = 0, cy_comp = 0;
  for (int b = b0; b <= b1; ++b) {
    const int r0 = rl(r0v, b), cnt = rl(cntv, b);
    const int iend = i + cnt;
    const int lastt = min(15, rl(len, b) - 16 * (r0 + cnt - 1) - 1);
    const float cs = (unimask >> b) & 1 ? 0.f : CS;
    u32x4 qf[4];
#pragma unroll
    for (int s = 0; s < 4; ++s) qf[s] = qn[s];
    asm volatile("" : "+v"(qf[0]), "+v"(qf[1]), "+v"(qf[2]), "+v"(qf[3]));
    if (b < b1) {
#pragma unroll
      for (int s = 0; s < 4; ++s) qn[s] = __builtin_amdgcn_raw_buffer_load_b128(qr, qoff + 64 * s, (b + 1) * 32768, 0);
    }
    f32x4 acc[8];
#pragma unroll
    for (int k = 0; k < 8; ++k) acc[k] = f32x4{0.f, 0.f, 0.f, 0.f};
    float m = -1e30f, l = 0.f;
    do {
      while (full <= i) { full = lds_get(fullw); if (full <= i) __builtin_amdgcn_s_sleep(1); }
      asm volatile("" ::: "memory");
      if (i == 0) ts_first = NOW();
      ts_real = NOW();
      const unsigned c3 = (unsigned)__builtin_amdgcn_s_memtime();
      u32x4 kf[4];
      u32x2 vt[8];
#pragma unroll
      for (int s = 0; s < 4; ++s) kf[s] = *LDSP(const u32x4, (cur + ka) ^ (s << 6));
#pragma unroll
      for (int k = 0; k < 8; ++k)
        vt[k] = __builtin_bit_cast(u32x2, __builtin_amdgcn_ds_read_tr16_b64_v4i16(LDSP(s16x4, ((cur + va) ^ ((k >> 1) << 4)) + 8 * (k & 1))));
      f32x4 sc = {0.f, 0.f, 0.f, 0.f};
#pragma unroll
      for (int s = 0; s < 4; ++s)
        sc = __builtin_amdgcn_mfma_f32_16x16x32_bf16(__builtin_bit_cast(bf16x8, kf[s]), __builtin_bit_cast(bf16x8, qf[s]), sc, 0, 0, 0);
      ++i;
      asm volatile("" : "+v"(vt[0]), "+v"(vt[1]), "+v"(vt[2]), "+v"(vt[3]), "+v"(vt[4]), "+v"(vt[5]), "+v"(vt[6]), "+v"(vt[7]) :: "memory");
      lds_put(freew, i);
      cur = cur + RS == ring + NS * RS ? ring : cur + RS;
      if (i == iend && lastt < 15) {
        u32x2 vm = {(4 * g <= lastt ? 0xffffu : 0u) | (4 * g + 1 <= lastt ? 0xffff0000u : 0u),
                    (4 * g + 2 <= lastt ? 0xffffu : 0u) | (4 * g + 3 <= lastt ? 0xffff0000u : 0u)};
        asm volatile("" : "+v"(vm));
#pragma unroll
        for (int k = 0; k < 8; ++k) vt[k] &= vm;
#pragma unroll
        for (int r = 0; r < 4; ++r) sc[r] = 4 * g + r <= lastt ? sc[r] : -__builtin_inff();
      }
      const float mx = xg_max(vmax(vmax(sc[0], sc[1]), vmax(sc[2], sc[3])));
      const float mn = vmax(m, mx * cs);
      const float alpha = __builtin_amdgcn_exp2f(m - mn);
      m = mn;
      float pr[4];
#pragma unroll
      for (int r = 0; r < 4; ++r) pr[r] = __builtin_amdgcn_exp2f(__builtin_fmaf(sc[r], cs, -mn));
      l = l * alpha + ((pr[0] + pr[1]) + (pr[2] + pr[3]));
      const u32x2 pf = {cvtpk(pr[0], pr[1]), cvtpk(pr[2], pr[3])};
#pragma unroll
      for (int k = 0; k < 8; ++k) {
        acc[k] *= alpha;
        acc[k] = __builtin_amdgcn_mfma_f32_16x16x16bf16_1k(__builtin_bit_cast(s16x4, vt[k]), __builtin_bit_cast(s16x4, pf), acc[k], 0, 0, 0);
      }
      asm volatile("" : "+v"(acc[7])); cy_comp += (unsigned)__builtin_amdgcn_s_memtime() - c3;
    } while (i < iend);
    if (b == b1) {
      for (int j = 1; j <= nv; ++j) {
        while (full <= i) { full = lds_get(fullw); if (full <= i) __builtin_amdgcn_s_sleep(1); }
        asm volatile("" ::: "memory");
        const unsigned tag = rl(tagv, j);
        u32x4 x[8];
        u32x2 y;
        for (int spin = 0;; ++spin) {
#pragma unroll
          for (int k = 0; k < 8; ++k) x[k] = *LDSP(const u32x4, cur + 1024 * k + loff);
          y = *LDSP(const u32x2, cur + 8192 + 8 * c);
          unsigned bad = y[1] ^ tag;
#pragma unroll
          for (int k = 0; k < 8; ++k) bad |= (x[k][1] ^ tag) | (x[k][3] ^ tag);
          if (__builtin_amdgcn_ballot_w64(bad != 0) == 0 || spin > (1 << 22)) break;
          __builtin_amdgcn_s_sleep(2);
          ++ts_spins;
          load_partial(cur, j);
          WAITVM(0);
        }
        ++i;
        asm volatile("" ::: "memory");
        lds_put(freew, i);
        cur = cur + RS == ring + NS * RS ? ring : cur + RS;
        const float lse = __uint_as_float(y[0]);
        const float mn = vmax(m, lse);
        const float alpha = __builtin_amdgcn_exp2f(m - mn), w = __builtin_amdgcn_exp2f(lse - mn);
        m = mn;
        l = l * alpha + (g == 0 ? w : 0.f);
#pragma unroll
        for (int k = 0; k < 8; ++k) {
          const f32x4 o = {__uint_as_float(x[k][0] << 16), __uint_as_float(x[k][0] & 0xffff0000u),
                           __uint_as_float(x[k][2] << 16), __uint_as_float(x[k][2] & 0xffff0000u)};
          acc[k] = acc[k] * alpha + o * w;
        }
      }
    }
    const float lt = xg_sum(l);
    const float inv = __builtin_amdgcn_rcpf(lt);
    u32x2 ob[8];
#pragma unroll
    for (int k = 0; k < 8; ++k) ob[k] = u32x2{cvtpk(acc[k][0] * inv, acc[k][1] * inv), cvtpk(acc[k][2] * inv, acc[k][3] * inv)};
    if (r0 > 0) {
      const unsigned tag = rl(tagv, 0);
      const __amdgpu_buffer_rsrc_t pr = rsrc(ws + (unsigned)(p * 8 + h) * SLOT, SLOT);
#pragma unroll
      for (int k = 0; k < 8; ++k)
        __builtin_amdgcn_raw_buffer_store_b128(u32x4{ob[k][0], tag, ob[k][1], tag}, pr, loff + 1024 * (k & 3), (k >> 2) * 4096, 16);
      const u32x2 y = {__float_as_uint(m + __builtin_amdgcn_logf(lt)), tag};
      __builtin_amdgcn_raw_buffer_store_b64(y, pr, g == 0 ? c * 8 : 0x80000000u, 8192, 16);
    } else {
      const __amdgpu_buffer_rsrc_t orr = rsrc(out + (b * 8 + h) * 4096, 4096);
#pragma unroll
      for (int r = 0; r < 4; ++r)
        __builtin_amdgcn_raw_buffer_store_b128(u32x4{ob[2 * r][0], ob[2 * r][1], ob[2 * r + 1][0], ob[2 * r + 1][1]}, orr, c * 256 + g * 64 + 16 * r, 0, 0);
    }
  }
  if (lane == 0) { tsp[0] = t0; tsp[1] = ts_first; tsp[2] = ts_real; tsp[3] = NOW();
    tsp[5] = nv; tsp[6] = ts_spins; tsp[7] = 0; tsp[8] = 0; tsp[9] = 0; tsp[10] = cy_comp; tsp[11] = h; tsp[12] = b1 - b0; tsp[13] = __builtin_amdgcn_s_getreg((31 << 11) | 20); }
  if (lane >= 1 && lane <= nv) __hip_atomic_store(ep, tagv, __ATOMIC_RELAXED, __HIP_MEMORY_SCOPE_AGENT);
}

__global__ void tiny_kernel(unsigned* t) {
  uint64_t t0l; asm volatile("s_memrealtime %0\n\ts_waitcnt lgkmcnt(0)" : "=s"(t0l) :: "memory");
  if (threadIdx.x == 0 && blockIdx.x == 0) t[512 * 16] = (unsigned)t0l;
}
__global__ void diag_kernel(const unsigned* __restrict__ t) {
  __shared__ float sm[512];
  __shared__ unsigned lo_s;
  const int w = threadIdx.x;
  const unsigned* x = t + w * 16;
  if (w == 0) { unsigned lo = t[0]; for (int i = 0; i < 512; ++i) if ((int)(t[i * 16] - lo) < 0) lo = t[i * 16]; lo_s = lo; }
  __syncthreads();
  const unsigned lo = lo_s;
  float v = 0.f;
  if (STAT == 1 || STAT == 11) v = x[3] - lo;
  if (STAT == 2 || STAT == 12) v = x[1] - lo;
  if (STAT == 3 || STAT == 13) v = x[2] - lo;
  if (STAT == 4 || STAT == 14) v = x[14] - lo;
  if (STAT == 5 || STAT == 15) v = x[15] - lo;
  if (STAT == 6 || STAT == 16) v = x[0] - lo;
  if (STAT == 7 || STAT == 17) v = x[3] - x[2];
  if (STAT == 8 || STAT == 18) v = x[7] - lo;
  if (STAT == 9 || STAT == 19) v = x[8] - lo;
  sm[w] = v;
  __syncthreads();
  if (w == 0) {
    float s = 0.f, m = 0.f;
    for (int i = 0; i < 512; ++i) { s += sm[i]; m = fmaxf(m, sm[i]); }
    float r = STAT > 10 ? m : s / 512;
    r = fminf(fmaxf(r, 0.f), 10000.f);
    if (STAT == 20) r = lo - t[512 * 16];
    const int due = 15000 + (int)(r * 5);
    while ((int)((unsigned)__builtin_amdgcn_s_memrealtime() - t[512 * 16]) < due) __builtin_amdgcn_s_sleep(1);
  }
}

extern "C" void kernel_launch(void* const* d_in, const int* in_sizes, int n_in, void* d_out, int out_size,
                              void* d_ws, size_t ws_size, hipStream_t stream) {
  tiny_kernel<<<1, 64, 0, stream>>>((unsigned*)((char*)d_ws + (64u << 20)));
  pa_kernel<<<256, 256, 0, stream>>>((const char*)d_in[0], (const char*)d_in[1], (const char*)d_in[2],
                                     (const int*)d_in[3], (const int*)d_in[4], (char*)d_out, (char*)d_ws);
  diag_kernel<<<1, 512, 0, stream>>>((const unsigned*)((char*)d_ws + (64u << 20)));
}
```

```cpp
#include <hip/hip_runtime.h>
#include <hip/hip_bf16.h>
#include <stdint.h>

constexpr int CAPS = 67, LMAX = 84, QRND = 1;
constexpr int SCOST = 8, MG = 5;
constexpr int TAU16 = 451, TSUB = 38, D0 = 10;
constexpr int EDEL = 90;
constexpr int NOM0 = 185, NOM2 = 243, GAIN = 4;
constexpr int LREF = 88, LREF2 = 110;
constexpr int KAP = 300, LNEG = 40, LCAP = 100, BETA = 6;
constexpr int T0 = 420, WAIT0 = 200, WAITX = 110;
constexpr int R0 = 5, RS = 3, NR = 3;
constexpr int G2 = 24, DZ = 15, RHO = 16, LC2 = 127, NMIN = 20, PSTR = 256;
constexpr int OPOL = 2;
#define CPFS "0x2500"
#define KPOL " nt"

typedef __attribute__((ext_vector_type(8))) __bf16 bf16x8;
typedef __attribute__((ext_vector_type(4))) short s16x4;
typedef __attribute__((ext_vector_type(4))) float f32x4;
typedef __attribute__((ext_vector_type(4))) unsigned u32x4;
typedef __attribute__((ext_vector_type(2))) unsigned u32x2;
typedef __attribute__((address_space(1))) unsigned gu32;
#define LDSP(T, a) ((__attribute__((address_space(3))) T*)(uintptr_t)(a))

constexpr int NBPS = 128, SKV = NBPS * 16;
constexpr unsigned PB = 8 * 8192, MB = PB + 64 * 1024, FD = MB + 64 * 64, XL = FD + 4096 + 256;
constexpr unsigned WORDS = XL + 4 * 512, CNT = WORDS + 128, TAB = CNT + 256;
constexpr unsigned EARLY = PB + 16 * 1024;
constexpr unsigned SLOT = 8192, FLAG_OFF = 4096 + 64, EPOCH_OFF = 4096 + 68;
constexpr unsigned STARTS = 256 * SLOT;
constexpr unsigned TAGK = 0x9E3779B9u;
typedef __attribute__((ext_vector_type(2))) unsigned short u16x2;
typedef __attribute__((ext_vector_type(2))) float f32x2;
typedef __attribute__((ext_vector_type(2))) __bf16 bf16x2;

__device__ __forceinline__ unsigned cvtpk(float lo, float hi) {
  bf16x2 r = {(__bf16)lo, (__bf16)hi};
  return __builtin_bit_cast(unsigned, r);
}
__device__ __forceinline__ float vmax(float a, float b) { return __builtin_elementwise_maximum(a, b); }
__device__ __forceinline__ float xg_max(float x) {
  auto s = __builtin_amdgcn_permlane16_swap(__float_as_uint(x), __float_as_uint(x), false, false);
  x = vmax(__uint_as_float(s[0]), __uint_as_float(s[1]));
  auto t = __builtin_amdgcn_permlane32_swap(__float_as_uint(x), __float_as_uint(x), false, false);
  return vmax(__uint_as_float(t[0]), __uint_as_float(t[1]));
}
__device__ __forceinline__ float xg_sum(float x) {
  auto s = __builtin_amdgcn_permlane16_swap(__float_as_uint(x), __float_as_uint(x), false, false);
  x = __uint_as_float(s[0]) + __uint_as_float(s[1]);
  auto t = __builtin_amdgcn_permlane32_swap(__float_as_uint(x), __float_as_uint(x), false, false);
  return __uint_as_float(t[0]) + __uint_as_float(t[1]);
}
__device__ __forceinline__ int scan32(int v) {
  v += __builtin_amdgcn_update_dpp(0, v, 0x111, 0xf, 0xf, false);
  v += __builtin_amdgcn_update_dpp(0, v, 0x112, 0xf, 0xf, false);
  v += __builtin_amdgcn_update_dpp(0, v, 0x114, 0xf, 0xf, false);
  v += __builtin_amdgcn_update_dpp(0, v, 0x118, 0xf, 0xf, false);
  v += __builtin_amdgcn_update_dpp(0, v, 0x142, 0xa, 0xf, false);
  return v;
}
__device__ __forceinline__ __amdgpu_buffer_rsrc_t rsrc(const void* p, unsigned n) {
  return __builtin_amdgcn_make_buffer_rsrc((void*)p, 0, n, 0x00020000);
}
__device__ __forceinline__ u32x4 srd(const void* p, unsigned n) {
  const uint64_t a = (uint64_t)p;
  return u32x4{(unsigned)a, (unsigned)(a >> 32) & 0xffffu, n, 0x00020000u};
}
__device__ __forceinline__ int rl(int v, int l) { return __builtin_amdgcn_readlane(v, l); }
__device__ __forceinline__ unsigned bal32(bool c) { return (unsigned)__builtin_amdgcn_ballot_w64(c); }
__device__ __forceinline__ int lds_ld(unsigned a) {
  return __hip_atomic_load(LDSP(int, a), __ATOMIC_RELAXED, __HIP_MEMORY_SCOPE_WORKGROUP);
}
__device__ __forceinline__ void lds_put(unsigned a, int v) {
  __hip_atomic_store(LDSP(int, a), v, __ATOMIC_RELAXED, __HIP_MEMORY_SCOPE_WORKGROUP);
}

#define DMA4(POL, dst, rs, v)                                                                         \
  do {                                                                                                \
    unsigned keep_;                                                                                   \
    asm volatile("s_mov_b32 %0, m0\n\ts_mov_b32 m0, %1\n\ts_nop 4\n\t"                                \
                 "buffer_load_dwordx4 %3, %2, 0 offen" POL " lds\n\t"                                 \
                 "buffer_load_dwordx4 %4, %2, 0 offen offset:1024" POL " lds\n\t"                     \
                 "buffer_load_dwordx4 %5, %2, 0 offen offset:2048" POL " lds\n\t"                     \
                 "buffer_load_dwordx4 %6, %2, 0 offen offset:3072" POL " lds\n\t"                     \
                 "s_mov_b32 m0, %0"                                                                   \
                 : "=&s"(keep_) : "s"(dst), "s"(rs), "v"(v[0]), "v"(v[1]), "v"(v[2]), "v"(v[3]) : "memory"); \
  } while (0)
#define WAITVM0() asm volatile("s_waitcnt vmcnt(0)" ::: "memory")
#define NOW() ((unsigned)__builtin_amdgcn_s_memrealtime())

__global__ void __launch_bounds__(768)
pa_kernel(const char* __restrict__ q, const char* __restrict__ kc, const char* __restrict__ vc,
          const int* __restrict__ bt, const int* __restrict__ lens, char* __restrict__ out, char* __restrict__ ws) {
  __shared__ __attribute__((aligned(16384))) char lds[TAB + 512];
  int cpf;
  {
    uint64_t pc;
    asm volatile("s_getpc_b64 %1\n\tv_lshlrev_b32 %0, 4, %2\n\tv_min_u32 %0, " CPFS ", %0\n\tglobal_load_dword %0, %0, %1" : "=&v"(cpf), "=&s"(pc) : "v"(threadIdx.x));
  }
  const uint64_t t64 = __builtin_amdgcn_s_memrealtime();
  const int lane = threadIdx.x & 63;
  const int wave = __builtin_amdgcn_readfirstlane(threadIdx.x >> 6);
  const int b = blockIdx.x >> 3, h = (blockIdx.x - b) & 7;
  const int g = lane >> 4, c = lane & 15;
  const unsigned base = (unsigned)(uintptr_t)LDSP(char, lds);
  if (threadIdx.x - 704 < 48) *LDSP(u32x2, base + WORDS + 8 * lane) = u32x2{0, 0};

  const int ly = lane < 32 ? lane : (lane ^ 16) & 31;
  unsigned go[4];
#pragma unroll
  for (int u = 0; u < 4; ++u) go[u] = (4 * u + g) * 2048 + ((c ^ (4 * u + g)) << 4) - 1024 * u;
  const unsigned ring = base + wave * 8192;
  u32x2 first;
  int len, leny;
  u32x2 pg;
  asm volatile("s_waitcnt lgkmcnt(0)\n\ts_load_dwordx2 %3, %8, %9\n\tglobal_load_dword %0, %4, %7\n\tglobal_load_dword %1, %5, %7\n\tglobal_load_dwordx2 %2, %6, %8"
               : "=&v"(len), "=&v"(leny), "=&v"(pg), "=&s"(first)
               : "v"(4 * (lane & 31)), "v"(4 * ly), "v"(4 * b * NBPS + 8 * lane), "s"(lens), "s"(bt), "s"(4 * b * NBPS + 8 * wave),
                 "s"(q), "s"(kc), "s"(vc), "s"(out), "s"(ws), "s"(t64), "v"(go[0]), "v"(go[1]), "v"(go[2]), "v"(go[3]), "s"(ring), "s"(h) : "memory");
  const unsigned t0 = t64;
  u32x4 qf[4];
  if ((wave & 14) == 8)
    asm volatile("global_load_dwordx4 %0, %4, %5\n\tglobal_load_dwordx4 %1, %4, %5 offset:64\n\tglobal_load_dwordx4 %2, %4, %5 offset:128\n\tglobal_load_dwordx4 %3, %4, %5 offset:192\n\ts_setprio 3"
                 : "=&v"(qf[0]), "=&v"(qf[1]), "=&v"(qf[2]), "=&v"(qf[3]) : "v"((b * 8 + h) * 4096 + c * 256 + g * 16), "s"(q) : "memory");
  asm volatile("s_barrier\n\ts_waitcnt lgkmcnt(0)" : "+s"(first) :: "memory");
  if (wave < 4) {
    const u32x4 r0 = srd(kc + (size_t)(first[0] * 128 + h) * 256, 32768), r1 = srd(kc + (size_t)(first[1] * 128 + h) * 256, 32768);
    if (wave > 1) while ((int)(NOW() - t0) < EDEL) {}
    DMA4(KPOL, ring + EARLY, r0, go);
    DMA4(KPOL, ring + EARLY + 4096, r1, go);
  }
  asm volatile("s_waitcnt vmcnt(8)\n\ts_cmp_lt_u32 %8, 4\n\ts_cbranch_scc1 1f\n\ts_waitcnt vmcnt(0)\n1:"
               : "+v"(len), "+v"(leny), "+v"(pg), "+v"(cpf), "+v"(qf[0]), "+v"(qf[1]), "+v"(qf[2]), "+v"(qf[3]) : "s"(wave) : "scc", "memory");
  const unsigned unimask = bal32(len <= 0);
  if (len <= 0 || len > SKV) len = SKV;
  const int lenb = rl(len, b), qb = (lenb + 63) >> 6, pre = qb > 2 ? 6 : 4;
#pragma unroll
  for (int u = 0; u < 2; ++u) pg[u] = (pg[u] * 128 + h) | min(max(lenb - 32 * lane - 16 * u, 0), 16) << 27;
  if (wave >= 4 && wave < pre) {
    const unsigned p0 = rl(pg[0], 2 * qb - 6 + wave), p1 = rl(pg[1], 2 * qb - 6 + wave);
    const u32x4 r0 = srd(kc + (size_t)(p0 & 0x7ffffff) * 256, p0 >> 27 << 11), r1 = srd(kc + (size_t)(p1 & 0x7ffffff) * 256, p1 >> 27 << 11);
    DMA4(KPOL, ring + EARLY, r0, go);
    DMA4(KPOL, ring + EARLY + 4096, r1, go);
  }

  const bool gives = (h & 3) == 1;
  if (wave == 11) {
    __builtin_amdgcn_s_setprio(3);
    const unsigned k0 = min(pg[0] >> 13 & 0x3fff, 63u), k1 = min(pg[1] >> 13 & 0x3fff, 63u);
    const bool last = lane >> 1 == qb - 1;
    int p0 = 2 * lane + (lane < 4 ? 0 : last ? 12 - 4 * qb : 4), p1 = p0 + 1, lim = 2 * qb;
    if (gives) while (!(lim = __builtin_amdgcn_readfirstlane(lds_ld(base + WORDS + 80)))) __builtin_amdgcn_s_sleep(1);
    const bool mid = lane >= 6 && lane < lim - 2;
    if (mid) {
      p0 = __hip_atomic_fetch_add(LDSP(int, base + CNT + 4 * k0), 1, __ATOMIC_RELAXED, __HIP_MEMORY_SCOPE_WORKGROUP);
      p1 = __hip_atomic_fetch_add(LDSP(int, base + CNT + 4 * k1), 1, __ATOMIC_RELAXED, __HIP_MEMORY_SCOPE_WORKGROUP);
    }
    const int n = lds_ld(base + CNT + 4 * lane);
    int s = scan32(n);
    s += __builtin_amdgcn_update_dpp(0, s, 0x143, 0xc, 0xf, false) + 16 - n;
    const int s0 = __builtin_amdgcn_ds_bpermute(4 * k0, s), s1 = __builtin_amdgcn_ds_bpermute(4 * k1, s);
    if (lane < 4 || lane < lim - 2 || last) {
      *LDSP(unsigned, base + TAB + 4 * (mid ? p0 + s0 : p0)) = pg[0];
      *LDSP(unsigned, base + TAB + 4 * (mid ? p1 + s1 : p1)) = pg[1];
    }
    asm volatile("s_waitcnt lgkmcnt(0)" ::: "memory");
    lds_put(base + WORDS + 92, 1);
  }

  if (leny <= 0 || leny > SKV) leny = SKV;
  const int key = ((len + 15) >> 4) * 32 + (lane & 31), keyy = ((leny + 15) >> 4) * 32 + ly;
  unsigned less = (unsigned)(keyy - key) >> 31;
#define ROR(n) less = __builtin_amdgcn_alignbit(less, __builtin_amdgcn_update_dpp(0, keyy, 0x120 + n, 0xf, 0xf, true) - key, 31);
  ROR(1) ROR(2) ROR(3) ROR(4) ROR(5) ROR(6) ROR(7) ROR(8) ROR(9) ROR(10) ROR(11) ROR(12) ROR(13) ROR(14) ROR(15)
  int rank = __builtin_popcount(less);
  {
    auto t = __builtin_amdgcn_permlane32_swap(rank, rank, false, false);
    rank = t[0] + t[1];
  }
  const int tot = rl(scan32(key >> 5), 31), rb = rl(rank, b);
  const int rg = gives ? 31 - rb : 2 * rb + (h >> 1 & 1);
  const int ph = gives ? h - 1 + 2 * (rg & 1) : (h & 4) | 1;
  const int pb = __builtin_ctz(bal32(rank == (gives ? rg >> 1 : 31 - rg)) | 0x80000000u);
  const int npb = (lenb + 15) >> 4, npp = (rl(len, pb) + 15) >> 4;
  const int npg = gives ? npb : npp, nph = gives ? npp : npb;
  int nq = min(min((npg - ((tot * CAPS) >> 11) + QRND) >> 2, ((npg + 3) >> 2) - 3), min(((((tot * LMAX) >> 11) - nph) * 43) >> 8, min((128 - nph) >> 2, 16)));
  if (nq < 0 || rg > 31 || nph < 13 || (h & 3) == 3) nq = 0;
  nq = __builtin_amdgcn_readfirstlane(nq);
  const int xw = gives ? 0 : 2 * nq, nb = 2 * __builtin_amdgcn_readfirstlane(max(gives ? qb - nq : qb, 2));
  const int tend = ((tot - TSUB) * TAU16) >> 9;

  if (wave < 8) {
    lds_put(base + WORDS + 80, nb);
    __builtin_amdgcn_sched_barrier(0);
    const int ev = wave + 8 * (lane >> 1), f = ev - 8, eo = f < 0 ? ev : max(ev - 2 * xw, 8);
    const bool foreign = (unsigned)f < (unsigned)(2 * xw), isv = foreign ? f >= xw : eo >= nb;
    const int i = isv && !foreign ? eo - nb : eo, rest = qb > 2 ? lenb + 64 - 64 * qb : 64, nt = 32 * nb - 64 + rest;
    const int cost = max(32 * i - 64 + rest, min(32 * i, 128 + rest)) + (isv && !foreign ? nt : 0) + (32 + 2 * SCOST) * min(max(f, 0), 2 * xw);
    const float rc = __builtin_amdgcn_rcpf((float)(2 * nt + (64 + 4 * SCOST) * xw - 32)) * (float)max(tend - D0 - TAU16 / 16, 0);
    const unsigned tab = ((unsigned)((float)cost * rc) + D0) << 1 | isv;
    const unsigned ta = base + TAB + ((8 * (foreign ? nb + f - (isv ? xw : 0) : i) + 4 * (lane & 1)) & 511);
    const int kap = __builtin_amdgcn_readfirstlane((int)(rc * (32 * KAP)));
    int freed = pre;
    asm volatile("" :: "v"(tab), "v"(ta), "s"(kap));
    const int l0 = (wave - (wave < nb ? 2 : nb)) & 63;
    const unsigned f0 = rl(pg[0], l0), f1 = rl(pg[1], l0);
    unsigned pt = lane & 1 ? f1 : f0;
    for (int e = wave;;) {
      unsigned ti;
      if (e >= pre) {
        const unsigned d = rl(tab, e >> 2), p0 = rl(pt, e >> 2 & 62), p1 = rl(pt, e >> 2 | 1);
        const int due = (int)d >> 1;
        const char* const from = d & 1 ? vc : kc;
        const u32x4 r0 = srd(from + (size_t)(p0 & 0x7ffffff) * 256, p0 >> 27 << 11), r1 = srd(from + (size_t)(p1 & 0x7ffffff) * 256, p1 >> 27 << 11);
        asm volatile("" :: "s"(r0), "s"(r1));
        while (freed + 7 < e) {
          const uint64_t x = __hip_atomic_load(LDSP(uint64_t, base + WORDS + 32), __ATOMIC_RELAXED, __HIP_MEMORY_SCOPE_WORKGROUP);
          const uint64_t y = __hip_atomic_load(LDSP(uint64_t, base + WORDS + 40), __ATOMIC_RELAXED, __HIP_MEMORY_SCOPE_WORKGROUP);
          freed = __builtin_amdgcn_readfirstlane(min(min((int)x, (int)(x >> 32)), min((int)y, (int)(y >> 32))));
          if (freed + 7 < e) __builtin_amdgcn_s_sleep(1);
        }
        int late, hold;
        do {
          ti = NOW();
          u32x4 w = *LDSP(const volatile u32x4, base + WORDS + 96);
          hold = __builtin_amdgcn_readfirstlane(w[0]) >> 16;
          const unsigned fs = __builtin_amdgcn_readfirstlane(w[3]);
          const int part = (due - (int)(fs & 0xffff)) * (int)(fs >> 16) >> 4;
          late = ti - t0 - due - __builtin_amdgcn_readfirstlane(w[1]) - (part * (__builtin_amdgcn_readfirstlane(w[2]) - (int)t0) >> 16);
        } while (late < hold);
        ti += (late * BETA >> 4) + (gives ? LREF2 : LREF);
        DMA4(KPOL, ring, r0, go);
        DMA4(KPOL, ring + 4096, r1, go);
        lds_put(base + WORDS + 112, late);
      }
      WAITVM0();
      lds_put(base + WORDS + 4 * wave, e + 1);
      if (e >= pre) {
        const int ex = min(max((int)(NOW() - ti), -LNEG), LCAP) * kap;
        if (lane == 0) {
          __hip_atomic_fetch_add(LDSP(int, base + WORDS + 96), ex, __ATOMIC_RELAXED, __HIP_MEMORY_SCOPE_WORKGROUP);
          __hip_atomic_fetch_max(LDSP(int, base + WORDS + 96), 0, __ATOMIC_RELAXED, __HIP_MEMORY_SCOPE_WORKGROUP);
        }
      } else if (e < 4) {
        const int ex = min(max((int)(NOW() - t0) - (e < 2 ? NOM0 : NOM2), 0), 400) * GAIN >> 4;
        if (lane == 0) __hip_atomic_fetch_add(LDSP(int, base + WORDS + 100), ex, __ATOMIC_RELAXED, __HIP_MEMORY_SCOPE_WORKGROUP);
      }
      e += 8;
      if (e >= 2 * (nb + xw)) break;
      if (e < 16) {
        for (;;) {
          const uint64_t x = __hip_atomic_load(LDSP(uint64_t, base + WORDS + 88), __ATOMIC_RELAXED, __HIP_MEMORY_SCOPE_WORKGROUP);
          asm volatile("" ::: "memory");
          pt = *LDSP(const unsigned, ta);
          asm volatile("" : "+v"(pt) :: "memory");
          if (__builtin_amdgcn_readfirstlane(((unsigned)x | !xw) & (unsigned)(x >> 32))) break;
          __builtin_amdgcn_s_sleep(1);
        }
      }
    }
    return;
  }

  auto review = [&]() __attribute__((always_inline)) {
    __builtin_amdgcn_s_setprio(0);
    unsigned from = t0 + T0;
    if (tend > 1200)
#pragma nounroll
      for (int k = 0; k <= NR; ++k) {
        while ((int)(NOW() - from) < 0) __builtin_amdgcn_s_sleep(4);
        const u32x4 w = *LDSP(const volatile u32x4, base + WORDS + 96);
        const int late = lds_ld(base + WORDS + 112);
        unsigned mine = __builtin_amdgcn_readfirstlane((w[3] ? w[2] : t0) + w[1]);
        char* const line = ws + STARTS + 4096 * k;
        if (lane == b && h == (((2 * b + (b >> 3) + k) & 7) | (rb < 16))) __hip_atomic_store((gu32*)(line + (unsigned)(PSTR * (lane >> 3 & 3) + 4 * (lane & 7))), k ? (mine & ~511u) | min(late, LC2) : mine, __ATOMIC_RELAXED, __HIP_MEMORY_SCOPE_AGENT);
        from += k ? WAITX : WAIT0;
        while ((int)(NOW() - from) < 0) __builtin_amdgcn_s_sleep(4);
        int st;
        asm volatile("global_load_dword %0, %1, %2 sc1\n\ts_waitcnt vmcnt(0)" : "=v"(st) : "v"(PSTR * (lane >> 3 & 3) + 4 * (lane & 7)), "s"(line) : "memory");
        const bool ok = st - mine + 600u < 1200u;
        st = k ? st & 511 : st - mine;
        const int n = __builtin_popcount(bal32(ok));
        int d = (int)((float)rl(scan32(ok ? st : 0), 31) * __builtin_amdgcn_rcpf((float)max(n, 1)));
        if (!k) {
          const int now = max((int)(NOW() - mine), 0);
          if (tend - now > 300) *LDSP(volatile u32x2, base + WORDS + 104) = u32x2{t0 + d, (unsigned)now | (unsigned)(__builtin_amdgcn_rcpf((float)(tend - now)) * 1048576.f) << 16};
        } else {
          d = n < NMIN ? 0 : max((d * G2 >> 4) - DZ, 0);
          const float r = w[3] ? (float)d * (RHO / 16.f / RS) : 0.f;
          const int tilt = r * 16.f * (float)(tend - (int)(w[3] & 0xffff)) * __builtin_amdgcn_rcpf((float)tend), all = d + (int)(r * (16.f - 1.3f - R0 + RS - RS * k));
          asm volatile("s_mov_b64 exec, 1\n\tds_add_u32 %0, %4 offset:8\n\tds_add_u32 %0, %5 offset:4\n\tds_sub_u32 %0, %2\n\tds_max_i32 %0, %3\n\tds_sub_u32 %0, %1 offset:16\n\tds_max_i32 %0, %3 offset:16\n\ts_mov_b64 exec, -1"
                       :: "v"(base + WORDS + 96), "v"(d), "v"(d << 16), "v"(0), "v"(tilt), "v"(all - tilt) : "memory");
          d = all;
        }
        from = mine + __builtin_amdgcn_readfirstlane(d) + (tend * (R0 + RS * k) >> 4);
      }
  };
  const int x = wave & 1;
  const bool other = wave >= 10;
  const unsigned used = base + WORDS + 4 * wave;
  char* const slot = ws + (unsigned)(gives ? b * 8 + h : pb * 8 + ph) * SLOT;
  unsigned tag = 0;
  lds_put(used, !other ? x : xw ? 8 + x : 1 << 20);
  if (!other || (nq && !(gives && x))) {
  if (other) {
    if (!x && !gives) {
      if (lane < 2 * xw) *LDSP(unsigned, base + TAB + 8 * nb + 4 * lane) = (bt[pb * NBPS + 4 * (((npp + 3) >> 2) - 1 - nq) + lane] * 128 + ph) | 16u << 27;
      asm volatile("s_waitcnt lgkmcnt(0)" ::: "memory");
      lds_put(base + WORDS + 88, 1);
    }
    tag = __hip_atomic_load((gu32*)(slot + EPOCH_OFF), __ATOMIC_RELAXED, __HIP_MEMORY_SCOPE_AGENT) + TAGK;
    if (gives) {
      while ((int)(NOW() - t0) < tend - 500 - 16 * rg) __builtin_amdgcn_s_sleep(32);
#pragma nounroll
      for (int spin = 0; spin < (1 << 22); ++spin) {
        if (__builtin_amdgcn_readfirstlane(__hip_atomic_load((gu32*)(slot + FLAG_OFF), __ATOMIC_RELAXED, __HIP_MEMORY_SCOPE_AGENT) == tag)) break;
        __builtin_amdgcn_s_sleep(8);
      }
      asm volatile("" ::: "memory");
      const __amdgpu_buffer_rsrc_t pr = rsrc(slot, 4096 + 64);
      u32x4 y[4];
#pragma unroll
      for (int r = 0; r < 4; ++r) y[r] = __builtin_amdgcn_raw_buffer_load_b128(pr, lane * 16 + r * 1024, 0, 16);
      const unsigned lse = __builtin_amdgcn_raw_buffer_load_b32(pr, 4096 + 4 * c, 0, 16);
#pragma unroll
      for (int r = 0; r < 4; ++r) *LDSP(u32x4, base + FD + 1024 * r + 16 * lane) = y[r];
      *LDSP(unsigned, base + FD + 4096 + 4 * lane) = lse;
      asm volatile("s_waitcnt lgkmcnt(0)" ::: "memory");
      lds_put(base + WORDS + 56, 1);
      if (lane == 0) __hip_atomic_store((gu32*)(slot + EPOCH_OFF), tag, __ATOMIC_RELAXED, __HIP_MEMORY_SCOPE_AGENT);
      return;
    }
  }
  __builtin_amdgcn_s_setprio(3);
  if (other) {
#pragma unroll
    for (int u = 0; u < 4; ++u) qf[u] = *(const u32x4*)(q + (pb * 8 + ph) * 4096 + c * 256 + g * 16 + 64 * u);
  }
  if ((unimask >> (other ? pb : b)) & 1) {
#pragma unroll
    for (int u = 0; u < 4; ++u) qf[u] = u32x4{0, 0, 0, 0};
  }
  const float CS = 0.088388347648318440f * 1.4426950408889634f;
  const unsigned ka = base + c * 256 + ((g ^ c) << 4);
  const unsigned va = base + (lane >> 2) * 256 + ((((lane & 3) << 2) ^ (lane >> 2) ^ (2 * x)) << 4);
  float m = -1e30f, l = 0.f;
  asm volatile("" : "+v"(qf[0]), "+v"(qf[1]), "+v"(qf[2]), "+v"(qf[3]), "+v"(tag));
  int seen = 0;
  auto look = [&](int e) __attribute__((always_inline)) { seen = lds_ld(base + WORDS + 4 * (e & 7)); };
  auto landed = [&](int e) __attribute__((always_inline)) {
    while (__builtin_amdgcn_readfirstlane(seen) <= e) look(e);
    asm volatile("" ::: "memory");
  };
  if (other) while (__builtin_amdgcn_readfirstlane(min(lds_ld(base + WORDS + 32), lds_ld(base + WORDS + 36))) < 6) __builtin_amdgcn_s_sleep(1);
#define PIN() __builtin_amdgcn_sched_barrier(0)
  const int i0 = other ? nb : 0, i1 = other ? nb + xw : nb, ev = other ? 8 + xw : nb + 2 * xw;
  int e = (other ? 8 : 0) + x;
  for (int i = i0 + x; i < i1; i += 2) {
    landed(e);
    u32x4 kf[2][4];
#pragma unroll
    for (int u = 0; u < 4; ++u) {
      const unsigned a = ((e & 7) * 8192 + (e < 6 ? EARLY : 0) + ka) ^ (u << 6);
      kf[0][u] = *LDSP(const u32x4, a);
      kf[1][u] = *LDSP(const u32x4, a + 4096);
    }
    e = min(e + (i >> 1 == 3 ? 2 + 2 * xw : 2), ev);
    asm volatile("" ::: "memory");
    lds_put(used, e);
    PIN();
    f32x4 sc[2] = {{0.f, 0.f, 0.f, 0.f}, {0.f, 0.f, 0.f, 0.f}};
#pragma unroll
    for (int u = 0; u < 4; ++u)
#pragma unroll
      for (int t = 0; t < 2; ++t)
        sc[t] = __builtin_amdgcn_mfma_f32_16x16x32_bf16(__builtin_bit_cast(bf16x8, kf[t][u]), __builtin_bit_cast(bf16x8, qf[u]), sc[t], 0, 0, 0);
    if (i < 6) {
      const int rest = (i < 4 ? lenb : lenb + 192 - 64 * qb) - 32 * i;
#pragma unroll
      for (int r = 0; r < 8; ++r) sc[r >> 2][r & 3] = 16 * (r >> 2) + 4 * g + (r & 3) < rest ? sc[r >> 2][r & 3] : -__builtin_inff();
    }
    const float mx = xg_max(vmax(vmax(vmax(sc[0][0], sc[0][1]), vmax(sc[0][2], sc[0][3])), vmax(vmax(sc[1][0], sc[1][1]), vmax(sc[1][2], sc[1][3]))));
    const float mn = vmax(m, __builtin_ceilf(mx * CS));
    PIN();
    look(e);
    PIN();
    float pr[8];
#pragma unroll
    for (int r = 0; r < 8; ++r) pr[r] = __builtin_amdgcn_exp2f(__builtin_fmaf(sc[r >> 2][r & 3], CS, -mn));
    l *= __builtin_amdgcn_exp2f(m - mn);
    m = mn;
    u32x4 pw;
#pragma unroll
    for (int r = 0; r < 4; ++r) {
      const unsigned w = cvtpk(pr[2 * r], pr[2 * r + 1]);
      pw[r] = w;
      l = __builtin_amdgcn_fdot2_f32_bf16(__builtin_bit_cast(bf16x2, w), bf16x2{1, 1}, l, false);
    }
    *LDSP(u32x4, base + PB + 1024 * i + 16 * lane) = pw;
    *LDSP(float, base + MB + 64 * i + 4 * c) = mn;
  }
  l = xg_sum(l);
  *LDSP(f32x2, base + XL - 4096 + 512 * wave + 8 * lane) = f32x2{m, l};
  asm volatile("" ::: "memory");
  lds_put(used + 32, 1);
  unsigned pa = base + PB + 1024 * i0 + 16 * lane, ma = base + MB + 64 * i0 + 4 * c;
  f32x4 acc[4] = {{0.f, 0.f, 0.f, 0.f}, {0.f, 0.f, 0.f, 0.f}, {0.f, 0.f, 0.f, 0.f}, {0.f, 0.f, 0.f, 0.f}};
  u32x4 pw;
  float mi;
  {
    f32x2 o;
    int ok;
    do {
      ok = lds_ld((used ^ 4) + 32);
      asm volatile("" ::: "memory");
      o = *LDSP(const f32x2, base + XL - 4096 + 512 * (wave ^ 1) + 8 * lane);
      pw = *LDSP(const u32x4, pa);
      mi = *LDSP(const float, ma);
      asm volatile("" : "+v"(o), "+v"(pw), "+v"(mi) :: "memory");
    } while (!__builtin_amdgcn_readfirstlane(ok));
    const float mn = vmax(m, o[0]);
    l = l * __builtin_amdgcn_exp2f(m - mn) + o[1] * __builtin_amdgcn_exp2f(o[0] - mn);
    m = mn;
  }
  float rl_ = __builtin_amdgcn_rcpf(l);
  const int e1 = e + i1 - i0;
  for (int e2 = nq && gives ? e1 - MG : e1;;) {
    asm volatile("" : "+v"(rl_));
    landed(e);
    u32x4 vt[4];
#pragma unroll
    for (int k = 0; k < 4; ++k) {
      const unsigned a = (((e & 7) * 8192 + va) ^ ((k >> 1) << 4)) + 8 * (k & 1);
      const u32x2 lo = __builtin_bit_cast(u32x2, __builtin_amdgcn_ds_read_tr16_b64_v4i16(LDSP(s16x4, a)));
      const u32x2 hi = __builtin_bit_cast(u32x2, __builtin_amdgcn_ds_read_tr16_b64_v4i16(LDSP(s16x4, a + 4096)));
      vt[k] = u32x4{lo[0], lo[1], hi[0], hi[1]};
    }
    ++e;
    asm volatile("" ::: "memory");
    lds_put(used, e == e1 ? 1 << 20 : e);
    PIN();
    const float dn = (m - mi) * (128.f / 65535.f);
    const u16x2 sh = __builtin_amdgcn_cvt_pknorm_u16(dn, dn);
    u32x4 pf;
#pragma unroll
    for (int r = 0; r < 4; ++r) {
      const unsigned w = pw[r];
      pf[r] = __builtin_bit_cast(unsigned, __builtin_elementwise_sub_sat(__builtin_bit_cast(u16x2, w), sh));
    }
    PIN();
    look(e);
    pa += 1024; ma += 64;
    pw = *LDSP(const u32x4, pa);
    mi = *LDSP(const float, ma);
    PIN();
#pragma unroll
    for (int k = 0; k < 4; ++k) acc[k] = __builtin_amdgcn_mfma_f32_16x16x32_bf16(__builtin_bit_cast(bf16x8, vt[k]), __builtin_bit_cast(bf16x8, pf), acc[k], 0, 0, 0);
    if (e == e2) {
      if (e == e1) break;
      e2 = e1;
      while (!__builtin_amdgcn_readfirstlane(lds_ld(base + WORDS + 56))) {}
      asm volatile("" ::: "memory");
      const u32x4 y0 = *LDSP(const u32x4, base + FD + 2048 * x + 16 * lane), y1 = *LDSP(const u32x4, base + FD + 2048 * x + 1024 + 16 * lane);
      const float lf = *LDSP(const float, base + FD + 4096 + 4 * lane);
      const float mn = vmax(m, __builtin_ceilf(lf)), sa = __builtin_amdgcn_exp2f(m - mn), wt = __builtin_amdgcn_exp2f(lf - mn);
      m = mn;
      rl_ = __builtin_amdgcn_rcpf(l * sa + wt);
#pragma unroll
      for (int k = 0; k < 4; ++k) {
        const unsigned lo = (k >> 1 ? y1 : y0)[2 * (k & 1)], hi = (k >> 1 ? y1 : y0)[2 * (k & 1) + 1];
        acc[k] = acc[k] * sa + __builtin_bit_cast(f32x4, u32x4{lo << 16, lo & 0xffff0000u, hi << 16, hi & 0xffff0000u}) * wt;
      }
    }
  }
#pragma unroll
  for (int k = 0; k < 4; ++k) acc[k] *= rl_;
  u32x4 y[2];
#pragma unroll
  for (int r = 0; r < 2; ++r)
    y[r] = u32x4{cvtpk(acc[2 * r][0], acc[2 * r][1]), cvtpk(acc[2 * r][2], acc[2 * r][3]),
                 cvtpk(acc[2 * r + 1][0], acc[2 * r + 1][1]), cvtpk(acc[2 * r + 1][2], acc[2 * r + 1][3])};
  if (!other) {
    const __amdgpu_buffer_rsrc_t orr = rsrc(out + (b * 8 + h) * 4096, 4096);
#pragma unroll
    for (int r = 0; r < 2; ++r) __builtin_amdgcn_raw_buffer_store_b128(y[r], orr, c * 256 + g * 64 + x * 32 + r * 16, 0, OPOL);
  } else {
    const __amdgpu_buffer_rsrc_t orr = rsrc(slot, 4096 + 64);
#pragma unroll
    for (int r = 0; r < 2; ++r) __builtin_amdgcn_raw_buffer_store_b128(y[r], orr, lane * 16 + x * 2048 + r * 1024, 0, 16);
    if (!x) __builtin_amdgcn_raw_buffer_store_b32(__float_as_uint(m + __builtin_amdgcn_logf(l)), orr, lane * 4 + 4096, 0, 16);
    WAITVM0();
    if (lane == 0 && __hip_atomic_fetch_add(LDSP(int, base + WORDS + 60), 1, __ATOMIC_RELAXED, __HIP_MEMORY_SCOPE_WORKGROUP))
      __hip_atomic_store((gu32*)(slot + FLAG_OFF), tag, __ATOMIC_RELAXED, __HIP_MEMORY_SCOPE_AGENT);
  }
  }
  if (wave == 11) review();
}

extern "C" void kernel_launch(void* const* d_in, const int* in_sizes, int n_in, void* d_out, int out_size,
                              void* d_ws, size_t ws_size, hipStream_t stream) {
  pa_kernel<<<256, 768, 0, stream>>>((const char*)d_in[0], (const char*)d_in[1], (const char*)d_in[2],
                                     (const int*)d_in[3], (const int*)d_in[4], (char*)d_out, (char*)d_ws);
}
```
